```python
import torch

def _softmax(x, axis=-1):
    return torch.softmax(x, dim=axis)
def _gelu(x):
    return torch.nn.functional.gelu(x, approximate="tanh")
def _silu(x):
    return torch.nn.functional.silu(x)
def _sigmoid(x):
    return torch.sigmoid(x)
def _layernorm(x, axis=-1, eps=1e-5):
    m = x.mean(dim=axis, keepdim=True)
    v = x.var(dim=axis, keepdim=True, unbiased=False)
    return (x - m) / torch.sqrt(v + eps)
def _scatter_add(tgt, idx, src, axis=0):
    return tgt.clone().index_add_(axis, idx, src)
def _segment_sum(src, seg, n):
    out = torch.zeros((n,) + src.shape[1:], dtype=src.dtype)
    return out.index_add_(0, seg, src)
def _pad(x, pw):
    flat = tuple(v for pair in reversed(pw) for v in pair)
    return torch.nn.functional.pad(x, flat)

kernel_name = 'standalone__weight_only_int_quip__prompt'

def setup_inputs():

    _w = (lambda a: (lambda t: t.to(torch.bfloat16) if t.is_floating_point() else t)(torch.as_tensor(a)))
    B, S, D, F = 1, 2048, 1024, 3584
    G = 32 if 32 > 0 else D
    D = (D // G) * G
    ng = D // G
    bits = 8 if 8 > 0 else 4
    qmax = 1 << bits
    qw = torch.randint(0, qmax, (D, F)).to(torch.int32)
    U, _ = (lambda *_a, **_k: torch.linalg.qr(*[torch.as_tensor(_x).float() for _x in _a], **_k))((lambda *s: torch.randn(*s, dtype=torch.bfloat16) * 0.05)(D, D))
    V, _ = (lambda *_a, **_k: torch.linalg.qr(*[torch.as_tensor(_x).float() for _x in _a], **_k))((lambda *s: torch.randn(*s, dtype=torch.bfloat16) * 0.05)(F, F))
    return {
        "x": _w((lambda *s: torch.randn(*s, dtype=torch.bfloat16) * 0.05)(B, S, D)),
        "qw": _w(qw),
        "scales": _w(torch.abs((lambda *s: torch.randn(*s, dtype=torch.bfloat16) * 0.05)(ng, F)) * 0.02 + 0.005),
        "zeros": _w(torch.full((ng, F), qmax / 2.0, dtype=float)),
        "U": _w(U.to(float)),
        "V": _w(V.to(float)),
    }

def reference(x, qw, scales, zeros, U, V):
    B, S, D = x.shape
    ng, F = scales.shape
    G = D // ng
    s = (lambda x, n, axis: torch.repeat_interleave(x, n, dim=axis))(scales, G, axis=0)
    z = (lambda x, n, axis: torch.repeat_interleave(x, n, dim=axis))(zeros, G, axis=0)
    wq = ((lambda x, dt: x.to(dt))(qw, x.dtype) - z) * s
    xr = torch.einsum("bsd,de->bse", x, U)
    h  = torch.einsum("bse,ef->bsf", xr, wq)
    y  = torch.einsum("bsf,fg->bsg", h, V)
    return y
```

```cpp
#include <hip/hip_runtime.h>
#include <hip/hip_bf16.h>
#include <stdint.h>

typedef short bf16x8 __attribute__((ext_vector_type(8)));
typedef float f32x4 __attribute__((ext_vector_type(4)));
typedef unsigned u32x4 __attribute__((ext_vector_type(4)));
typedef unsigned u32x2 __attribute__((ext_vector_type(2)));
typedef int i32x4 __attribute__((ext_vector_type(4)));

constexpr int S_ = 2048, D_ = 1024, F_ = 3584, G_ = 32;

__device__ __forceinline__ float bf2f(unsigned short u) { return __uint_as_float(((unsigned)u) << 16); }
__device__ __forceinline__ unsigned cvt_pk_bf16(float lo, float hi) {
  unsigned r; asm("v_cvt_pk_bf16_f32 %0, %1, %2" : "=v"(r) : "v"(lo), "v"(hi)); return r;
}

__global__ void k_dequant(const int* __restrict__ qw, const unsigned short* __restrict__ sc, const unsigned short* __restrict__ zr,
                          unsigned short* __restrict__ wq) {
  int idx = blockIdx.x * blockDim.x + threadIdx.x;
  int d = idx / (F_ / 8), f = (idx % (F_ / 8)) * 8;
  const i32x4* p = (const i32x4*)(qw + (size_t)d * F_ + f);
  i32x4 q0 = p[0], q1 = p[1];
  u32x4 s = *(const u32x4*)(sc + (size_t)(d / G_) * F_ + f);
  u32x4 z = *(const u32x4*)(zr + (size_t)(d / G_) * F_ + f);
  int q[8] = {q0[0], q0[1], q0[2], q0[3], q1[0], q1[1], q1[2], q1[3]};
  u32x4 o;
#pragma unroll
  for (int j = 0; j < 4; ++j) {
    unsigned qq = cvt_pk_bf16((float)q[2 * j], (float)q[2 * j + 1]);
    float a0 = __uint_as_float(qq << 16) - __uint_as_float(z[j] << 16);
    float a1 = __uint_as_float(qq & 0xffff0000u) - __uint_as_float(z[j] & 0xffff0000u);
    unsigned dd = cvt_pk_bf16(a0, a1);
    float b0 = __uint_as_float(dd << 16) * __uint_as_float(s[j] << 16);
    float b1 = __uint_as_float(dd & 0xffff0000u) * __uint_as_float(s[j] & 0xffff0000u);
    o[j] = cvt_pk_bf16(b0, b1);
  }
  *(u32x4*)(wq + (size_t)d * F_ + f) = o;
}

__global__ __launch_bounds__(256) void k_gemm(const unsigned short* __restrict__ A, const unsigned short* __restrict__ B,
                                              unsigned short* __restrict__ C, int M, int N, int K) {
  int lane = threadIdx.x & 63, wid = threadIdx.x >> 6;
  int c = lane & 15, kq = lane >> 4;
  int m0 = blockIdx.y * 256 + wid * 64, n0 = blockIdx.x * 128;
  f32x4 acc[4][8];
#pragma unroll
  for (int i = 0; i < 4; ++i)
#pragma unroll
    for (int j = 0; j < 8; ++j) acc[i][j] = (f32x4){0.f, 0.f, 0.f, 0.f};
  for (int k0 = 0; k0 < K; k0 += 32) {
    u32x4 a[4], b[8];
#pragma unroll
    for (int i = 0; i < 4; ++i) a[i] = *(const u32x4*)(A + (size_t)(m0 + i * 16 + c) * K + k0 + kq * 8);
#pragma unroll
    for (int r = 0; r < 8; ++r) b[r] = *(const u32x4*)(B + (size_t)(k0 + kq * 8 + r) * N + n0 + c * 8);
#pragma unroll
    for (int j = 0; j < 8; ++j) {
      u32x4 t;
#pragma unroll
      for (int r2 = 0; r2 < 4; ++r2) {
        unsigned lo = b[2 * r2][j >> 1], hi = b[2 * r2 + 1][j >> 1];
        t[r2] = (j & 1) ? __builtin_amdgcn_perm(hi, lo, 0x07060302u) : __builtin_amdgcn_perm(hi, lo, 0x05040100u);
      }
#pragma unroll
      for (int i = 0; i < 4; ++i)
        acc[i][j] = __builtin_amdgcn_mfma_f32_16x16x32_bf16(__builtin_bit_cast(bf16x8, a[i]), __builtin_bit_cast(bf16x8, t), acc[i][j], 0, 0, 0);
    }
  }
#pragma unroll
  for (int i = 0; i < 4; ++i)
#pragma unroll
    for (int r = 0; r < 4; ++r) {
      u32x4 o;
#pragma unroll
      for (int j2 = 0; j2 < 4; ++j2) o[j2] = cvt_pk_bf16(acc[i][2 * j2][r], acc[i][2 * j2 + 1][r]);
      *(u32x4*)(C + (size_t)(m0 + i * 16 + kq * 4 + r) * N + n0 + c * 8) = o;
    }
}

extern "C" void kernel_launch(void* const* d_in, const int* in_sizes, int n_in, void* d_out, int out_size, void* d_ws,
                              size_t ws_size, hipStream_t stream) {
  const unsigned short* x = (const unsigned short*)d_in[0];
  const int* qw = (const int*)d_in[1];
  const unsigned short* sc = (const unsigned short*)d_in[2];
  const unsigned short* zr = (const unsigned short*)d_in[3];
  const unsigned short* U = (const unsigned short*)d_in[4];
  const unsigned short* V = (const unsigned short*)d_in[5];
  unsigned short* y = (unsigned short*)d_out;
  unsigned short* wq = (unsigned short*)d_ws;
  unsigned short* xr = wq + (size_t)D_ * F_;
  unsigned short* W1 = xr + (size_t)S_ * D_;
  k_dequant<<<D_ * F_ / 8 / 256, 256, 0, stream>>>(qw, sc, zr, wq);
  k_gemm<<<dim3(D_ / 128, S_ / 256), 256, 0, stream>>>(x, U, xr, S_, D_, D_);
  k_gemm<<<dim3(F_ / 128, D_ / 256), 256, 0, stream>>>(wq, V, W1, D_, F_, F_);
  k_gemm<<<dim3(F_ / 128, S_ / 256), 256, 0, stream>>>(xr, W1, y, S_, F_, D_);
}
```

```cpp
#include <hip/hip_runtime.h>
#include <hip/hip_bf16.h>
#include <stdint.h>
#include <stdio.h>

typedef short bf16x8 __attribute__((ext_vector_type(8)));
typedef float f32x4 __attribute__((ext_vector_type(4)));
typedef unsigned u32x4 __attribute__((ext_vector_type(4)));
typedef unsigned u32x2 __attribute__((ext_vector_type(2)));
typedef int i32x4 __attribute__((ext_vector_type(4)));

constexpr int S_ = 2048, D_ = 1024, F_ = 3584, G_ = 32;

__device__ __forceinline__ float bf2f(unsigned short u) { return __uint_as_float(((unsigned)u) << 16); }
__device__ __forceinline__ unsigned cvt_pk_bf16(float lo, float hi) {
  unsigned r; asm("v_cvt_pk_bf16_f32 %0, %1, %2" : "=v"(r) : "v"(lo), "v"(hi)); return r;
}

__global__ void k_dequant(const int* __restrict__ qw, const unsigned short* __restrict__ sc, const unsigned short* __restrict__ zr,
                          unsigned short* __restrict__ wq) {
  int idx = blockIdx.x * blockDim.x + threadIdx.x;
  int d = idx / (F_ / 8), f = (idx % (F_ / 8)) * 8;
  const i32x4* p = (const i32x4*)(qw + (size_t)d * F_ + f);
  i32x4 q0 = p[0], q1 = p[1];
  u32x4 s = *(const u32x4*)(sc + (size_t)(d / G_) * F_ + f);
  u32x4 z = *(const u32x4*)(zr + (size_t)(d / G_) * F_ + f);
  int q[8] = {q0[0], q0[1], q0[2], q0[3], q1[0], q1[1], q1[2], q1[3]};
  u32x4 o;
#pragma unroll
  for (int j = 0; j < 4; ++j) {
    unsigned qq = cvt_pk_bf16((float)q[2 * j], (float)q[2 * j + 1]);
    float a0 = __uint_as_float(qq << 16) - __uint_as_float(z[j] << 16);
    float a1 = __uint_as_float(qq & 0xffff0000u) - __uint_as_float(z[j] & 0xffff0000u);
    unsigned dd = cvt_pk_bf16(a0, a1);
    float b0 = __uint_as_float(dd << 16) * __uint_as_float(s[j] << 16);
    float b1 = __uint_as_float(dd & 0xffff0000u) * __uint_as_float(s[j] & 0xffff0000u);
    o[j] = cvt_pk_bf16(b0, b1);
  }
  *(u32x4*)(wq + (size_t)d * F_ + f) = o;
}

__global__ __launch_bounds__(256) void k_gemm(const unsigned short* __restrict__ A, const unsigned short* __restrict__ B,
                                              unsigned short* __restrict__ C, int M, int N, int K) {
  int lane = threadIdx.x & 63, wid = threadIdx.x >> 6;
  int c = lane & 15, kq = lane >> 4;
  int m0 = blockIdx.y * 256 + wid * 64, n0 = blockIdx.x * 128;
  f32x4 acc[4][8];
#pragma unroll
  for (int i = 0; i < 4; ++i)
#pragma unroll
    for (int j = 0; j < 8; ++j) acc[i][j] = (f32x4){0.f, 0.f, 0.f, 0.f};
  for (int k0 = 0; k0 < K; k0 += 32) {
    u32x4 a[4], b[8];
#pragma unroll
    for (int i = 0; i < 4; ++i) a[i] = *(const u32x4*)(A + (size_t)(m0 + i * 16 + c) * K + k0 + kq * 8);
#pragma unroll
    for (int r = 0; r < 8; ++r) b[r] = *(const u32x4*)(B + (size_t)(k0 + kq * 8 + r) * N + n0 + c * 8);
#pragma unroll
    for (int j = 0; j < 8; ++j) {
      u32x4 t;
#pragma unroll
      for (int r2 = 0; r2 < 4; ++r2) {
        unsigned lo = b[2 * r2][j >> 1], hi = b[2 * r2 + 1][j >> 1];
        t[r2] = (j & 1) ? __builtin_amdgcn_perm(hi, lo, 0x07060302u) : __builtin_amdgcn_perm(hi, lo, 0x05040100u);
      }
#pragma unroll
      for (int i = 0; i < 4; ++i)
        acc[i][j] = __builtin_amdgcn_mfma_f32_16x16x32_bf16(__builtin_bit_cast(bf16x8, a[i]), __builtin_bit_cast(bf16x8, t), acc[i][j], 0, 0, 0);
    }
  }
#pragma unroll
  for (int i = 0; i < 4; ++i)
#pragma unroll
    for (int r = 0; r < 4; ++r) {
      u32x4 o;
#pragma unroll
      for (int j2 = 0; j2 < 4; ++j2) o[j2] = cvt_pk_bf16(acc[i][2 * j2][r], acc[i][2 * j2 + 1][r]);
      *(u32x4*)(C + (size_t)(m0 + i * 16 + kq * 4 + r) * N + n0 + c * 8) = o;
    }
}


template <int NT>
__global__ __launch_bounds__(1024) void k_probe(const u32x4* __restrict__ p, int n16, unsigned long long* __restrict__ st, unsigned* sink) {
  unsigned long long t0 = __builtin_amdgcn_s_memrealtime();
  int per = n16 / gridDim.x;
  const u32x4* q = p + (size_t)blockIdx.x * per;
  u32x4 acc = {0,0,0,0};
  for (int i = threadIdx.x; i < per; i += 4096) {
    u32x4 v[4];
#pragma unroll
    for (int j = 0; j < 4; ++j) {
      int k = i + j * 1024; if (k >= per) k = i;
      v[j] = NT ? __builtin_nontemporal_load(q + k) : q[k];
    }
#pragma unroll
    for (int j = 0; j < 4; ++j) acc ^= v[j];
  }
  if ((acc[0] ^ acc[1] ^ acc[2] ^ acc[3]) == 0x12345678u) sink[threadIdx.x] = 1;
  __syncthreads();
  unsigned long long t1 = __builtin_amdgcn_s_memrealtime();
  if (threadIdx.x == 0) { st[blockIdx.x * 2] = t0; st[blockIdx.x * 2 + 1] = t1; }
}
__global__ void k_print(const unsigned long long* st, int np) {
  if (threadIdx.x == 0) {
    float r[8];
    for (int p = 0; p < np; ++p) {
      unsigned long long a = ~0ull, b = 0;
      for (int i = 0; i < 256; ++i) { unsigned long long u = st[p * 512 + 2 * i], v = st[p * 512 + 2 * i + 1]; if (u < a) a = u; if (v > b) b = v; }
      r[p] = (b - a) * 0.01f;
    }
    printf("probe V %.2f qwnt %.2f x %.2f V2 %.2f qw2 %.2f\n", r[0], r[1], r[2], r[3], r[4]);
  }
}

extern "C" void kernel_launch(void* const* d_in, const int* in_sizes, int n_in, void* d_out, int out_size, void* d_ws,
                              size_t ws_size, hipStream_t stream) {
  const unsigned short* x = (const unsigned short*)d_in[0];
  const int* qw = (const int*)d_in[1];
  const unsigned short* sc = (const unsigned short*)d_in[2];
  const unsigned short* zr = (const unsigned short*)d_in[3];
  const unsigned short* U = (const unsigned short*)d_in[4];
  const unsigned short* V = (const unsigned short*)d_in[5];
  unsigned short* y = (unsigned short*)d_out;
  unsigned short* wq = (unsigned short*)d_ws;
  unsigned short* xr = wq + (size_t)D_ * F_;
  unsigned short* W1 = xr + (size_t)S_ * D_;
  unsigned long long* st = (unsigned long long*)((char*)d_ws + (64u << 20));
  unsigned* sink = (unsigned*)((char*)d_ws + (65u << 20));
  k_probe<0><<<256, 1024, 0, stream>>>((const u32x4*)V, F_ * F_ / 8, st, sink);
  k_probe<1><<<256, 1024, 0, stream>>>((const u32x4*)qw, D_ * F_ / 4, st + 512, sink);
  k_probe<0><<<256, 1024, 0, stream>>>((const u32x4*)x, S_ * D_ / 8, st + 1024, sink);
  k_probe<0><<<256, 1024, 0, stream>>>((const u32x4*)V, F_ * F_ / 8, st + 1536, sink);
  k_probe<0><<<256, 1024, 0, stream>>>((const u32x4*)qw, D_ * F_ / 4, st + 2048, sink);
  k_print<<<1, 64, 0, stream>>>(st, 5);
  k_dequant<<<D_ * F_ / 8 / 256, 256, 0, stream>>>(qw, sc, zr, wq);
  k_gemm<<<dim3(D_ / 128, S_ / 256), 256, 0, stream>>>(x, U, xr, S_, D_, D_);
  k_gemm<<<dim3(F_ / 128, D_ / 256), 256, 0, stream>>>(wq, V, W1, D_, F_, F_);
  k_gemm<<<dim3(F_ / 128, S_ / 256), 256, 0, stream>>>(xr, W1, y, S_, F_, D_);
}
```

```cpp
#include <hip/hip_runtime.h>
#include <hip/hip_bf16.h>
#include <stdint.h>
#include <stdio.h>

typedef short bf16x8 __attribute__((ext_vector_type(8)));
typedef float f32x4 __attribute__((ext_vector_type(4)));
typedef unsigned u32x4 __attribute__((ext_vector_type(4)));
typedef unsigned u32x2 __attribute__((ext_vector_type(2)));
typedef int i32x4 __attribute__((ext_vector_type(4)));

constexpr int S_ = 2048, D_ = 1024, F_ = 3584, G_ = 32;

__device__ __forceinline__ float bf2f(unsigned short u) { return __uint_as_float(((unsigned)u) << 16); }
__device__ __forceinline__ unsigned cvt_pk_bf16(float lo, float hi) {
  unsigned r; asm("v_cvt_pk_bf16_f32 %0, %1, %2" : "=v"(r) : "v"(lo), "v"(hi)); return r;
}

__global__ void k_dequant(const int* __restrict__ qw, const unsigned short* __restrict__ sc, const unsigned short* __restrict__ zr,
                          unsigned short* __restrict__ wq) {
  int idx = blockIdx.x * blockDim.x + threadIdx.x;
  int d = idx / (F_ / 8), f = (idx % (F_ / 8)) * 8;
  const i32x4* p = (const i32x4*)(qw + (size_t)d * F_ + f);
  i32x4 q0 = p[0], q1 = p[1];
  u32x4 s = *(const u32x4*)(sc + (size_t)(d / G_) * F_ + f);
  u32x4 z = *(const u32x4*)(zr + (size_t)(d / G_) * F_ + f);
  int q[8] = {q0[0], q0[1], q0[2], q0[3], q1[0], q1[1], q1[2], q1[3]};
  u32x4 o;
#pragma unroll
  for (int j = 0; j < 4; ++j) {
    unsigned qq = cvt_pk_bf16((float)q[2 * j], (float)q[2 * j + 1]);
    float a0 = __uint_as_float(qq << 16) - __uint_as_float(z[j] << 16);
    float a1 = __uint_as_float(qq & 0xffff0000u) - __uint_as_float(z[j] & 0xffff0000u);
    unsigned dd = cvt_pk_bf16(a0, a1);
    float b0 = __uint_as_float(dd << 16) * __uint_as_float(s[j] << 16);
    float b1 = __uint_as_float(dd & 0xffff0000u) * __uint_as_float(s[j] & 0xffff0000u);
    o[j] = cvt_pk_bf16(b0, b1);
  }
  *(u32x4*)(wq + (size_t)d * F_ + f) = o;
}

__global__ __launch_bounds__(256) void k_gemm(const unsigned short* __restrict__ A, const unsigned short* __restrict__ B,
                                              unsigned short* __restrict__ C, int M, int N, int K) {
  int lane = threadIdx.x & 63, wid = threadIdx.x >> 6;
  int c = lane & 15, kq = lane >> 4;
  int m0 = blockIdx.y * 256 + wid * 64, n0 = blockIdx.x * 128;
  f32x4 acc[4][8];
#pragma unroll
  for (int i = 0; i < 4; ++i)
#pragma unroll
    for (int j = 0; j < 8; ++j) acc[i][j] = (f32x4){0.f, 0.f, 0.f, 0.f};
  for (int k0 = 0; k0 < K; k0 += 32) {
    u32x4 a[4], b[8];
#pragma unroll
    for (int i = 0; i < 4; ++i) a[i] = *(const u32x4*)(A + (size_t)(m0 + i * 16 + c) * K + k0 + kq * 8);
#pragma unroll
    for (int r = 0; r < 8; ++r) b[r] = *(const u32x4*)(B + (size_t)(k0 + kq * 8 + r) * N + n0 + c * 8);
#pragma unroll
    for (int j = 0; j < 8; ++j) {
      u32x4 t;
#pragma unroll
      for (int r2 = 0; r2 < 4; ++r2) {
        unsigned lo = b[2 * r2][j >> 1], hi = b[2 * r2 + 1][j >> 1];
        t[r2] = (j & 1) ? __builtin_amdgcn_perm(hi, lo, 0x07060302u) : __builtin_amdgcn_perm(hi, lo, 0x05040100u);
      }
#pragma unroll
      for (int i = 0; i < 4; ++i)
        acc[i][j] = __builtin_amdgcn_mfma_f32_16x16x32_bf16(__builtin_bit_cast(bf16x8, a[i]), __builtin_bit_cast(bf16x8, t), acc[i][j], 0, 0, 0);
    }
  }
#pragma unroll
  for (int i = 0; i < 4; ++i)
#pragma unroll
    for (int r = 0; r < 4; ++r) {
      u32x4 o;
#pragma unroll
      for (int j2 = 0; j2 < 4; ++j2) o[j2] = cvt_pk_bf16(acc[i][2 * j2][r], acc[i][2 * j2 + 1][r]);
      *(u32x4*)(C + (size_t)(m0 + i * 16 + kq * 4 + r) * N + n0 + c * 8) = o;
    }
}


template <int NT>
__global__ __launch_bounds__(1024) void k_probe(const u32x4* __restrict__ p, int n16, unsigned long long* __restrict__ st, unsigned* sink) {
  unsigned long long t0 = __builtin_amdgcn_s_memrealtime();
  int per = n16 / gridDim.x;
  const u32x4* q = p + (size_t)blockIdx.x * per;
  u32x4 acc = {0,0,0,0};
  for (int i = threadIdx.x; i < per; i += 4096) {
    u32x4 v[4];
#pragma unroll
    for (int j = 0; j < 4; ++j) {
      int k = i + j * 1024; if (k >= per) k = i;
      v[j] = NT ? __builtin_nontemporal_load(q + k) : q[k];
    }
#pragma unroll
    for (int j = 0; j < 4; ++j) acc ^= v[j];
  }
  if ((acc[0] ^ acc[1] ^ acc[2] ^ acc[3]) == 0x12345678u) sink[threadIdx.x] = 1;
  __syncthreads();
  unsigned long long t1 = __builtin_amdgcn_s_memrealtime();
  if (threadIdx.x == 0) { st[blockIdx.x * 2] = t0; st[blockIdx.x * 2 + 1] = t1; }
}

__global__ void k_spin(const unsigned long long* st, int mult) {
  if (threadIdx.x == 0) {
    unsigned long long a = ~0ull, b = 0;
    for (int i = 0; i < 256; ++i) { unsigned long long u = st[2 * i], v = st[2 * i + 1]; if (u < a) a = u; if (v > b) b = v; }
    unsigned long long d = (b - a) * mult; if (d > 100000) d = 100000;
    unsigned long long t0 = __builtin_amdgcn_s_memrealtime();
    while (__builtin_amdgcn_s_memrealtime() - t0 < d) __builtin_amdgcn_s_sleep(1);
  }
}
extern "C" void kernel_launch(void* const* d_in, const int* in_sizes, int n_in, void* d_out, int out_size, void* d_ws,
                              size_t ws_size, hipStream_t stream) {
  const unsigned short* x = (const unsigned short*)d_in[0];
  const int* qw = (const int*)d_in[1];
  const unsigned short* sc = (const unsigned short*)d_in[2];
  const unsigned short* zr = (const unsigned short*)d_in[3];
  const unsigned short* U = (const unsigned short*)d_in[4];
  const unsigned short* V = (const unsigned short*)d_in[5];
  unsigned short* y = (unsigned short*)d_out;
  unsigned short* wq = (unsigned short*)d_ws;
  unsigned short* xr = wq + (size_t)D_ * F_;
  unsigned short* W1 = xr + (size_t)S_ * D_;
  unsigned long long* st = (unsigned long long*)((char*)d_ws + (64u << 20));
  unsigned* sink = (unsigned*)((char*)d_ws + (65u << 20));
  k_probe<0><<<256, 1024, 0, stream>>>((const u32x4*)V, F_ * F_ / 8, st, sink);
  k_spin<<<1, 64, 0, stream>>>(st, 0);
  k_dequant<<<D_ * F_ / 8 / 256, 256, 0, stream>>>(qw, sc, zr, wq);
  k_gemm<<<dim3(D_ / 128, S_ / 256), 256, 0, stream>>>(x, U, xr, S_, D_, D_);
  k_gemm<<<dim3(F_ / 128, D_ / 256), 256, 0, stream>>>(wq, V, W1, D_, F_, F_);
  k_gemm<<<dim3(F_ / 128, S_ / 256), 256, 0, stream>>>(xr, W1, y, S_, F_, D_);
}
```

```cpp
#include <hip/hip_runtime.h>
#include <stdint.h>

extern "C" __global__ __launch_bounds__(256, 1) void k_all(const char* x, const char* U, const int* qw, const unsigned short* sc, const unsigned short* zr, const char* V, char* ws, char* y) {
  asm volatile(
R"(.macro MA d,a,b
v_mfma_f32_16x16x32_bf16 v[\d:\d+3], a[\a:\a+3], v[\b:\b+3], v[\d:\d+3]
.endm
.macro MV d,a,b
v_mfma_f32_16x16x32_bf16 v[\d:\d+3], v[\a:\a+3], a[\b:\b+3], v[\d:\d+3]
.endm
.macro MC d,a,b
v_mfma_f32_16x16x32_bf16 a[\d:\d+3], v[\a:\a+3], a[\b:\b+3], a[\d:\d+3]
.endm
.macro RT d,a,o
ds_read_b64_tr_b16 v[\d:\d+1], v[\a] offset:\o
.endm
.macro RB d,a,o
ds_read_b128 v[\d:\d+3], v[\a] offset:\o
.endm
.macro WL n
s_waitcnt lgkmcnt(\n)
.endm
s_load_dwordx16 s[8:23], %0, 0
v_lshrrev_b32 v2, 6, %2
v_and_b32 v1, 63, %2
s_lshr_b32 s6, %1, 3
v_lshl_add_u32 v3, s6, 2, v2
v_min_u32 v4, 9, v1
v_lshl_add_u32 v3, v4, 7, v3
v_lshlrev_b32 v3, 7, v3
s_getpc_b64 s[4:5]
.Lpc:
v_min_u32 v3, .Lkend-.Lpc-4, v3
global_load_dword v4, v3, s[4:5]
v_readfirstlane_b32 s7, v2
v_lshlrev_b32 v3, 4, v1
s_cmp_ge_u32 %1, 224
s_cbranch_scc1 L_g_%=
s_and_b32 s24, %1, 1
s_bfe_u32 s25, %1, 0x20003
s_bfe_u32 s26, %1, 0x20001
s_mul_i32 s26, s26, 7
s_lshr_b32 s27, %1, 5
s_add_u32 s26, s26, s27
s_lshl_b32 s52, s7, 11
s_lshl_b32 s27, s25, 2
s_add_u32 s27, s27, s7
s_and_b32 s34, s26, 1
s_lshl_b32 s28, s27, 1
s_add_u32 s28, s28, s34
s_mul_i32 s29, s24, 56
s_lshr_b32 s31, s26, 1
s_add_u32 s29, s29, s31
s_mul_i32 s31, s28, 458752
s_lshl_b32 s84, s29, 7
s_add_u32 s84, s84, s31
s_mul_i32 s31, s28, 7168
s_lshl_b32 s85, s29, 6
s_add_u32 s85, s85, s31
v_lshrrev_b32 v134, 4, v1
v_lshlrev_b32 v84, 4, v134
v_lshrrev_b32 v135, 3, v1
v_mul_u32_u24 v135, 14336, v135
v_or_b32 v136, 0, v134
v_xor_b32 v136, v136, v1
v_and_b32 v136, 7, v136
v_lshl_add_u32 v78, v136, 4, v135
v_or_b32 v136, 4, v134
v_xor_b32 v136, v136, v1
v_and_b32 v136, 7, v136
v_lshl_add_u32 v79, v136, 4, v135
v_add_u32 v80, 229376, v78
v_add_u32 v81, 343040, v79
v_add_u32 v79, 113664, v79
s_mov_b32 s74, 0xe00000
s_mov_b32 s75, 0x20000
s_mov_b32 s78, 0x38000
s_mov_b32 s79, 0x20000
s_mov_b32 s82, 0x38000
s_mov_b32 s83, 0x20000
s_mov_b32 s31, 0x3020000
WL 0
s_mov_b64 s[72:73], s[12:13]
s_mov_b64 s[76:77], s[14:15]
s_mov_b64 s[80:81], s[16:17]
buffer_load_dwordx4 v[102:105], v84, s[80:83], s85 offen offset:0
buffer_load_dwordx4 v[86:89], v84, s[76:79], s85 offen offset:0
s_add_u32 m0, s52, 98304
s_nop 0
buffer_load_dwordx4 v78, s[72:75], s84 offen offset:0 nt lds
buffer_load_dwordx4 v79, s[72:75], s84 offen offset:1024 nt lds
s_add_u32 m0, s52, 106496
s_nop 0
buffer_load_dwordx4 v80, s[72:75], s84 offen offset:0 nt lds
buffer_load_dwordx4 v81, s[72:75], s84 offen offset:1024 nt lds
s_add_u32 s84, s84, 1792
s_load_dwordx2 s[98:99], s[20:21], s31
buffer_load_dwordx4 v[106:109], v84, s[80:83], s85 offen offset:896
buffer_load_dwordx4 v[90:93], v84, s[76:79], s85 offen offset:896
s_add_u32 m0, s52, 114688
s_nop 0
buffer_load_dwordx4 v78, s[72:75], s84 offen offset:0 nt lds
buffer_load_dwordx4 v79, s[72:75], s84 offen offset:1024 nt lds
s_add_u32 m0, s52, 122880
s_nop 0
buffer_load_dwordx4 v80, s[72:75], s84 offen offset:0 nt lds
buffer_load_dwordx4 v81, s[72:75], s84 offen offset:1024 nt lds
s_add_u32 s84, s84, 1792
s_mul_i32 s31, s27, 112
s_add_u32 s31, s31, s29
s_lshl_b32 s31, s31, 1
s_add_u32 s31, s31, s34
s_lshl_b32 s87, s31, 11
v_and_b32 v135, 15, v1
v_lshrrev_b32 v136, 1, v135
v_lshlrev_b32 v134, 1, v134
v_xor_b32 v136, v136, v134
v_lshlrev_b32 v136, 4, v136
v_lshlrev_b32 v135, 7, v135
s_or_b32 s31, s52, 0x18000
v_add3_u32 v82, v135, v136, s31
v_xor_b32 v83, 16, v82
s_mov_b32 s88, 0xffff0000
s_mov_b64 s[40:41], s[20:21]
s_mov_b32 s42, 0x700000
s_mov_b32 s43, 0x20000
s_add_u32 s92, s20, 0x2000000
s_addc_u32 s93, s21, 0
s_mov_b32 s94, 0x1040000
s_mov_b32 s95, 0x20000
s_lshl_b32 s31, s27, 1
s_add_u32 s31, s31, s24
s_lshl_b32 s31, s31, 9
s_add_u32 s31, s31, 0x1008000
s_lshl_b32 s89, s26, 3
s_add_u32 s89, s89, s31
v_min_u32 v135, 27, v1
v_lshl_add_u32 v47, v135, 3, s31
s_and_b32 s31, %1, 1
s_lshl_b32 s31, s31, 3
s_bfe_u32 s30, %1, 0x30003
s_add_u32 s31, s31, s30
s_lshl_b32 s31, s31, 8
s_add_u32 s31, s31, 0x1001000
v_min_u32 v135, 7, v1
v_lshl_add_u32 v48, v135, 3, s31
s_and_b32 s24, %1, 1
s_bfe_u32 s25, %1, 0x20003
s_bfe_u32 s26, %1, 0x20001
s_mul_i32 s26, s26, 7
s_lshr_b32 s27, %1, 5
s_add_u32 s26, s26, s27
s_mul_i32 s60, s24, 224
s_add_u32 s60, s60, s7
s_mul_i32 s60, s60, 57344
s_lshl_b32 s27, s26, 8
s_add_u32 s60, s60, s27
s_lshl_b32 s52, s7, 11
v_lshrrev_b32 v54, 4, v1
v_and_b32 v55, 15, v1
s_and_b32 s53, s7, 1
s_lshl_b32 s53, s53, 2
v_or_b32 v56, s53, v54
v_lshlrev_b32 v56, 1, v56
v_xor_b32 v56, v56, v55
v_mul_u32_u24 v57, 7168, v54
v_lshl_add_u32 v18, v56, 4, v57
v_add_u32 v19, 27648, v18
s_mov_b32 s46, 0x1880000
s_mov_b32 s47, 0x20000
s_mov_b64 s[44:45], s[18:19]
s_mul_i32 s30, s24, 56
s_lshl_b32 s31, s25, 2
s_add_u32 s30, s30, s31
s_add_u32 s30, s30, s7
s_add_u32 s30, s30, 8
s_mul_i32 s30, s30, 229376
s_lshl_b32 s31, s26, 8
s_add_u32 s58, s30, s31
v_lshrrev_b32 v50, 1, v1
v_mul_u32_u24 v50, 7168, v50
v_and_b32 v49, 1, v1
v_lshl_add_u32 v49, v49, 7, v50
s_add_u32 s30, s24, 1
s_mul_i32 s61, s30, 12845056
s_lshr_b32 s30, %1, 5
s_lshl_b32 s30, s30, 3
s_and_b32 s31, %1, 7
s_add_u32 s30, s30, s31
s_lshl_b32 s30, s30, 2
s_add_u32 s30, s30, s7
s_lshl_b32 s30, s30, 13
s_add_u32 s30, s30, 262144
s_cmp_eq_u32 s25, 0
s_cselect_b32 s59, s30, 0x40000000
s_mov_b64 s[12:13], s[10:11]
s_mov_b32 s14, 0x200000
s_mov_b32 s15, 0x20000
v_lshrrev_b32 v46, 4, v1
v_lshlrev_b32 v46, 11, v46
v_and_b32 v51, 15, v1
v_lshl_add_u32 v51, v51, 7, v46
s_lshl_b32 s27, s25, 2
s_add_u32 s27, s27, s7
s_lshl_b32 s55, s27, 1
s_add_u32 s55, s55, s24
s_mul_i32 s63, s55, 229376
s_mov_b32 s86, s63
s_lshl_b32 s53, s24, 11
v_add_u32 v16, s53, v3
s_xor_b32 s53, s53, 2048
v_add_u32 v17, s53, v3
v_lshrrev_b32 v54, 4, v1
v_bfe_u32 v55, v1, 2, 2
v_and_b32 v56, 1, v54
v_lshlrev_b32 v56, 2, v56
v_or_b32 v56, v56, v55
v_lshlrev_b32 v57, 3, v54
v_add_u32 v57, v57, v55
v_lshlrev_b32 v57, 8, v57
v_and_b32 v58, 3, v1
v_lshlrev_b32 v58, 3, v58
v_add_u32 v57, v57, v58
v_add_u32 v57, 0, v57
v_xor_b32 v58, 0, v56
v_lshlrev_b32 v58, 5, v58
v_add_u32 v20, v57, v58
v_xor_b32 v58, 1, v56
v_lshlrev_b32 v58, 5, v58
v_add_u32 v21, v57, v58
v_xor_b32 v58, 2, v56
v_lshlrev_b32 v58, 5, v58
v_add_u32 v22, v57, v58
v_xor_b32 v58, 3, v56
v_lshlrev_b32 v58, 5, v58
v_add_u32 v23, v57, v58
v_xor_b32 v58, 4, v56
v_lshlrev_b32 v58, 5, v58
v_add_u32 v24, v57, v58
v_xor_b32 v58, 5, v56
v_lshlrev_b32 v58, 5, v58
v_add_u32 v25, v57, v58
v_xor_b32 v58, 6, v56
v_lshlrev_b32 v58, 5, v58
v_add_u32 v26, v57, v58
v_xor_b32 v58, 7, v56
v_lshlrev_b32 v58, 5, v58
v_add_u32 v27, v57, v58
v_add_u32 v28, 65536, v20
v_add_u32 v29, 65536, v21
v_add_u32 v30, 65536, v22
v_add_u32 v31, 65536, v23
v_add_u32 v32, 65536, v24
v_add_u32 v33, 65536, v25
v_add_u32 v34, 65536, v26
v_add_u32 v35, 65536, v27
v_add_u32 v36, 131072, v20
v_add_u32 v37, 131072, v21
v_add_u32 v38, 131072, v22
v_add_u32 v39, 131072, v23
v_add_u32 v40, 131072, v24
v_add_u32 v41, 131072, v25
v_add_u32 v42, 131072, v26
v_add_u32 v43, 131072, v27
s_add_u32 s48, s20, 0x1000000
s_addc_u32 s49, s21, 0
s_mov_b32 s50, 0x700000
s_mov_b32 s51, 0x20000
s_lshl_b32 s28, s26, 3
s_add_u32 s53, s28, 0
s_mul_i32 s64, s53, 4682
s_lshr_b32 s64, s64, 16
s_mul_i32 s64, s64, 434
s_add_u32 s64, s64, s53
s_mul_i32 s53, s55, 14
s_add_u32 s64, s64, s53
s_lshl_b32 s64, s64, 10
s_add_u32 s53, s28, 1
s_mul_i32 s65, s53, 4682
s_lshr_b32 s65, s65, 16
s_mul_i32 s65, s65, 434
s_add_u32 s65, s65, s53
s_mul_i32 s53, s55, 14
s_add_u32 s65, s65, s53
s_lshl_b32 s65, s65, 10
s_add_u32 s53, s28, 2
s_mul_i32 s66, s53, 4682
s_lshr_b32 s66, s66, 16
s_mul_i32 s66, s66, 434
s_add_u32 s66, s66, s53
s_mul_i32 s53, s55, 14
s_add_u32 s66, s66, s53
s_lshl_b32 s66, s66, 10
s_add_u32 s53, s28, 3
s_mul_i32 s67, s53, 4682
s_lshr_b32 s67, s67, 16
s_mul_i32 s67, s67, 434
s_add_u32 s67, s67, s53
s_mul_i32 s53, s55, 14
s_add_u32 s67, s67, s53
s_lshl_b32 s67, s67, 10
s_add_u32 s53, s28, 4
s_mul_i32 s68, s53, 4682
s_lshr_b32 s68, s68, 16
s_mul_i32 s68, s68, 434
s_add_u32 s68, s68, s53
s_mul_i32 s53, s55, 14
s_add_u32 s68, s68, s53
s_lshl_b32 s68, s68, 10
s_add_u32 s53, s28, 5
s_mul_i32 s69, s53, 4682
s_lshr_b32 s69, s69, 16
s_mul_i32 s69, s69, 434
s_add_u32 s69, s69, s53
s_mul_i32 s53, s55, 14
s_add_u32 s69, s69, s53
s_lshl_b32 s69, s69, 10
s_add_u32 s53, s28, 6
s_mul_i32 s70, s53, 4682
s_lshr_b32 s70, s70, 16
s_mul_i32 s70, s70, 434
s_add_u32 s70, s70, s53
s_mul_i32 s53, s55, 14
s_add_u32 s70, s70, s53
s_lshl_b32 s70, s70, 10
s_add_u32 s53, s28, 7
s_mul_i32 s71, s53, 4682
s_lshr_b32 s71, s71, 16
s_mul_i32 s71, s71, 434
s_add_u32 s71, s71, s53
s_mul_i32 s53, s55, 14
s_add_u32 s71, s71, s53
s_lshl_b32 s71, s71, 10
s_lshl_b32 s56, %1, 2
s_add_u32 s56, s56, s7
s_lshl_b32 s56, s56, 13
s_xor_b32 s57, s56, 0x8000
v_bfe_u32 v54, v1, 2, 2
v_lshrrev_b32 v55, 4, v1
v_cmp_eq_u32 vcc, v54, v55
v_and_b32 v54, 1, v1
v_lshlrev_b32 v54, 4, v54
v_mov_b32 v55, 0x3f80
v_lshlrev_b32 v54, v54, v55
v_cndmask_b32 v54, 0, v54, vcc
v_and_b32 v55, 2, v1
v_cmp_eq_u32 vcc, 0, v55
v_mov_b32 v224, 0
v_mov_b32 v225, 0
v_mov_b32 v226, 0
v_mov_b32 v227, 0
v_cndmask_b32 v222, 0, v54, vcc
v_cndmask_b32 v223, v54, v224, vcc
v_mov_b32 v228, v222
v_mov_b32 v229, v223
s_and_b32 s8, %1, 1
s_lshl_b32 s8, s8, 3
s_bfe_u32 s9, %1, 0x30003
s_add_u32 s8, s8, s9
s_lshl_b32 s8, s8, 1
s_lshr_b32 s9, s7, 1
s_add_u32 s8, s8, s9
s_lshl_b32 s8, s8, 17
s_and_b32 s9, s7, 1
s_lshl_b32 s9, s9, 11
s_add_u32 s8, s8, s9
v_add_u32 v2, s8, v3
s_mov_b32 s35, 0
s_add_u32 s36, s20, 0x800000
s_addc_u32 s37, s21, 0
s_mov_b32 s38, 0x400000
s_mov_b32 s39, 0x20000
s_waitcnt vmcnt(6)
WL 0
s_add_u32 s98, s98, 1
s_addc_u32 s99, s99, 0
v_mov_b32 v206, s98
v_mov_b32 v207, s99
RB 134,82,0
RB 138,83,0
RB 142,82,8192
RB 146,83,8192
v_lshlrev_b32 v126, 16, v102
v_and_b32 v127, s88, v102
v_lshlrev_b32 v118, 16, v86
v_and_b32 v119, s88, v86
v_lshlrev_b32 v128, 16, v103
v_and_b32 v129, s88, v103
v_lshlrev_b32 v120, 16, v87
v_and_b32 v121, s88, v87
v_lshlrev_b32 v130, 16, v104
v_and_b32 v131, s88, v104
v_lshlrev_b32 v122, 16, v88
v_and_b32 v123, s88, v88
v_lshlrev_b32 v132, 16, v105
v_and_b32 v133, s88, v105
v_lshlrev_b32 v124, 16, v89
v_and_b32 v125, s88, v89
WL 2
v_cvt_f32_i32 v134, v134
v_cvt_f32_i32 v135, v135
v_cvt_f32_i32 v136, v136
v_cvt_f32_i32 v137, v137
v_cvt_f32_i32 v138, v138
)"
R"(v_cvt_f32_i32 v139, v139
v_cvt_f32_i32 v140, v140
v_cvt_f32_i32 v141, v141
v_cvt_pk_bf16_f32 v166, v134, v135
v_cvt_pk_bf16_f32 v167, v136, v137
v_cvt_pk_bf16_f32 v168, v138, v139
v_cvt_pk_bf16_f32 v169, v140, v141
v_lshlrev_b32 v134, 16, v166
v_and_b32 v135, s88, v166
v_lshlrev_b32 v136, 16, v167
v_and_b32 v137, s88, v167
v_lshlrev_b32 v138, 16, v168
v_and_b32 v139, s88, v168
v_lshlrev_b32 v140, 16, v169
v_and_b32 v141, s88, v169
v_pk_add_f32 v[134:135], v[134:135], v[126:127] neg_lo:[0,1] neg_hi:[0,1]
v_pk_add_f32 v[136:137], v[136:137], v[128:129] neg_lo:[0,1] neg_hi:[0,1]
v_pk_add_f32 v[138:139], v[138:139], v[130:131] neg_lo:[0,1] neg_hi:[0,1]
v_pk_add_f32 v[140:141], v[140:141], v[132:133] neg_lo:[0,1] neg_hi:[0,1]
v_cvt_pk_bf16_f32 v166, v134, v135
v_cvt_pk_bf16_f32 v167, v136, v137
v_cvt_pk_bf16_f32 v168, v138, v139
v_cvt_pk_bf16_f32 v169, v140, v141
v_lshlrev_b32 v134, 16, v166
v_and_b32 v135, s88, v166
v_lshlrev_b32 v136, 16, v167
v_and_b32 v137, s88, v167
v_lshlrev_b32 v138, 16, v168
v_and_b32 v139, s88, v168
v_lshlrev_b32 v140, 16, v169
v_and_b32 v141, s88, v169
v_pk_mul_f32 v[134:135], v[134:135], v[118:119]
v_pk_mul_f32 v[136:137], v[136:137], v[120:121]
v_pk_mul_f32 v[138:139], v[138:139], v[122:123]
v_pk_mul_f32 v[140:141], v[140:141], v[124:125]
v_cvt_pk_bf16_f32 v174, v134, v135
v_cvt_pk_bf16_f32 v175, v136, v137
v_cvt_pk_bf16_f32 v176, v138, v139
v_cvt_pk_bf16_f32 v177, v140, v141
buffer_store_dwordx4 v[174:177], v3, s[40:43], s87 offen offset:0 sc1
WL 0
v_cvt_f32_i32 v142, v142
v_cvt_f32_i32 v143, v143
v_cvt_f32_i32 v144, v144
v_cvt_f32_i32 v145, v145
v_cvt_f32_i32 v146, v146
v_cvt_f32_i32 v147, v147
v_cvt_f32_i32 v148, v148
v_cvt_f32_i32 v149, v149
v_cvt_pk_bf16_f32 v170, v142, v143
v_cvt_pk_bf16_f32 v171, v144, v145
v_cvt_pk_bf16_f32 v172, v146, v147
v_cvt_pk_bf16_f32 v173, v148, v149
v_lshlrev_b32 v142, 16, v170
v_and_b32 v143, s88, v170
v_lshlrev_b32 v144, 16, v171
v_and_b32 v145, s88, v171
v_lshlrev_b32 v146, 16, v172
v_and_b32 v147, s88, v172
v_lshlrev_b32 v148, 16, v173
v_and_b32 v149, s88, v173
v_pk_add_f32 v[142:143], v[142:143], v[126:127] neg_lo:[0,1] neg_hi:[0,1]
v_pk_add_f32 v[144:145], v[144:145], v[128:129] neg_lo:[0,1] neg_hi:[0,1]
v_pk_add_f32 v[146:147], v[146:147], v[130:131] neg_lo:[0,1] neg_hi:[0,1]
v_pk_add_f32 v[148:149], v[148:149], v[132:133] neg_lo:[0,1] neg_hi:[0,1]
v_cvt_pk_bf16_f32 v170, v142, v143
v_cvt_pk_bf16_f32 v171, v144, v145
v_cvt_pk_bf16_f32 v172, v146, v147
v_cvt_pk_bf16_f32 v173, v148, v149
v_lshlrev_b32 v142, 16, v170
v_and_b32 v143, s88, v170
v_lshlrev_b32 v144, 16, v171
v_and_b32 v145, s88, v171
v_lshlrev_b32 v146, 16, v172
v_and_b32 v147, s88, v172
v_lshlrev_b32 v148, 16, v173
v_and_b32 v149, s88, v173
v_pk_mul_f32 v[142:143], v[142:143], v[118:119]
v_pk_mul_f32 v[144:145], v[144:145], v[120:121]
v_pk_mul_f32 v[146:147], v[146:147], v[122:123]
v_pk_mul_f32 v[148:149], v[148:149], v[124:125]
v_cvt_pk_bf16_f32 v178, v142, v143
v_cvt_pk_bf16_f32 v179, v144, v145
v_cvt_pk_bf16_f32 v180, v146, v147
v_cvt_pk_bf16_f32 v181, v148, v149
buffer_store_dwordx4 v[178:181], v3, s[40:43], s87 offen offset:1024 sc1
s_add_u32 s87, s87, 57344
buffer_load_dwordx4 v[110:113], v84, s[80:83], s85 offen offset:1792
buffer_load_dwordx4 v[94:97], v84, s[76:79], s85 offen offset:1792
s_add_u32 m0, s52, 131072
s_nop 0
buffer_load_dwordx4 v78, s[72:75], s84 offen offset:0 nt lds
buffer_load_dwordx4 v79, s[72:75], s84 offen offset:1024 nt lds
s_add_u32 m0, s52, 139264
s_nop 0
buffer_load_dwordx4 v80, s[72:75], s84 offen offset:0 nt lds
buffer_load_dwordx4 v81, s[72:75], s84 offen offset:1024 nt lds
s_add_u32 s84, s84, 1792
buffer_load_dwordx4 v[114:117], v84, s[80:83], s85 offen offset:2688
buffer_load_dwordx4 v[98:101], v84, s[76:79], s85 offen offset:2688
s_add_u32 m0, s52, 147456
s_nop 0
buffer_load_dwordx4 v78, s[72:75], s84 offen offset:0 nt lds
buffer_load_dwordx4 v79, s[72:75], s84 offen offset:1024 nt lds
s_add_u32 m0, s52, 155648
s_nop 0
buffer_load_dwordx4 v80, s[72:75], s84 offen offset:0 nt lds
buffer_load_dwordx4 v81, s[72:75], s84 offen offset:1024 nt lds
s_waitcnt vmcnt(14)
RB 150,82,16384
RB 154,83,16384
RB 158,82,24576
RB 162,83,24576
v_lshlrev_b32 v126, 16, v106
v_and_b32 v127, s88, v106
v_lshlrev_b32 v118, 16, v90
v_and_b32 v119, s88, v90
v_lshlrev_b32 v128, 16, v107
v_and_b32 v129, s88, v107
v_lshlrev_b32 v120, 16, v91
v_and_b32 v121, s88, v91
v_lshlrev_b32 v130, 16, v108
v_and_b32 v131, s88, v108
v_lshlrev_b32 v122, 16, v92
v_and_b32 v123, s88, v92
v_lshlrev_b32 v132, 16, v109
v_and_b32 v133, s88, v109
v_lshlrev_b32 v124, 16, v93
v_and_b32 v125, s88, v93
WL 2
v_cvt_f32_i32 v150, v150
v_cvt_f32_i32 v151, v151
v_cvt_f32_i32 v152, v152
v_cvt_f32_i32 v153, v153
v_cvt_f32_i32 v154, v154
v_cvt_f32_i32 v155, v155
v_cvt_f32_i32 v156, v156
v_cvt_f32_i32 v157, v157
v_cvt_pk_bf16_f32 v166, v150, v151
v_cvt_pk_bf16_f32 v167, v152, v153
v_cvt_pk_bf16_f32 v168, v154, v155
v_cvt_pk_bf16_f32 v169, v156, v157
v_lshlrev_b32 v150, 16, v166
v_and_b32 v151, s88, v166
v_lshlrev_b32 v152, 16, v167
v_and_b32 v153, s88, v167
v_lshlrev_b32 v154, 16, v168
v_and_b32 v155, s88, v168
v_lshlrev_b32 v156, 16, v169
v_and_b32 v157, s88, v169
v_pk_add_f32 v[150:151], v[150:151], v[126:127] neg_lo:[0,1] neg_hi:[0,1]
v_pk_add_f32 v[152:153], v[152:153], v[128:129] neg_lo:[0,1] neg_hi:[0,1]
v_pk_add_f32 v[154:155], v[154:155], v[130:131] neg_lo:[0,1] neg_hi:[0,1]
v_pk_add_f32 v[156:157], v[156:157], v[132:133] neg_lo:[0,1] neg_hi:[0,1]
v_cvt_pk_bf16_f32 v166, v150, v151
v_cvt_pk_bf16_f32 v167, v152, v153
v_cvt_pk_bf16_f32 v168, v154, v155
v_cvt_pk_bf16_f32 v169, v156, v157
v_lshlrev_b32 v150, 16, v166
v_and_b32 v151, s88, v166
v_lshlrev_b32 v152, 16, v167
v_and_b32 v153, s88, v167
v_lshlrev_b32 v154, 16, v168
v_and_b32 v155, s88, v168
v_lshlrev_b32 v156, 16, v169
v_and_b32 v157, s88, v169
v_pk_mul_f32 v[150:151], v[150:151], v[118:119]
v_pk_mul_f32 v[152:153], v[152:153], v[120:121]
v_pk_mul_f32 v[154:155], v[154:155], v[122:123]
v_pk_mul_f32 v[156:157], v[156:157], v[124:125]
v_cvt_pk_bf16_f32 v182, v150, v151
v_cvt_pk_bf16_f32 v183, v152, v153
v_cvt_pk_bf16_f32 v184, v154, v155
v_cvt_pk_bf16_f32 v185, v156, v157
buffer_store_dwordx4 v[182:185], v3, s[40:43], s87 offen offset:0 sc1
WL 0
v_cvt_f32_i32 v158, v158
v_cvt_f32_i32 v159, v159
v_cvt_f32_i32 v160, v160
v_cvt_f32_i32 v161, v161
v_cvt_f32_i32 v162, v162
v_cvt_f32_i32 v163, v163
v_cvt_f32_i32 v164, v164
v_cvt_f32_i32 v165, v165
v_cvt_pk_bf16_f32 v170, v158, v159
v_cvt_pk_bf16_f32 v171, v160, v161
v_cvt_pk_bf16_f32 v172, v162, v163
v_cvt_pk_bf16_f32 v173, v164, v165
v_lshlrev_b32 v158, 16, v170
v_and_b32 v159, s88, v170
v_lshlrev_b32 v160, 16, v171
v_and_b32 v161, s88, v171
v_lshlrev_b32 v162, 16, v172
v_and_b32 v163, s88, v172
v_lshlrev_b32 v164, 16, v173
v_and_b32 v165, s88, v173
v_pk_add_f32 v[158:159], v[158:159], v[126:127] neg_lo:[0,1] neg_hi:[0,1]
v_pk_add_f32 v[160:161], v[160:161], v[128:129] neg_lo:[0,1] neg_hi:[0,1]
v_pk_add_f32 v[162:163], v[162:163], v[130:131] neg_lo:[0,1] neg_hi:[0,1]
v_pk_add_f32 v[164:165], v[164:165], v[132:133] neg_lo:[0,1] neg_hi:[0,1]
v_cvt_pk_bf16_f32 v170, v158, v159
v_cvt_pk_bf16_f32 v171, v160, v161
v_cvt_pk_bf16_f32 v172, v162, v163
v_cvt_pk_bf16_f32 v173, v164, v165
v_lshlrev_b32 v158, 16, v170
v_and_b32 v159, s88, v170
v_lshlrev_b32 v160, 16, v171
v_and_b32 v161, s88, v171
v_lshlrev_b32 v162, 16, v172
v_and_b32 v163, s88, v172
v_lshlrev_b32 v164, 16, v173
v_and_b32 v165, s88, v173
v_pk_mul_f32 v[158:159], v[158:159], v[118:119]
v_pk_mul_f32 v[160:161], v[160:161], v[120:121]
v_pk_mul_f32 v[162:163], v[162:163], v[122:123]
v_pk_mul_f32 v[164:165], v[164:165], v[124:125]
v_cvt_pk_bf16_f32 v186, v158, v159
v_cvt_pk_bf16_f32 v187, v160, v161
v_cvt_pk_bf16_f32 v188, v162, v163
v_cvt_pk_bf16_f32 v189, v164, v165
buffer_store_dwordx4 v[186:189], v3, s[40:43], s87 offen offset:1024 sc1
s_add_u32 s87, s87, 57344
s_waitcnt vmcnt(14)
s_mov_b64 exec, 1
buffer_store_dwordx2 v[206:207], off, s[92:95], s89 offset:0 sc1
s_mov_b64 exec, -1
buffer_load_dword v50, v51, s[12:15], s59 offen
s_add_u32 m0, s52, 0
s_nop 0
buffer_load_dwordx4 v18, s[44:47], s60 offen offset:0 nt lds
buffer_load_dwordx4 v19, s[44:47], s60 offen offset:1024 nt lds
s_add_u32 s60, s60, 229376
s_add_u32 m0, s52, 8192
s_nop 0
buffer_load_dwordx4 v18, s[44:47], s60 offen offset:0 nt lds
buffer_load_dwordx4 v19, s[44:47], s60 offen offset:1024 nt lds
s_add_u32 s60, s60, 229376
s_add_u32 m0, s52, 16384
s_nop 0
buffer_load_dwordx4 v18, s[44:47], s60 offen offset:0 nt lds
buffer_load_dwordx4 v19, s[44:47], s60 offen offset:1024 nt lds
s_add_u32 s60, s60, 229376
s_add_u32 m0, s52, 24576
s_nop 0
buffer_load_dwordx4 v18, s[44:47], s60 offen offset:0 nt lds
buffer_load_dwordx4 v19, s[44:47], s60 offen offset:1024 nt lds
s_add_u32 s60, s60, 229376
s_add_u32 m0, s52, 32768
s_nop 0
buffer_load_dwordx4 v18, s[44:47], s60 offen offset:0 nt lds
buffer_load_dwordx4 v19, s[44:47], s60 offen offset:1024 nt lds
s_add_u32 s60, s60, 229376
s_add_u32 m0, s52, 40960
s_nop 0
buffer_load_dwordx4 v18, s[44:47], s60 offen offset:0 nt lds
buffer_load_dwordx4 v19, s[44:47], s60 offen offset:1024 nt lds
s_add_u32 s60, s60, 229376
s_add_u32 m0, s52, 49152
s_nop 0
buffer_load_dwordx4 v18, s[44:47], s60 offen offset:0 nt lds
buffer_load_dwordx4 v19, s[44:47], s60 offen offset:1024 nt lds
s_add_u32 s60, s60, 229376
s_add_u32 m0, s52, 57344
s_nop 0
buffer_load_dwordx4 v18, s[44:47], s60 offen offset:0 nt lds
buffer_load_dwordx4 v19, s[44:47], s60 offen offset:1024 nt lds
s_add_u32 s60, s60, 229376
s_waitcnt vmcnt(26)
RB 134,82,32768
RB 138,83,32768
RB 142,82,40960
RB 146,83,40960
v_lshlrev_b32 v126, 16, v110
v_and_b32 v127, s88, v110
v_lshlrev_b32 v118, 16, v94
v_and_b32 v119, s88, v94
v_lshlrev_b32 v128, 16, v111
v_and_b32 v129, s88, v111
v_lshlrev_b32 v120, 16, v95
v_and_b32 v121, s88, v95
v_lshlrev_b32 v130, 16, v112
v_and_b32 v131, s88, v112
v_lshlrev_b32 v122, 16, v96
v_and_b32 v123, s88, v96
v_lshlrev_b32 v132, 16, v113
v_and_b32 v133, s88, v113
v_lshlrev_b32 v124, 16, v97
v_and_b32 v125, s88, v97
WL 2
v_cvt_f32_i32 v134, v134
v_cvt_f32_i32 v135, v135
v_cvt_f32_i32 v136, v136
v_cvt_f32_i32 v137, v137
v_cvt_f32_i32 v138, v138
v_cvt_f32_i32 v139, v139
v_cvt_f32_i32 v140, v140
v_cvt_f32_i32 v141, v141
v_cvt_pk_bf16_f32 v166, v134, v135
v_cvt_pk_bf16_f32 v167, v136, v137
v_cvt_pk_bf16_f32 v168, v138, v139
v_cvt_pk_bf16_f32 v169, v140, v141
v_lshlrev_b32 v134, 16, v166
v_and_b32 v135, s88, v166
v_lshlrev_b32 v136, 16, v167
v_and_b32 v137, s88, v167
v_lshlrev_b32 v138, 16, v168
v_and_b32 v139, s88, v168
v_lshlrev_b32 v140, 16, v169
v_and_b32 v141, s88, v169
v_pk_add_f32 v[134:135], v[134:135], v[126:127] neg_lo:[0,1] neg_hi:[0,1]
v_pk_add_f32 v[136:137], v[136:137], v[128:129] neg_lo:[0,1] neg_hi:[0,1]
v_pk_add_f32 v[138:139], v[138:139], v[130:131] neg_lo:[0,1] neg_hi:[0,1]
v_pk_add_f32 v[140:141], v[140:141], v[132:133] neg_lo:[0,1] neg_hi:[0,1]
v_cvt_pk_bf16_f32 v166, v134, v135
v_cvt_pk_bf16_f32 v167, v136, v137
v_cvt_pk_bf16_f32 v168, v138, v139
v_cvt_pk_bf16_f32 v169, v140, v141
v_lshlrev_b32 v134, 16, v166
v_and_b32 v135, s88, v166
v_lshlrev_b32 v136, 16, v167
v_and_b32 v137, s88, v167
v_lshlrev_b32 v138, 16, v168
v_and_b32 v139, s88, v168
v_lshlrev_b32 v140, 16, v169
v_and_b32 v141, s88, v169
v_pk_mul_f32 v[134:135], v[134:135], v[118:119]
v_pk_mul_f32 v[136:137], v[136:137], v[120:121]
v_pk_mul_f32 v[138:139], v[138:139], v[122:123]
v_pk_mul_f32 v[140:141], v[140:141], v[124:125]
v_cvt_pk_bf16_f32 v190, v134, v135
v_cvt_pk_bf16_f32 v191, v136, v137
v_cvt_pk_bf16_f32 v192, v138, v139
v_cvt_pk_bf16_f32 v193, v140, v141
buffer_store_dwordx4 v[190:193], v3, s[40:43], s87 offen offset:0 sc1
WL 0
v_cvt_f32_i32 v142, v142
v_cvt_f32_i32 v143, v143
v_cvt_f32_i32 v144, v144
v_cvt_f32_i32 v145, v145
v_cvt_f32_i32 v146, v146
v_cvt_f32_i32 v147, v147
v_cvt_f32_i32 v148, v148
v_cvt_f32_i32 v149, v149
v_cvt_pk_bf16_f32 v170, v142, v143
v_cvt_pk_bf16_f32 v171, v144, v145
v_cvt_pk_bf16_f32 v172, v146, v147
v_cvt_pk_bf16_f32 v173, v148, v149
v_lshlrev_b32 v142, 16, v170
v_and_b32 v143, s88, v170
v_lshlrev_b32 v144, 16, v171
v_and_b32 v145, s88, v171
v_lshlrev_b32 v146, 16, v172
v_and_b32 v147, s88, v172
v_lshlrev_b32 v148, 16, v173
v_and_b32 v149, s88, v173
v_pk_add_f32 v[142:143], v[142:143], v[126:127] neg_lo:[0,1] neg_hi:[0,1]
v_pk_add_f32 v[144:145], v[144:145], v[128:129] neg_lo:[0,1] neg_hi:[0,1]
v_pk_add_f32 v[146:147], v[146:147], v[130:131] neg_lo:[0,1] neg_hi:[0,1]
v_pk_add_f32 v[148:149], v[148:149], v[132:133] neg_lo:[0,1] neg_hi:[0,1]
v_cvt_pk_bf16_f32 v170, v142, v143
v_cvt_pk_bf16_f32 v171, v144, v145
v_cvt_pk_bf16_f32 v172, v146, v147
v_cvt_pk_bf16_f32 v173, v148, v149
v_lshlrev_b32 v142, 16, v170
v_and_b32 v143, s88, v170
v_lshlrev_b32 v144, 16, v171
v_and_b32 v145, s88, v171
v_lshlrev_b32 v146, 16, v172
v_and_b32 v147, s88, v172
v_lshlrev_b32 v148, 16, v173
v_and_b32 v149, s88, v173
v_pk_mul_f32 v[142:143], v[142:143], v[118:119]
v_pk_mul_f32 v[144:145], v[144:145], v[120:121]
v_pk_mul_f32 v[146:147], v[146:147], v[122:123]
v_pk_mul_f32 v[148:149], v[148:149], v[124:125]
v_cvt_pk_bf16_f32 v194, v142, v143
v_cvt_pk_bf16_f32 v195, v144, v145
v_cvt_pk_bf16_f32 v196, v146, v147
v_cvt_pk_bf16_f32 v197, v148, v149
buffer_store_dwordx4 v[194:197], v3, s[40:43], s87 offen offset:1024 sc1
s_add_u32 s87, s87, 57344
s_lshl_b32 s30, %1, 8
s_add_u32 s30, s30, 0x1010000
s_lshl_b32 s31, s26, 8
s_add_u32 s31, s31, 0x1002000
v_lshlrev_b32 v50, 7, v1
v_add_u32 v50, s30, v50
v_writelane_b32 v50, s31, 2
s_and_b32 s30, %1, 15
s_lshl_b32 s30, s30, 8
s_add_u32 s30, s30, 0x1001000
v_writelane_b32 v50, s30, 3
s_mov_b64 exec, 15
buffer_load_dword v50, v50, s[92:95], 0 offen sc1
s_mov_b64 exec, -1
buffer_load_dwordx2 v[44:45], v47, s[92:95], 0 offen sc1
s_waitcnt vmcnt(24)
RB 150,82,49152
RB 154,83,49152
RB 158,82,57344
)"
R"(RB 162,83,57344
v_lshlrev_b32 v126, 16, v114
v_and_b32 v127, s88, v114
v_lshlrev_b32 v118, 16, v98
v_and_b32 v119, s88, v98
v_lshlrev_b32 v128, 16, v115
v_and_b32 v129, s88, v115
v_lshlrev_b32 v120, 16, v99
v_and_b32 v121, s88, v99
v_lshlrev_b32 v130, 16, v116
v_and_b32 v131, s88, v116
v_lshlrev_b32 v122, 16, v100
v_and_b32 v123, s88, v100
v_lshlrev_b32 v132, 16, v117
v_and_b32 v133, s88, v117
v_lshlrev_b32 v124, 16, v101
v_and_b32 v125, s88, v101
WL 2
v_cvt_f32_i32 v150, v150
v_cvt_f32_i32 v151, v151
v_cvt_f32_i32 v152, v152
v_cvt_f32_i32 v153, v153
v_cvt_f32_i32 v154, v154
v_cvt_f32_i32 v155, v155
v_cvt_f32_i32 v156, v156
v_cvt_f32_i32 v157, v157
v_cvt_pk_bf16_f32 v166, v150, v151
v_cvt_pk_bf16_f32 v167, v152, v153
v_cvt_pk_bf16_f32 v168, v154, v155
v_cvt_pk_bf16_f32 v169, v156, v157
v_lshlrev_b32 v150, 16, v166
v_and_b32 v151, s88, v166
v_lshlrev_b32 v152, 16, v167
v_and_b32 v153, s88, v167
v_lshlrev_b32 v154, 16, v168
v_and_b32 v155, s88, v168
v_lshlrev_b32 v156, 16, v169
v_and_b32 v157, s88, v169
v_pk_add_f32 v[150:151], v[150:151], v[126:127] neg_lo:[0,1] neg_hi:[0,1]
v_pk_add_f32 v[152:153], v[152:153], v[128:129] neg_lo:[0,1] neg_hi:[0,1]
v_pk_add_f32 v[154:155], v[154:155], v[130:131] neg_lo:[0,1] neg_hi:[0,1]
v_pk_add_f32 v[156:157], v[156:157], v[132:133] neg_lo:[0,1] neg_hi:[0,1]
v_cvt_pk_bf16_f32 v166, v150, v151
v_cvt_pk_bf16_f32 v167, v152, v153
v_cvt_pk_bf16_f32 v168, v154, v155
v_cvt_pk_bf16_f32 v169, v156, v157
v_lshlrev_b32 v150, 16, v166
v_and_b32 v151, s88, v166
v_lshlrev_b32 v152, 16, v167
v_and_b32 v153, s88, v167
v_lshlrev_b32 v154, 16, v168
v_and_b32 v155, s88, v168
v_lshlrev_b32 v156, 16, v169
v_and_b32 v157, s88, v169
v_pk_mul_f32 v[150:151], v[150:151], v[118:119]
v_pk_mul_f32 v[152:153], v[152:153], v[120:121]
v_pk_mul_f32 v[154:155], v[154:155], v[122:123]
v_pk_mul_f32 v[156:157], v[156:157], v[124:125]
v_cvt_pk_bf16_f32 v198, v150, v151
v_cvt_pk_bf16_f32 v199, v152, v153
v_cvt_pk_bf16_f32 v200, v154, v155
v_cvt_pk_bf16_f32 v201, v156, v157
buffer_store_dwordx4 v[198:201], v3, s[40:43], s87 offen offset:0 sc1
WL 0
v_cvt_f32_i32 v158, v158
v_cvt_f32_i32 v159, v159
v_cvt_f32_i32 v160, v160
v_cvt_f32_i32 v161, v161
v_cvt_f32_i32 v162, v162
v_cvt_f32_i32 v163, v163
v_cvt_f32_i32 v164, v164
v_cvt_f32_i32 v165, v165
v_cvt_pk_bf16_f32 v170, v158, v159
v_cvt_pk_bf16_f32 v171, v160, v161
v_cvt_pk_bf16_f32 v172, v162, v163
v_cvt_pk_bf16_f32 v173, v164, v165
v_lshlrev_b32 v158, 16, v170
v_and_b32 v159, s88, v170
v_lshlrev_b32 v160, 16, v171
v_and_b32 v161, s88, v171
v_lshlrev_b32 v162, 16, v172
v_and_b32 v163, s88, v172
v_lshlrev_b32 v164, 16, v173
v_and_b32 v165, s88, v173
v_pk_add_f32 v[158:159], v[158:159], v[126:127] neg_lo:[0,1] neg_hi:[0,1]
v_pk_add_f32 v[160:161], v[160:161], v[128:129] neg_lo:[0,1] neg_hi:[0,1]
v_pk_add_f32 v[162:163], v[162:163], v[130:131] neg_lo:[0,1] neg_hi:[0,1]
v_pk_add_f32 v[164:165], v[164:165], v[132:133] neg_lo:[0,1] neg_hi:[0,1]
v_cvt_pk_bf16_f32 v170, v158, v159
v_cvt_pk_bf16_f32 v171, v160, v161
v_cvt_pk_bf16_f32 v172, v162, v163
v_cvt_pk_bf16_f32 v173, v164, v165
v_lshlrev_b32 v158, 16, v170
v_and_b32 v159, s88, v170
v_lshlrev_b32 v160, 16, v171
v_and_b32 v161, s88, v171
v_lshlrev_b32 v162, 16, v172
v_and_b32 v163, s88, v172
v_lshlrev_b32 v164, 16, v173
v_and_b32 v165, s88, v173
v_pk_mul_f32 v[158:159], v[158:159], v[118:119]
v_pk_mul_f32 v[160:161], v[160:161], v[120:121]
v_pk_mul_f32 v[162:163], v[162:163], v[122:123]
v_pk_mul_f32 v[164:165], v[164:165], v[124:125]
v_cvt_pk_bf16_f32 v202, v158, v159
v_cvt_pk_bf16_f32 v203, v160, v161
v_cvt_pk_bf16_f32 v204, v162, v163
v_cvt_pk_bf16_f32 v205, v164, v165
buffer_store_dwordx4 v[202:205], v3, s[40:43], s87 offen offset:1024 sc1
s_waitcnt vmcnt(2)
v_cmp_ne_u64 vcc, s[98:99], v[44:45]
s_cmp_eq_u64 vcc, 0
s_cbranch_scc0 L_slowa_%=
L_backa_%=:
buffer_load_dwordx4 a[32:35], v16, s[40:43], s63 offen offset:0 sc1
buffer_load_dwordx4 a[36:39], v16, s[40:43], s63 offen offset:1024 sc1
s_add_u32 s63, s63, 4096
buffer_load_dwordx4 a[40:43], v17, s[40:43], s86 offen offset:0 sc1
buffer_load_dwordx4 a[44:47], v17, s[40:43], s86 offen offset:1024 sc1
s_add_u32 s86, s86, 4096
buffer_load_dwordx4 a[48:51], v16, s[40:43], s63 offen offset:0 sc1
buffer_load_dwordx4 a[52:55], v16, s[40:43], s63 offen offset:1024 sc1
s_add_u32 s63, s63, 4096
buffer_load_dwordx4 a[56:59], v17, s[40:43], s86 offen offset:0 sc1
buffer_load_dwordx4 a[60:63], v17, s[40:43], s86 offen offset:1024 sc1
s_add_u32 s86, s86, 4096
buffer_load_dwordx4 a[64:67], v16, s[40:43], s63 offen offset:0 sc1
buffer_load_dwordx4 a[68:71], v16, s[40:43], s63 offen offset:1024 sc1
s_add_u32 s63, s63, 4096
buffer_load_dwordx4 a[72:75], v17, s[40:43], s86 offen offset:0 sc1
buffer_load_dwordx4 a[76:79], v17, s[40:43], s86 offen offset:1024 sc1
s_add_u32 s86, s86, 4096
buffer_load_dwordx4 a[80:83], v16, s[40:43], s63 offen offset:0 sc1
buffer_load_dwordx4 a[84:87], v16, s[40:43], s63 offen offset:1024 sc1
s_add_u32 s63, s63, 4096
buffer_load_dwordx4 a[88:91], v17, s[40:43], s86 offen offset:0 sc1
buffer_load_dwordx4 a[92:95], v17, s[40:43], s86 offen offset:1024 sc1
s_add_u32 s86, s86, 4096
s_xor_b32 s53, %1, 1
s_lshl_b32 s53, s53, 8
s_add_u32 s53, s53, 0x1010000
s_lshl_b32 s30, s7, 6
s_add_u32 s53, s53, s30
v_mov_b32 v46, s53
s_barrier
RT 54,20,0
RT 56,20,1024
RT 58,21,0
RT 60,21,1024
RT 62,22,0
RT 64,22,1024
s_cmp_eq_u32 s25, 0
s_cbranch_scc1 L_cls0_%=
s_cmp_eq_u32 s25, 2
s_cbranch_scc1 L_cls2_%=
s_cmp_eq_u32 s25, 3
s_cbranch_scc1 L_cls3_%=
L_cls1_%=:
s_waitcnt vmcnt(12) lgkmcnt(4)
v_mov_b32 v206, s98
v_mov_b32 v207, s99
s_mov_b64 exec, 1
buffer_store_dwordx2 v[206:207], off, s[92:95], s89 offset:256 sc1
s_mov_b64 exec, -1
v_mfma_f32_16x16x32_bf16 v[78:81], a[32:35], v[54:57], 0
RT 66,23,0
v_mfma_f32_16x16x32_bf16 v[82:85], a[36:39], v[54:57], 0
RT 68,23,1024
v_mfma_f32_16x16x32_bf16 v[142:145], a[40:43], v[54:57], 0
buffer_load_dwordx4 a[96:99], v16, s[40:43], s63 offen offset:0 sc1
v_mfma_f32_16x16x32_bf16 v[146:149], a[44:47], v[54:57], 0
WL 4
v_mfma_f32_16x16x32_bf16 v[86:89], a[32:35], v[58:61], 0
RT 70,24,0
v_mfma_f32_16x16x32_bf16 v[90:93], a[36:39], v[58:61], 0
RT 72,24,1024
v_mfma_f32_16x16x32_bf16 v[150:153], a[40:43], v[58:61], 0
buffer_load_dwordx4 a[100:103], v16, s[40:43], s63 offen offset:1024 sc1
v_mfma_f32_16x16x32_bf16 v[154:157], a[44:47], v[58:61], 0
s_add_u32 s63, s63, 4096
WL 4
v_mfma_f32_16x16x32_bf16 v[94:97], a[32:35], v[62:65], 0
RT 74,25,0
v_mfma_f32_16x16x32_bf16 v[98:101], a[36:39], v[62:65], 0
RT 76,25,1024
v_mfma_f32_16x16x32_bf16 v[158:161], a[40:43], v[62:65], 0
buffer_load_dwordx4 a[104:107], v17, s[40:43], s86 offen offset:0 sc1
v_mfma_f32_16x16x32_bf16 v[162:165], a[44:47], v[62:65], 0
WL 4
v_mfma_f32_16x16x32_bf16 v[102:105], a[32:35], v[66:69], 0
RT 54,26,0
v_mfma_f32_16x16x32_bf16 v[106:109], a[36:39], v[66:69], 0
RT 56,26,1024
v_mfma_f32_16x16x32_bf16 v[166:169], a[40:43], v[66:69], 0
buffer_load_dwordx4 a[108:111], v17, s[40:43], s86 offen offset:1024 sc1
v_mfma_f32_16x16x32_bf16 v[170:173], a[44:47], v[66:69], 0
s_add_u32 s86, s86, 4096
WL 4
v_mfma_f32_16x16x32_bf16 v[110:113], a[32:35], v[70:73], 0
RT 58,27,0
v_mfma_f32_16x16x32_bf16 v[114:117], a[36:39], v[70:73], 0
RT 60,27,1024
v_mfma_f32_16x16x32_bf16 v[174:177], a[40:43], v[70:73], 0
buffer_load_dwordx4 a[112:115], v16, s[40:43], s63 offen offset:0 sc1
v_mfma_f32_16x16x32_bf16 v[178:181], a[44:47], v[70:73], 0
WL 4
v_mfma_f32_16x16x32_bf16 v[118:121], a[32:35], v[74:77], 0
RT 62,20,8192
v_mfma_f32_16x16x32_bf16 v[122:125], a[36:39], v[74:77], 0
RT 64,20,9216
v_mfma_f32_16x16x32_bf16 v[182:185], a[40:43], v[74:77], 0
buffer_load_dwordx4 a[116:119], v16, s[40:43], s63 offen offset:1024 sc1
v_mfma_f32_16x16x32_bf16 v[186:189], a[44:47], v[74:77], 0
s_add_u32 s63, s63, 4096
WL 4
v_mfma_f32_16x16x32_bf16 v[126:129], a[32:35], v[54:57], 0
RT 66,21,8192
v_mfma_f32_16x16x32_bf16 v[130:133], a[36:39], v[54:57], 0
RT 68,21,9216
v_mfma_f32_16x16x32_bf16 v[190:193], a[40:43], v[54:57], 0
buffer_load_dwordx4 a[120:123], v17, s[40:43], s86 offen offset:0 sc1
v_mfma_f32_16x16x32_bf16 v[194:197], a[44:47], v[54:57], 0
WL 4
v_mfma_f32_16x16x32_bf16 v[134:137], a[32:35], v[58:61], 0
RT 70,22,8192
v_mfma_f32_16x16x32_bf16 v[138:141], a[36:39], v[58:61], 0
RT 72,22,9216
v_mfma_f32_16x16x32_bf16 v[198:201], a[40:43], v[58:61], 0
buffer_load_dwordx4 a[124:127], v17, s[40:43], s86 offen offset:1024 sc1
v_mfma_f32_16x16x32_bf16 v[202:205], a[44:47], v[58:61], 0
s_add_u32 s86, s86, 4096
s_waitcnt vmcnt(17) lgkmcnt(4)
MA 78,48,62
RT 74,23,8192
MA 82,52,62
RT 76,23,9216
MA 142,56,62
buffer_load_dword v50, v49, s[44:47], s58 offen
MA 146,60,62
s_add_u32 s58, s58, 3670016
s_cmp_ge_u32 s58, s61
s_cselect_b32 s58, 0x40000000, s58
WL 4
MA 86,48,66
RT 54,24,8192
MA 90,52,66
RT 56,24,9216
MA 150,56,66
MA 154,60,66
WL 4
MA 94,48,70
RT 58,25,8192
MA 98,52,70
RT 60,25,9216
MA 158,56,70
MA 162,60,70
WL 4
MA 102,48,74
RT 62,26,8192
MA 106,52,74
RT 64,26,9216
MA 166,56,74
MA 170,60,74
WL 4
MA 110,48,54
RT 66,27,8192
MA 114,52,54
RT 68,27,9216
MA 174,56,54
MA 178,60,54
WL 4
MA 118,48,58
s_barrier
RT 70,20,16384
MA 122,52,58
RT 72,20,17408
MA 182,56,58
MA 186,60,58
WL 4
MA 126,48,62
RT 74,21,16384
MA 130,52,62
RT 76,21,17408
MA 190,56,62
MA 194,60,62
WL 4
MA 134,48,66
RT 54,22,16384
MA 138,52,66
RT 56,22,17408
MA 198,56,66
MA 202,60,66
s_waitcnt vmcnt(14) lgkmcnt(4)
MA 78,64,70
RT 58,23,16384
MA 82,68,70
RT 60,23,17408
MA 142,72,70
buffer_load_dwordx4 a[128:131], v16, s[40:43], s63 offen offset:0 sc1
MA 146,76,70
WL 4
MA 86,64,74
RT 62,24,16384
MA 90,68,74
RT 64,24,17408
MA 150,72,74
buffer_load_dwordx4 a[132:135], v16, s[40:43], s63 offen offset:1024 sc1
MA 154,76,74
s_add_u32 s63, s63, 4096
WL 4
MA 94,64,54
RT 66,25,16384
MA 98,68,54
RT 68,25,17408
MA 158,72,54
buffer_load_dwordx4 a[136:139], v17, s[40:43], s86 offen offset:0 sc1
MA 162,76,54
WL 4
MA 102,64,58
RT 70,26,16384
MA 106,68,58
RT 72,26,17408
MA 166,72,58
buffer_load_dwordx4 a[140:143], v17, s[40:43], s86 offen offset:1024 sc1
MA 170,76,58
s_add_u32 s86, s86, 4096
WL 4
MA 110,64,62
RT 74,27,16384
MA 114,68,62
RT 76,27,17408
MA 174,72,62
buffer_load_dwordx4 a[144:147], v16, s[40:43], s63 offen offset:0 sc1
MA 178,76,62
WL 4
MA 118,64,66
RT 54,20,24576
MA 122,68,66
RT 56,20,25600
MA 182,72,66
buffer_load_dwordx4 a[148:151], v16, s[40:43], s63 offen offset:1024 sc1
MA 186,76,66
s_add_u32 s63, s63, 4096
WL 4
MA 126,64,70
RT 58,21,24576
MA 130,68,70
RT 60,21,25600
MA 190,72,70
buffer_load_dwordx4 a[152:155], v17, s[40:43], s86 offen offset:0 sc1
MA 194,76,70
WL 4
MA 134,64,74
RT 62,22,24576
MA 138,68,74
RT 64,22,25600
MA 198,72,74
buffer_load_dwordx4 a[156:159], v17, s[40:43], s86 offen offset:1024 sc1
MA 202,76,74
s_add_u32 s86, s86, 4096
buffer_load_dwordx2 v[44:45], v47, s[92:95], 0 offen offset:256 sc1
s_waitcnt vmcnt(19) lgkmcnt(4)
MA 78,80,54
RT 66,23,24576
MA 82,84,54
RT 68,23,25600
s_add_u32 m0, s52, 65536
MA 142,88,54
buffer_load_dwordx4 v18, s[44:47], s60 offen offset:0 lds
MA 146,92,54
WL 4
MA 86,80,58
RT 70,24,24576
MA 90,84,58
RT 72,24,25600
MA 150,88,58
buffer_load_dwordx4 v19, s[44:47], s60 offen offset:1024 lds
MA 154,92,58
s_add_u32 s60, s60, 229376
WL 4
MA 94,80,62
RT 74,25,24576
MA 98,84,62
RT 76,25,25600
MA 158,88,62
buffer_load_dwordx4 a[160:163], v16, s[40:43], s63 offen offset:0 sc1
MA 162,92,62
WL 4
MA 102,80,66
RT 54,26,24576
MA 106,84,66
RT 56,26,25600
MA 166,88,66
buffer_load_dwordx4 a[164:167], v16, s[40:43], s63 offen offset:1024 sc1
MA 170,92,66
s_add_u32 s63, s63, 4096
WL 4
MA 110,80,70
RT 58,27,24576
MA 114,84,70
)"
R"(RT 60,27,25600
MA 174,88,70
buffer_load_dwordx4 a[168:171], v17, s[40:43], s86 offen offset:0 sc1
MA 178,92,70
WL 4
MA 118,80,74
s_barrier
RT 62,20,32768
MA 122,84,74
RT 64,20,33792
MA 182,88,74
buffer_load_dwordx4 a[172:175], v17, s[40:43], s86 offen offset:1024 sc1
MA 186,92,74
s_add_u32 s86, s86, 4096
WL 4
MA 126,80,54
RT 66,21,32768
MA 130,84,54
RT 68,21,33792
s_add_u32 m0, s52, 73728
MA 190,88,54
buffer_load_dwordx4 v18, s[44:47], s60 offen offset:0 lds
MA 194,92,54
WL 4
MA 134,80,58
RT 70,22,32768
MA 138,84,58
RT 72,22,33792
MA 198,88,58
buffer_load_dwordx4 v19, s[44:47], s60 offen offset:1024 lds
MA 202,92,58
s_add_u32 s60, s60, 229376
s_waitcnt vmcnt(22) lgkmcnt(4)
MA 78,96,62
RT 74,23,32768
MA 82,100,62
RT 76,23,33792
s_add_u32 m0, s52, 81920
MA 142,104,62
buffer_load_dwordx4 v18, s[44:47], s60 offen offset:0 lds
MA 146,108,62
WL 4
MA 86,96,66
RT 54,24,32768
MA 90,100,66
RT 56,24,33792
MA 150,104,66
buffer_load_dwordx4 v19, s[44:47], s60 offen offset:1024 lds
MA 154,108,66
s_add_u32 s60, s60, 229376
WL 4
MA 94,96,70
RT 58,25,32768
MA 98,100,70
RT 60,25,33792
MA 158,104,70
buffer_load_dwordx4 a[176:179], v16, s[40:43], s63 offen offset:0 sc1
MA 162,108,70
WL 4
MA 102,96,74
RT 62,26,32768
MA 106,100,74
RT 64,26,33792
MA 166,104,74
buffer_load_dwordx4 a[180:183], v16, s[40:43], s63 offen offset:1024 sc1
MA 170,108,74
s_add_u32 s63, s63, 4096
WL 4
MA 110,96,54
RT 66,27,32768
MA 114,100,54
RT 68,27,33792
MA 174,104,54
buffer_load_dwordx4 a[184:187], v17, s[40:43], s86 offen offset:0 sc1
MA 178,108,54
WL 4
MA 118,96,58
RT 70,20,40960
MA 122,100,58
RT 72,20,41984
MA 182,104,58
buffer_load_dwordx4 a[188:191], v17, s[40:43], s86 offen offset:1024 sc1
MA 186,108,58
s_add_u32 s86, s86, 4096
WL 4
MA 126,96,62
RT 74,21,40960
MA 130,100,62
RT 76,21,41984
s_add_u32 m0, s52, 90112
MA 190,104,62
buffer_load_dwordx4 v18, s[44:47], s60 offen offset:0 lds
MA 194,108,62
WL 4
MA 134,96,66
RT 54,22,40960
MA 138,100,66
RT 56,22,41984
MA 198,104,66
buffer_load_dwordx4 v19, s[44:47], s60 offen offset:1024 lds
MA 202,108,66
s_add_u32 s60, s60, 229376
s_waitcnt vmcnt(26) lgkmcnt(4)
MA 78,112,70
RT 58,23,40960
MA 82,116,70
RT 60,23,41984
MA 142,120,70
buffer_load_dwordx4 a[32:35], v16, s[40:43], s63 offen offset:0 sc1
MA 146,124,70
WL 4
MA 86,112,74
RT 62,24,40960
MA 90,116,74
RT 64,24,41984
MA 150,120,74
buffer_load_dwordx4 a[36:39], v16, s[40:43], s63 offen offset:1024 sc1
MA 154,124,74
s_add_u32 s63, s63, 4096
WL 4
MA 94,112,54
RT 66,25,40960
MA 98,116,54
RT 68,25,41984
MA 158,120,54
buffer_load_dwordx4 a[40:43], v17, s[40:43], s86 offen offset:0 sc1
MA 162,124,54
WL 4
MA 102,112,58
RT 70,26,40960
MA 106,116,58
RT 72,26,41984
MA 166,120,58
buffer_load_dwordx4 a[44:47], v17, s[40:43], s86 offen offset:1024 sc1
MA 170,124,58
s_add_u32 s86, s86, 4096
WL 4
MA 110,112,62
RT 74,27,40960
MA 114,116,62
RT 76,27,41984
s_add_u32 m0, s52, 98304
MA 174,120,62
buffer_load_dwordx4 v18, s[44:47], s60 offen offset:0 lds
MA 178,124,62
WL 4
MA 118,112,66
s_barrier
RT 54,20,49152
MA 122,116,66
RT 56,20,50176
MA 182,120,66
buffer_load_dwordx4 v19, s[44:47], s60 offen offset:1024 lds
MA 186,124,66
s_add_u32 s60, s60, 229376
WL 4
MA 126,112,70
RT 58,21,49152
MA 130,116,70
RT 60,21,50176
MA 190,120,70
buffer_load_dwordx4 a[48:51], v16, s[40:43], s63 offen offset:0 sc1
MA 194,124,70
WL 4
MA 134,112,74
RT 62,22,49152
MA 138,116,74
RT 64,22,50176
MA 198,120,74
buffer_load_dwordx4 a[52:55], v16, s[40:43], s63 offen offset:1024 sc1
MA 202,124,74
s_add_u32 s63, s63, 4096
s_waitcnt vmcnt(29) lgkmcnt(4)
MA 78,128,54
RT 66,23,49152
MA 82,132,54
RT 68,23,50176
MA 142,136,54
buffer_load_dwordx4 a[56:59], v17, s[40:43], s86 offen offset:0 sc1
MA 146,140,54
WL 4
MA 86,128,58
RT 70,24,49152
MA 90,132,58
RT 72,24,50176
MA 150,136,58
buffer_load_dwordx4 a[60:63], v17, s[40:43], s86 offen offset:1024 sc1
MA 154,140,58
s_add_u32 s86, s86, 4096
WL 4
MA 94,128,62
RT 74,25,49152
MA 98,132,62
RT 76,25,50176
s_add_u32 m0, s52, 106496
MA 158,136,62
buffer_load_dwordx4 v18, s[44:47], s60 offen offset:0 lds
MA 162,140,62
WL 4
MA 102,128,66
RT 54,26,49152
MA 106,132,66
RT 56,26,50176
MA 166,136,66
buffer_load_dwordx4 v19, s[44:47], s60 offen offset:1024 lds
MA 170,140,66
s_add_u32 s60, s60, 229376
WL 4
MA 110,128,70
RT 58,27,49152
MA 114,132,70
RT 60,27,50176
MA 174,136,70
buffer_load_dwordx4 a[64:67], v16, s[40:43], s63 offen offset:0 sc1
MA 178,140,70
WL 4
MA 118,128,74
RT 62,20,57344
MA 122,132,74
RT 64,20,58368
MA 182,136,74
buffer_load_dwordx4 a[68:71], v16, s[40:43], s63 offen offset:1024 sc1
MA 186,140,74
s_add_u32 s63, s63, 4096
WL 4
MA 126,128,54
RT 66,21,57344
MA 130,132,54
RT 68,21,58368
MA 190,136,54
buffer_load_dwordx4 a[72:75], v17, s[40:43], s86 offen offset:0 sc1
MA 194,140,54
WL 4
MA 134,128,58
RT 70,22,57344
MA 138,132,58
RT 72,22,58368
MA 198,136,58
buffer_load_dwordx4 a[76:79], v17, s[40:43], s86 offen offset:1024 sc1
MA 202,140,58
s_add_u32 s86, s86, 4096
s_waitcnt vmcnt(33) lgkmcnt(4)
MA 78,144,62
RT 74,23,57344
MA 82,148,62
RT 76,23,58368
s_add_u32 m0, s52, 114688
MA 142,152,62
buffer_load_dwordx4 v18, s[44:47], s60 offen offset:0 lds
MA 146,156,62
WL 4
MA 86,144,66
RT 54,24,57344
MA 90,148,66
RT 56,24,58368
MA 150,152,66
buffer_load_dwordx4 v19, s[44:47], s60 offen offset:1024 lds
MA 154,156,66
s_add_u32 s60, s60, 229376
WL 4
MA 94,144,70
RT 58,25,57344
MA 98,148,70
RT 60,25,58368
MA 158,152,70
buffer_load_dwordx4 a[80:83], v16, s[40:43], s63 offen offset:0 sc1
MA 162,156,70
WL 4
MA 102,144,74
RT 62,26,57344
MA 106,148,74
RT 64,26,58368
MA 166,152,74
buffer_load_dwordx4 a[84:87], v16, s[40:43], s63 offen offset:1024 sc1
MA 170,156,74
s_add_u32 s63, s63, 4096
WL 4
MA 110,144,54
RT 66,27,57344
MA 114,148,54
RT 68,27,58368
MA 174,152,54
buffer_load_dwordx4 a[88:91], v17, s[40:43], s86 offen offset:0 sc1
MA 178,156,54
WL 4
MA 118,144,58
s_waitcnt vmcnt(29)
s_barrier
RT 70,28,0
MA 122,148,58
RT 72,28,1024
MA 182,152,58
buffer_load_dwordx4 a[92:95], v17, s[40:43], s86 offen offset:1024 sc1
MA 186,156,58
s_add_u32 s86, s86, 4096
WL 4
MA 126,144,62
RT 74,29,0
MA 130,148,62
RT 76,29,1024
s_add_u32 m0, s52, 122880
MA 190,152,62
buffer_load_dwordx4 v18, s[44:47], s60 offen offset:0 lds
MA 194,156,62
WL 4
MA 134,144,66
RT 54,30,0
MA 138,148,66
RT 56,30,1024
MA 198,152,66
buffer_load_dwordx4 v19, s[44:47], s60 offen offset:1024 lds
MA 202,156,66
s_add_u32 s60, s60, 229376
v_cmp_ne_u64 vcc, s[98:99], v[44:45]
s_cmp_eq_u64 vcc, 0
s_cbranch_scc0 L_slowgB1_%=
L_backgB1_%=:
WL 4
MA 78,160,70
RT 58,31,0
MA 82,164,70
RT 60,31,1024
MA 142,168,70
buffer_load_dwordx4 a[96:99], v16, s[40:43], s63 offen offset:0 sc1
MA 146,172,70
WL 4
MA 86,160,74
RT 62,32,0
MA 90,164,74
RT 64,32,1024
MA 150,168,74
buffer_load_dwordx4 a[100:103], v16, s[40:43], s63 offen offset:1024 sc1
MA 154,172,74
s_add_u32 s63, s63, 4096
WL 4
MA 94,160,54
RT 66,33,0
MA 98,164,54
RT 68,33,1024
MA 158,168,54
buffer_load_dwordx4 a[104:107], v17, s[40:43], s86 offen offset:0 sc1
MA 162,172,54
WL 4
MA 102,160,58
RT 70,34,0
MA 106,164,58
RT 72,34,1024
MA 166,168,58
buffer_load_dwordx4 a[108:111], v17, s[40:43], s86 offen offset:1024 sc1
MA 170,172,58
s_add_u32 s86, s86, 4096
WL 4
MA 110,160,62
RT 74,35,0
MA 114,164,62
RT 76,35,1024
MA 174,168,62
MA 178,172,62
WL 4
MA 118,160,66
RT 54,28,8192
MA 122,164,66
RT 56,28,9216
s_add_u32 m0, s52, 131072
MA 182,168,66
buffer_load_dwordx4 v18, s[44:47], s60 offen offset:0 lds
MA 186,172,66
WL 4
MA 126,160,70
RT 58,29,8192
MA 130,164,70
RT 60,29,9216
MA 190,168,70
buffer_load_dwordx4 v19, s[44:47], s60 offen offset:1024 lds
MA 194,172,70
s_add_u32 s60, s60, 229376
WL 4
MA 134,160,74
RT 62,30,8192
MA 138,164,74
RT 64,30,9216
MA 198,168,74
MA 202,172,74
s_waitcnt vmcnt(32) lgkmcnt(4)
MA 78,176,54
RT 66,31,8192
MA 82,180,54
RT 68,31,9216
MA 142,184,54
buffer_load_dwordx4 a[112:115], v16, s[40:43], s63 offen offset:0 sc1
MA 146,188,54
WL 4
MA 86,176,58
RT 70,32,8192
MA 90,180,58
RT 72,32,9216
MA 150,184,58
buffer_load_dwordx4 a[116:119], v16, s[40:43], s63 offen offset:1024 sc1
MA 154,188,58
s_add_u32 s63, s63, 4096
)"
R"(WL 4
MA 94,176,62
RT 74,33,8192
MA 98,180,62
RT 76,33,9216
MA 158,184,62
buffer_load_dwordx4 a[120:123], v17, s[40:43], s86 offen offset:0 sc1
MA 162,188,62
WL 4
MA 102,176,66
RT 54,34,8192
MA 106,180,66
RT 56,34,9216
MA 166,184,66
buffer_load_dwordx4 a[124:127], v17, s[40:43], s86 offen offset:1024 sc1
MA 170,188,66
s_add_u32 s86, s86, 4096
WL 4
MA 110,176,70
RT 58,35,8192
MA 114,180,70
RT 60,35,9216
MA 174,184,70
MA 178,188,70
WL 4
MA 118,176,74
s_waitcnt vmcnt(34)
s_barrier
RT 62,28,16384
MA 122,180,74
RT 64,28,17408
s_add_u32 m0, s52, 139264
MA 182,184,74
buffer_load_dwordx4 v18, s[44:47], s60 offen offset:0 lds
MA 186,188,74
WL 4
MA 126,176,54
RT 66,29,16384
MA 130,180,54
RT 68,29,17408
MA 190,184,54
buffer_load_dwordx4 v19, s[44:47], s60 offen offset:1024 lds
MA 194,188,54
s_add_u32 s60, s60, 229376
WL 4
MA 134,176,58
RT 70,30,16384
MA 138,180,58
RT 72,30,17408
MA 198,184,58
MA 202,188,58
s_waitcnt vmcnt(32) lgkmcnt(4)
MA 78,32,62
RT 74,31,16384
MA 82,36,62
RT 76,31,17408
MA 142,40,62
buffer_load_dwordx4 a[128:131], v16, s[40:43], s63 offen offset:0 sc1
MA 146,44,62
WL 4
MA 86,32,66
RT 54,32,16384
MA 90,36,66
RT 56,32,17408
MA 150,40,66
buffer_load_dwordx4 a[132:135], v16, s[40:43], s63 offen offset:1024 sc1
MA 154,44,66
s_add_u32 s63, s63, 4096
WL 4
MA 94,32,70
RT 58,33,16384
MA 98,36,70
RT 60,33,17408
MA 158,40,70
buffer_load_dwordx4 a[136:139], v17, s[40:43], s86 offen offset:0 sc1
MA 162,44,70
WL 4
MA 102,32,74
RT 62,34,16384
MA 106,36,74
RT 64,34,17408
MA 166,40,74
buffer_load_dwordx4 a[140:143], v17, s[40:43], s86 offen offset:1024 sc1
MA 170,44,74
s_add_u32 s86, s86, 4096
WL 4
MA 110,32,54
RT 66,35,16384
MA 114,36,54
RT 68,35,17408
MA 174,40,54
MA 178,44,54
WL 4
MA 118,32,58
RT 70,28,24576
MA 122,36,58
RT 72,28,25600
s_add_u32 m0, s52, 147456
MA 182,40,58
buffer_load_dwordx4 v18, s[44:47], s60 offen offset:0 lds
MA 186,44,58
WL 4
MA 126,32,62
RT 74,29,24576
MA 130,36,62
RT 76,29,25600
MA 190,40,62
buffer_load_dwordx4 v19, s[44:47], s60 offen offset:1024 lds
MA 194,44,62
s_add_u32 s60, s60, 229376
WL 4
MA 134,32,66
RT 54,30,24576
MA 138,36,66
RT 56,30,25600
MA 198,40,66
MA 202,44,66
s_waitcnt vmcnt(32) lgkmcnt(4)
MA 78,48,70
RT 58,31,24576
MA 82,52,70
RT 60,31,25600
MA 142,56,70
buffer_load_dword v50, v49, s[44:47], s58 offen
MA 146,60,70
s_add_u32 s58, s58, 3670016
s_cmp_ge_u32 s58, s61
s_cselect_b32 s58, 0x40000000, s58
WL 4
MA 86,48,74
RT 62,32,24576
MA 90,52,74
RT 64,32,25600
MA 150,56,74
MA 154,60,74
WL 4
MA 94,48,54
RT 66,33,24576
MA 98,52,54
RT 68,33,25600
MA 158,56,54
MA 162,60,54
WL 4
MA 102,48,58
RT 70,34,24576
MA 106,52,58
RT 72,34,25600
MA 166,56,58
MA 170,60,58
WL 4
MA 110,48,62
RT 74,35,24576
MA 114,52,62
RT 76,35,25600
MA 174,56,62
MA 178,60,62
WL 4
MA 118,48,66
s_waitcnt vmcnt(31)
s_barrier
RT 54,28,32768
MA 122,52,66
RT 56,28,33792
MA 182,56,66
MA 186,60,66
WL 4
MA 126,48,70
RT 58,29,32768
MA 130,52,70
RT 60,29,33792
MA 190,56,70
MA 194,60,70
WL 4
MA 134,48,74
RT 62,30,32768
MA 138,52,74
RT 64,30,33792
MA 198,56,74
MA 202,60,74
s_waitcnt vmcnt(27) lgkmcnt(4)
MA 78,64,54
RT 66,31,32768
MA 82,68,54
RT 68,31,33792
MA 142,72,54
MA 146,76,54
WL 4
MA 86,64,58
RT 70,32,32768
MA 90,68,58
RT 72,32,33792
MA 150,72,58
MA 154,76,58
WL 4
MA 94,64,62
RT 74,33,32768
MA 98,68,62
RT 76,33,33792
MA 158,72,62
MA 162,76,62
WL 4
MA 102,64,66
RT 54,34,32768
MA 106,68,66
RT 56,34,33792
MA 166,72,66
MA 170,76,66
WL 4
MA 110,64,70
RT 58,35,32768
MA 114,68,70
RT 60,35,33792
MA 174,72,70
MA 178,76,70
WL 4
MA 118,64,74
RT 62,28,40960
MA 122,68,74
RT 64,28,41984
MA 182,72,74
MA 186,76,74
WL 4
MA 126,64,54
RT 66,29,40960
MA 130,68,54
RT 68,29,41984
MA 190,72,54
MA 194,76,54
WL 4
MA 134,64,58
RT 70,30,40960
MA 138,68,58
RT 72,30,41984
MA 198,72,58
MA 202,76,58
s_waitcnt vmcnt(21) lgkmcnt(4)
MA 78,80,62
RT 74,31,40960
MA 82,84,62
RT 76,31,41984
MA 142,88,62
buffer_load_dwordx4 a[144:147], v16, s[40:43], s63 offen offset:0 sc1
MA 146,92,62
WL 4
MA 86,80,66
RT 54,32,40960
MA 90,84,66
RT 56,32,41984
MA 150,88,66
buffer_load_dwordx4 a[148:151], v16, s[40:43], s63 offen offset:1024 sc1
MA 154,92,66
s_add_u32 s63, s63, 4096
WL 4
MA 94,80,70
RT 58,33,40960
MA 98,84,70
RT 60,33,41984
MA 158,88,70
buffer_load_dwordx4 a[152:155], v17, s[40:43], s86 offen offset:0 sc1
MA 162,92,70
WL 4
MA 102,80,74
RT 62,34,40960
MA 106,84,74
RT 64,34,41984
MA 166,88,74
buffer_load_dwordx4 a[156:159], v17, s[40:43], s86 offen offset:1024 sc1
MA 170,92,74
s_add_u32 s86, s86, 4096
WL 4
MA 110,80,54
RT 66,35,40960
MA 114,84,54
RT 68,35,41984
MA 174,88,54
buffer_load_dwordx4 a[160:163], v16, s[40:43], s63 offen offset:0 sc1
MA 178,92,54
WL 4
MA 118,80,58
s_waitcnt vmcnt(24)
s_barrier
RT 70,28,49152
MA 122,84,58
RT 72,28,50176
MA 182,88,58
buffer_load_dwordx4 a[164:167], v16, s[40:43], s63 offen offset:1024 sc1
MA 186,92,58
s_add_u32 s63, s63, 4096
WL 4
MA 126,80,62
RT 74,29,49152
MA 130,84,62
RT 76,29,50176
MA 190,88,62
buffer_load_dwordx4 a[168:171], v17, s[40:43], s86 offen offset:0 sc1
MA 194,92,62
WL 4
MA 134,80,66
RT 54,30,49152
MA 138,84,66
RT 56,30,50176
MA 198,88,66
buffer_load_dwordx4 a[172:175], v17, s[40:43], s86 offen offset:1024 sc1
MA 202,92,66
s_add_u32 s86, s86, 4096
s_waitcnt vmcnt(23) lgkmcnt(4)
MA 78,96,70
RT 58,31,49152
MA 82,100,70
RT 60,31,50176
s_add_u32 m0, s52, 155648
MA 142,104,70
buffer_load_dwordx4 v18, s[44:47], s60 offen offset:0 lds
MA 146,108,70
WL 4
MA 86,96,74
RT 62,32,49152
MA 90,100,74
RT 64,32,50176
MA 150,104,74
buffer_load_dwordx4 v19, s[44:47], s60 offen offset:1024 lds
MA 154,108,74
s_add_u32 s60, s60, 229376
WL 4
MA 94,96,54
RT 66,33,49152
MA 98,100,54
RT 68,33,50176
MA 158,104,54
buffer_load_dwordx4 a[176:179], v16, s[40:43], s63 offen offset:0 sc1
MA 162,108,54
WL 4
MA 102,96,58
RT 70,34,49152
MA 106,100,58
RT 72,34,50176
MA 166,104,58
buffer_load_dwordx4 a[180:183], v16, s[40:43], s63 offen offset:1024 sc1
MA 170,108,58
s_add_u32 s63, s63, 4096
WL 4
MA 110,96,62
RT 74,35,49152
MA 114,100,62
RT 76,35,50176
MA 174,104,62
buffer_load_dwordx4 a[184:187], v17, s[40:43], s86 offen offset:0 sc1
MA 178,108,62
WL 4
MA 118,96,66
RT 54,28,57344
MA 122,100,66
RT 56,28,58368
MA 182,104,66
buffer_load_dwordx4 a[188:191], v17, s[40:43], s86 offen offset:1024 sc1
MA 186,108,66
s_add_u32 s86, s86, 4096
WL 4
MA 126,96,70
RT 58,29,57344
MA 130,100,70
RT 60,29,58368
s_add_u32 m0, s52, 0
MA 190,104,70
buffer_load_dwordx4 v18, s[44:47], s60 offen offset:0 lds
MA 194,108,70
WL 4
MA 134,96,74
RT 62,30,57344
MA 138,100,74
RT 64,30,58368
MA 198,104,74
buffer_load_dwordx4 v19, s[44:47], s60 offen offset:1024 lds
MA 202,108,74
s_add_u32 s60, s60, 229376
s_waitcnt vmcnt(25) lgkmcnt(4)
MA 78,112,54
RT 66,31,57344
MA 82,116,54
RT 68,31,58368
s_add_u32 m0, s52, 8192
MA 142,120,54
buffer_load_dwordx4 v18, s[44:47], s60 offen offset:0 lds
MA 146,124,54
WL 4
MA 86,112,58
RT 70,32,57344
MA 90,116,58
RT 72,32,58368
MA 150,120,58
buffer_load_dwordx4 v19, s[44:47], s60 offen offset:1024 lds
MA 154,124,58
s_add_u32 s60, s60, 229376
WL 4
MA 94,112,62
RT 74,33,57344
MA 98,116,62
RT 76,33,58368
MA 158,120,62
buffer_load_dwordx4 a[32:35], v16, s[40:43], s63 offen offset:0 sc1
)"
R"(MA 162,124,62
WL 4
MA 102,112,66
RT 54,34,57344
MA 106,116,66
RT 56,34,58368
MA 166,120,66
buffer_load_dwordx4 a[36:39], v16, s[40:43], s63 offen offset:1024 sc1
MA 170,124,66
s_add_u32 s63, s63, 4096
WL 4
MA 110,112,70
RT 58,35,57344
MA 114,116,70
RT 60,35,58368
MA 174,120,70
buffer_load_dwordx4 a[40:43], v17, s[40:43], s86 offen offset:0 sc1
MA 178,124,70
WL 4
MA 118,112,74
s_waitcnt vmcnt(28)
s_barrier
RT 62,36,0
MA 122,116,74
RT 64,36,1024
MA 182,120,74
buffer_load_dwordx4 a[44:47], v17, s[40:43], s86 offen offset:1024 sc1
MA 186,124,74
s_add_u32 s86, s86, 4096
WL 4
MA 126,112,54
RT 66,37,0
MA 130,116,54
RT 68,37,1024
s_add_u32 m0, s52, 16384
MA 190,120,54
buffer_load_dwordx4 v18, s[44:47], s60 offen offset:0 lds
MA 194,124,54
WL 4
MA 134,112,58
RT 70,38,0
MA 138,116,58
RT 72,38,1024
MA 198,120,58
buffer_load_dwordx4 v19, s[44:47], s60 offen offset:1024 lds
MA 202,124,58
s_add_u32 s60, s60, 229376
s_waitcnt vmcnt(27) lgkmcnt(4)
MA 78,128,62
RT 74,39,0
MA 82,132,62
RT 76,39,1024
MA 142,136,62
buffer_load_dwordx4 a[48:51], v16, s[40:43], s63 offen offset:0 sc1
MA 146,140,62
WL 4
MA 86,128,66
RT 54,40,0
MA 90,132,66
RT 56,40,1024
MA 150,136,66
buffer_load_dwordx4 a[52:55], v16, s[40:43], s63 offen offset:1024 sc1
MA 154,140,66
s_add_u32 s63, s63, 4096
WL 4
MA 94,128,70
RT 58,41,0
MA 98,132,70
RT 60,41,1024
MA 158,136,70
buffer_load_dwordx4 a[56:59], v17, s[40:43], s86 offen offset:0 sc1
MA 162,140,70
WL 4
MA 102,128,74
RT 62,42,0
MA 106,132,74
RT 64,42,1024
MA 166,136,74
buffer_load_dwordx4 a[60:63], v17, s[40:43], s86 offen offset:1024 sc1
MA 170,140,74
s_add_u32 s86, s86, 4096
WL 4
MA 110,128,54
RT 66,43,0
MA 114,132,54
RT 68,43,1024
s_add_u32 m0, s52, 24576
MA 174,136,54
buffer_load_dwordx4 v18, s[44:47], s60 offen offset:0 lds
MA 178,140,54
WL 4
MA 118,128,58
RT 70,36,8192
MA 122,132,58
RT 72,36,9216
MA 182,136,58
buffer_load_dwordx4 v19, s[44:47], s60 offen offset:1024 lds
MA 186,140,58
s_add_u32 s60, s60, 229376
WL 4
MA 126,128,62
RT 74,37,8192
MA 130,132,62
RT 76,37,9216
MA 190,136,62
buffer_load_dwordx4 a[64:67], v16, s[40:43], s63 offen offset:0 sc1
MA 194,140,62
WL 4
MA 134,128,66
RT 54,38,8192
MA 138,132,66
RT 56,38,9216
MA 198,136,66
buffer_load_dwordx4 a[68:71], v16, s[40:43], s63 offen offset:1024 sc1
MA 202,140,66
s_add_u32 s63, s63, 4096
s_waitcnt vmcnt(28) lgkmcnt(4)
MA 78,144,70
RT 58,39,8192
MA 82,148,70
RT 60,39,9216
MA 142,152,70
buffer_load_dwordx4 a[72:75], v17, s[40:43], s86 offen offset:0 sc1
MA 146,156,70
WL 4
MA 86,144,74
RT 62,40,8192
MA 90,148,74
RT 64,40,9216
MA 150,152,74
buffer_load_dwordx4 a[76:79], v17, s[40:43], s86 offen offset:1024 sc1
MA 154,156,74
s_add_u32 s86, s86, 4096
WL 4
MA 94,144,54
RT 66,41,8192
MA 98,148,54
RT 68,41,9216
s_add_u32 m0, s52, 32768
MA 158,152,54
buffer_load_dwordx4 v18, s[44:47], s60 offen offset:0 lds
MA 162,156,54
WL 4
MA 102,144,58
RT 70,42,8192
MA 106,148,58
RT 72,42,9216
MA 166,152,58
buffer_load_dwordx4 v19, s[44:47], s60 offen offset:1024 lds
MA 170,156,58
s_add_u32 s60, s60, 229376
WL 4
MA 110,144,62
RT 74,43,8192
MA 114,148,62
RT 76,43,9216
MA 174,152,62
buffer_load_dwordx4 a[80:83], v16, s[40:43], s63 offen offset:0 sc1
MA 178,156,62
WL 4
MA 118,144,66
s_waitcnt vmcnt(27)
s_barrier
RT 54,36,16384
MA 122,148,66
RT 56,36,17408
MA 182,152,66
buffer_load_dwordx4 a[84:87], v16, s[40:43], s63 offen offset:1024 sc1
MA 186,156,66
s_add_u32 s63, s63, 4096
WL 4
MA 126,144,70
RT 58,37,16384
MA 130,148,70
RT 60,37,17408
MA 190,152,70
buffer_load_dwordx4 a[88:91], v17, s[40:43], s86 offen offset:0 sc1
MA 194,156,70
WL 4
MA 134,144,74
RT 62,38,16384
MA 138,148,74
RT 64,38,17408
MA 198,152,74
buffer_load_dwordx4 a[92:95], v17, s[40:43], s86 offen offset:1024 sc1
MA 202,156,74
s_add_u32 s86, s86, 4096
WL 4
MA 78,160,54
RT 66,39,16384
MA 82,164,54
RT 68,39,17408
s_add_u32 m0, s52, 40960
MA 142,168,54
buffer_load_dwordx4 v18, s[44:47], s60 offen offset:0 lds
MA 146,172,54
WL 4
MA 86,160,58
RT 70,40,16384
MA 90,164,58
RT 72,40,17408
MA 150,168,58
buffer_load_dwordx4 v19, s[44:47], s60 offen offset:1024 lds
MA 154,172,58
s_add_u32 s60, s60, 229376
WL 4
MA 94,160,62
RT 74,41,16384
MA 98,164,62
RT 76,41,17408
MA 158,168,62
buffer_load_dwordx4 a[96:99], v16, s[40:43], s63 offen offset:0 sc1
MA 162,172,62
WL 4
MA 102,160,66
RT 54,42,16384
MA 106,164,66
RT 56,42,17408
MA 166,168,66
buffer_load_dwordx4 a[100:103], v16, s[40:43], s63 offen offset:1024 sc1
MA 170,172,66
s_add_u32 s63, s63, 4096
WL 4
MA 110,160,70
RT 58,43,16384
MA 114,164,70
RT 60,43,17408
MA 174,168,70
buffer_load_dwordx4 a[104:107], v17, s[40:43], s86 offen offset:0 sc1
MA 178,172,70
WL 4
MA 118,160,74
RT 62,36,24576
MA 122,164,74
RT 64,36,25600
MA 182,168,74
buffer_load_dwordx4 a[108:111], v17, s[40:43], s86 offen offset:1024 sc1
MA 186,172,74
s_add_u32 s86, s86, 4096
WL 4
MA 126,160,54
RT 66,37,24576
MA 130,164,54
RT 68,37,25600
s_add_u32 m0, s52, 49152
MA 190,168,54
buffer_load_dwordx4 v18, s[44:47], s60 offen offset:0 lds
MA 194,172,54
WL 4
MA 134,160,58
RT 70,38,24576
MA 138,164,58
RT 72,38,25600
MA 198,168,58
buffer_load_dwordx4 v19, s[44:47], s60 offen offset:1024 lds
MA 202,172,58
s_add_u32 s60, s60, 229376
s_waitcnt vmcnt(34) lgkmcnt(4)
MA 78,176,62
RT 74,39,24576
MA 82,180,62
RT 76,39,25600
MA 142,184,62
buffer_load_dwordx4 a[112:115], v16, s[40:43], s63 offen offset:0 sc1
MA 146,188,62
WL 4
MA 86,176,66
RT 54,40,24576
MA 90,180,66
RT 56,40,25600
MA 150,184,66
buffer_load_dwordx4 a[116:119], v16, s[40:43], s63 offen offset:1024 sc1
MA 154,188,66
s_add_u32 s63, s63, 4096
WL 4
MA 94,176,70
RT 58,41,24576
MA 98,180,70
RT 60,41,25600
MA 158,184,70
buffer_load_dwordx4 a[120:123], v17, s[40:43], s86 offen offset:0 sc1
MA 162,188,70
WL 4
MA 102,176,74
RT 62,42,24576
MA 106,180,74
RT 64,42,25600
MA 166,184,74
buffer_load_dwordx4 a[124:127], v17, s[40:43], s86 offen offset:1024 sc1
MA 170,188,74
s_add_u32 s86, s86, 4096
WL 4
MA 110,176,54
RT 66,43,24576
MA 114,180,54
RT 68,43,25600
MA 174,184,54
MA 178,188,54
WL 4
MA 118,176,58
s_waitcnt vmcnt(34)
s_barrier
RT 70,20,0
MA 122,180,58
RT 72,20,1024
s_add_u32 m0, s52, 57344
MA 182,184,58
buffer_load_dwordx4 v18, s[44:47], s60 offen offset:0 lds
MA 186,188,58
WL 4
MA 126,176,62
RT 74,21,0
MA 130,180,62
RT 76,21,1024
MA 190,184,62
buffer_load_dwordx4 v19, s[44:47], s60 offen offset:1024 lds
MA 194,188,62
s_add_u32 s60, s60, 229376
WL 4
MA 134,176,66
RT 54,22,0
MA 138,180,66
RT 56,22,1024
MA 198,184,66
MA 202,188,66
s_waitcnt vmcnt(32) lgkmcnt(4)
MA 78,32,70
RT 58,23,0
MA 82,36,70
RT 60,23,1024
MA 142,40,70
buffer_load_dwordx4 a[128:131], v16, s[40:43], s63 offen offset:0 sc1
MA 146,44,70
WL 4
MA 86,32,74
RT 62,24,0
MA 90,36,74
RT 64,24,1024
MA 150,40,74
buffer_load_dwordx4 a[132:135], v16, s[40:43], s63 offen offset:1024 sc1
MA 154,44,74
s_add_u32 s63, s63, 4096
WL 4
MA 94,32,54
RT 66,25,0
MA 98,36,54
RT 68,25,1024
MA 158,40,54
buffer_load_dwordx4 a[136:139], v17, s[40:43], s86 offen offset:0 sc1
MA 162,44,54
WL 4
MA 102,32,58
RT 70,26,0
MA 106,36,58
RT 72,26,1024
MA 166,40,58
buffer_load_dwordx4 a[140:143], v17, s[40:43], s86 offen offset:1024 sc1
MA 170,44,58
s_add_u32 s86, s86, 4096
WL 4
MA 110,32,62
RT 74,27,0
MA 114,36,62
RT 76,27,1024
MA 174,40,62
MA 178,44,62
WL 4
MA 118,32,66
RT 54,20,8192
MA 122,36,66
RT 56,20,9216
s_add_u32 m0, s52, 65536
MA 182,40,66
buffer_load_dwordx4 v18, s[44:47], s60 offen offset:0 lds
MA 186,44,66
WL 4
MA 126,32,70
RT 58,21,8192
MA 130,36,70
RT 60,21,9216
MA 190,40,70
buffer_load_dwordx4 v19, s[44:47], s60 offen offset:1024 lds
MA 194,44,70
s_add_u32 s60, s60, 229376
WL 4
MA 134,32,74
RT 62,22,8192
MA 138,36,74
RT 64,22,9216
MA 198,40,74
MA 202,44,74
s_waitcnt vmcnt(32) lgkmcnt(4)
MA 78,48,54
RT 66,23,8192
MA 82,52,54
RT 68,23,9216
MA 142,56,54
buffer_load_dwordx4 a[144:147], v16, s[40:43], s63 offen offset:0 sc1
MA 146,60,54
WL 4
)"
R"(MA 86,48,58
RT 70,24,8192
MA 90,52,58
RT 72,24,9216
MA 150,56,58
buffer_load_dwordx4 a[148:151], v16, s[40:43], s63 offen offset:1024 sc1
MA 154,60,58
s_add_u32 s63, s63, 4096
WL 4
MA 94,48,62
RT 74,25,8192
MA 98,52,62
RT 76,25,9216
MA 158,56,62
buffer_load_dwordx4 a[152:155], v17, s[40:43], s86 offen offset:0 sc1
MA 162,60,62
WL 4
MA 102,48,66
RT 54,26,8192
MA 106,52,66
RT 56,26,9216
MA 166,56,66
buffer_load_dwordx4 a[156:159], v17, s[40:43], s86 offen offset:1024 sc1
MA 170,60,66
s_add_u32 s86, s86, 4096
WL 4
MA 110,48,70
RT 58,27,8192
MA 114,52,70
RT 60,27,9216
MA 174,56,70
MA 178,60,70
WL 4
MA 118,48,74
s_waitcnt vmcnt(34)
s_barrier
RT 62,20,16384
MA 122,52,74
RT 64,20,17408
s_add_u32 m0, s52, 73728
MA 182,56,74
buffer_load_dwordx4 v18, s[44:47], s60 offen offset:0 lds
MA 186,60,74
WL 4
MA 126,48,54
RT 66,21,16384
MA 130,52,54
RT 68,21,17408
MA 190,56,54
buffer_load_dwordx4 v19, s[44:47], s60 offen offset:1024 lds
MA 194,60,54
s_add_u32 s60, s60, 229376
WL 4
MA 134,48,58
RT 70,22,16384
MA 138,52,58
RT 72,22,17408
MA 198,56,58
MA 202,60,58
s_waitcnt vmcnt(32) lgkmcnt(4)
MA 78,64,62
RT 74,23,16384
MA 82,68,62
RT 76,23,17408
MA 142,72,62
buffer_load_dword v50, v49, s[44:47], s58 offen
MA 146,76,62
s_add_u32 s58, s58, 3670016
s_cmp_ge_u32 s58, s61
s_cselect_b32 s58, 0x40000000, s58
WL 4
MA 86,64,66
RT 54,24,16384
MA 90,68,66
RT 56,24,17408
MA 150,72,66
MA 154,76,66
WL 4
MA 94,64,70
RT 58,25,16384
MA 98,68,70
RT 60,25,17408
MA 158,72,70
MA 162,76,70
WL 4
MA 102,64,74
RT 62,26,16384
MA 106,68,74
RT 64,26,17408
MA 166,72,74
MA 170,76,74
WL 4
MA 110,64,54
RT 66,27,16384
MA 114,68,54
RT 68,27,17408
MA 174,72,54
MA 178,76,54
WL 4
MA 118,64,58
RT 70,20,24576
MA 122,68,58
RT 72,20,25600
MA 182,72,58
MA 186,76,58
WL 4
MA 126,64,62
RT 74,21,24576
MA 130,68,62
RT 76,21,25600
MA 190,72,62
MA 194,76,62
WL 4
MA 134,64,66
RT 54,22,24576
MA 138,68,66
RT 56,22,25600
MA 198,72,66
MA 202,76,66
s_waitcnt vmcnt(27) lgkmcnt(4)
MA 78,80,70
RT 58,23,24576
MA 82,84,70
RT 60,23,25600
MA 142,88,70
MA 146,92,70
WL 4
MA 86,80,74
RT 62,24,24576
MA 90,84,74
RT 64,24,25600
MA 150,88,74
MA 154,92,74
WL 4
MA 94,80,54
RT 66,25,24576
MA 98,84,54
RT 68,25,25600
MA 158,88,54
MA 162,92,54
WL 4
MA 102,80,58
RT 70,26,24576
MA 106,84,58
RT 72,26,25600
MA 166,88,58
MA 170,92,58
WL 4
MA 110,80,62
RT 74,27,24576
MA 114,84,62
RT 76,27,25600
MA 174,88,62
MA 178,92,62
WL 4
MA 118,80,66
s_waitcnt vmcnt(25)
s_barrier
RT 54,20,32768
MA 122,84,66
RT 56,20,33792
MA 182,88,66
MA 186,92,66
WL 4
MA 126,80,70
RT 58,21,32768
MA 130,84,70
RT 60,21,33792
MA 190,88,70
MA 194,92,70
WL 4
MA 134,80,74
RT 62,22,32768
MA 138,84,74
RT 64,22,33792
MA 198,88,74
MA 202,92,74
s_waitcnt vmcnt(21) lgkmcnt(4)
MA 78,96,54
RT 66,23,32768
MA 82,100,54
RT 68,23,33792
MA 142,104,54
buffer_load_dwordx4 a[160:163], v16, s[40:43], s63 offen offset:0 sc1
MA 146,108,54
WL 4
MA 86,96,58
RT 70,24,32768
MA 90,100,58
RT 72,24,33792
MA 150,104,58
buffer_load_dwordx4 a[164:167], v16, s[40:43], s63 offen offset:1024 sc1
MA 154,108,58
s_add_u32 s63, s63, 4096
WL 4
MA 94,96,62
RT 74,25,32768
MA 98,100,62
RT 76,25,33792
MA 158,104,62
buffer_load_dwordx4 a[168:171], v17, s[40:43], s86 offen offset:0 sc1
MA 162,108,62
WL 4
MA 102,96,66
RT 54,26,32768
MA 106,100,66
RT 56,26,33792
MA 166,104,66
buffer_load_dwordx4 a[172:175], v17, s[40:43], s86 offen offset:1024 sc1
MA 170,108,66
s_add_u32 s86, s86, 4096
WL 4
MA 110,96,70
RT 58,27,32768
MA 114,100,70
RT 60,27,33792
MA 174,104,70
buffer_load_dwordx4 a[176:179], v16, s[40:43], s63 offen offset:0 sc1
MA 178,108,70
WL 4
MA 118,96,74
RT 62,20,40960
MA 122,100,74
RT 64,20,41984
MA 182,104,74
buffer_load_dwordx4 a[180:183], v16, s[40:43], s63 offen offset:1024 sc1
MA 186,108,74
s_add_u32 s63, s63, 4096
WL 4
MA 126,96,54
RT 66,21,40960
MA 130,100,54
RT 68,21,41984
MA 190,104,54
buffer_load_dwordx4 a[184:187], v17, s[40:43], s86 offen offset:0 sc1
MA 194,108,54
WL 4
MA 134,96,58
RT 70,22,40960
MA 138,100,58
RT 72,22,41984
MA 198,104,58
buffer_load_dwordx4 a[188:191], v17, s[40:43], s86 offen offset:1024 sc1
MA 202,108,58
s_add_u32 s86, s86, 4096
s_waitcnt vmcnt(23) lgkmcnt(4)
MA 78,112,62
RT 74,23,40960
MA 82,116,62
RT 76,23,41984
s_add_u32 m0, s52, 81920
MA 142,120,62
buffer_load_dwordx4 v18, s[44:47], s60 offen offset:0 lds
MA 146,124,62
WL 4
MA 86,112,66
RT 54,24,40960
MA 90,116,66
RT 56,24,41984
MA 150,120,66
buffer_load_dwordx4 v19, s[44:47], s60 offen offset:1024 lds
MA 154,124,66
s_add_u32 s60, s60, 229376
WL 4
MA 94,112,70
RT 58,25,40960
MA 98,116,70
RT 60,25,41984
MA 158,120,70
buffer_load_dwordx4 a[32:35], v16, s[40:43], s63 offen offset:0 sc1
MA 162,124,70
WL 4
MA 102,112,74
RT 62,26,40960
MA 106,116,74
RT 64,26,41984
MA 166,120,74
buffer_load_dwordx4 a[36:39], v16, s[40:43], s63 offen offset:1024 sc1
MA 170,124,74
s_add_u32 s63, s63, 4096
WL 4
MA 110,112,54
RT 66,27,40960
MA 114,116,54
RT 68,27,41984
MA 174,120,54
buffer_load_dwordx4 a[40:43], v17, s[40:43], s86 offen offset:0 sc1
MA 178,124,54
WL 4
MA 118,112,58
s_waitcnt vmcnt(26)
s_barrier
RT 70,20,49152
MA 122,116,58
RT 72,20,50176
MA 182,120,58
buffer_load_dwordx4 a[44:47], v17, s[40:43], s86 offen offset:1024 sc1
MA 186,124,58
s_add_u32 s86, s86, 4096
WL 4
MA 126,112,62
RT 74,21,49152
MA 130,116,62
RT 76,21,50176
s_add_u32 m0, s52, 90112
MA 190,120,62
buffer_load_dwordx4 v18, s[44:47], s60 offen offset:0 lds
MA 194,124,62
WL 4
MA 134,112,66
RT 54,22,49152
MA 138,116,66
RT 56,22,50176
MA 198,120,66
buffer_load_dwordx4 v19, s[44:47], s60 offen offset:1024 lds
MA 202,124,66
s_add_u32 s60, s60, 229376
s_waitcnt vmcnt(25) lgkmcnt(4)
MA 78,128,70
RT 58,23,49152
MA 82,132,70
RT 60,23,50176
s_add_u32 m0, s52, 98304
MA 142,136,70
buffer_load_dwordx4 v18, s[44:47], s60 offen offset:0 lds
MA 146,140,70
WL 4
MA 86,128,74
RT 62,24,49152
MA 90,132,74
RT 64,24,50176
MA 150,136,74
buffer_load_dwordx4 v19, s[44:47], s60 offen offset:1024 lds
MA 154,140,74
s_add_u32 s60, s60, 229376
WL 4
MA 94,128,54
RT 66,25,49152
MA 98,132,54
RT 68,25,50176
MA 158,136,54
buffer_load_dwordx4 a[48:51], v16, s[40:43], s63 offen offset:0 sc1
MA 162,140,54
WL 4
MA 102,128,58
RT 70,26,49152
MA 106,132,58
RT 72,26,50176
MA 166,136,58
buffer_load_dwordx4 a[52:55], v16, s[40:43], s63 offen offset:1024 sc1
MA 170,140,58
s_add_u32 s63, s63, 4096
WL 4
MA 110,128,62
RT 74,27,49152
MA 114,132,62
RT 76,27,50176
MA 174,136,62
buffer_load_dwordx4 a[56:59], v17, s[40:43], s86 offen offset:0 sc1
MA 178,140,62
WL 4
MA 118,128,66
RT 54,20,57344
MA 122,132,66
RT 56,20,58368
MA 182,136,66
buffer_load_dwordx4 a[60:63], v17, s[40:43], s86 offen offset:1024 sc1
MA 186,140,66
s_add_u32 s86, s86, 4096
WL 4
MA 126,128,70
RT 58,21,57344
MA 130,132,70
RT 60,21,58368
s_add_u32 m0, s52, 106496
MA 190,136,70
buffer_load_dwordx4 v18, s[44:47], s60 offen offset:0 lds
MA 194,140,70
WL 4
MA 134,128,74
RT 62,22,57344
MA 138,132,74
RT 64,22,58368
MA 198,136,74
buffer_load_dwordx4 v19, s[44:47], s60 offen offset:1024 lds
MA 202,140,74
s_add_u32 s60, s60, 229376
s_waitcnt vmcnt(27) lgkmcnt(4)
MA 78,144,54
RT 66,23,57344
MA 82,148,54
RT 68,23,58368
MA 142,152,54
buffer_load_dwordx4 a[64:67], v16, s[40:43], s63 offen offset:0 sc1
MA 146,156,54
WL 4
MA 86,144,58
RT 70,24,57344
MA 90,148,58
RT 72,24,58368
)"
R"(MA 150,152,58
buffer_load_dwordx4 a[68:71], v16, s[40:43], s63 offen offset:1024 sc1
MA 154,156,58
s_add_u32 s63, s63, 4096
WL 4
MA 94,144,62
RT 74,25,57344
MA 98,148,62
RT 76,25,58368
MA 158,152,62
buffer_load_dwordx4 a[72:75], v17, s[40:43], s86 offen offset:0 sc1
MA 162,156,62
WL 4
MA 102,144,66
RT 54,26,57344
MA 106,148,66
RT 56,26,58368
MA 166,152,66
buffer_load_dwordx4 a[76:79], v17, s[40:43], s86 offen offset:1024 sc1
MA 170,156,66
s_add_u32 s86, s86, 4096
WL 4
MA 110,144,70
RT 58,27,57344
MA 114,148,70
RT 60,27,58368
s_add_u32 m0, s52, 114688
MA 174,152,70
buffer_load_dwordx4 v18, s[44:47], s60 offen offset:0 lds
MA 178,156,70
WL 4
MA 118,144,74
s_waitcnt vmcnt(30)
s_barrier
RT 62,28,0
MA 122,148,74
RT 64,28,1024
MA 182,152,74
buffer_load_dwordx4 v19, s[44:47], s60 offen offset:1024 lds
MA 186,156,74
s_add_u32 s60, s60, 229376
WL 4
MA 126,144,54
RT 66,29,0
MA 130,148,54
RT 68,29,1024
MA 190,152,54
buffer_load_dwordx4 a[80:83], v16, s[40:43], s63 offen offset:0 sc1
MA 194,156,54
WL 4
MA 134,144,58
RT 70,30,0
MA 138,148,58
RT 72,30,1024
MA 198,152,58
buffer_load_dwordx4 a[84:87], v16, s[40:43], s63 offen offset:1024 sc1
MA 202,156,58
s_add_u32 s63, s63, 4096
s_waitcnt vmcnt(28) lgkmcnt(4)
MA 78,160,62
RT 74,31,0
MA 82,164,62
RT 76,31,1024
MA 142,168,62
buffer_load_dwordx4 a[88:91], v17, s[40:43], s86 offen offset:0 sc1
MA 146,172,62
WL 4
MA 86,160,66
RT 54,32,0
MA 90,164,66
RT 56,32,1024
MA 150,168,66
buffer_load_dwordx4 a[92:95], v17, s[40:43], s86 offen offset:1024 sc1
MA 154,172,66
s_add_u32 s86, s86, 4096
WL 4
MA 94,160,70
RT 58,33,0
MA 98,164,70
RT 60,33,1024
s_add_u32 m0, s52, 122880
MA 158,168,70
buffer_load_dwordx4 v18, s[44:47], s60 offen offset:0 lds
MA 162,172,70
WL 4
MA 102,160,74
RT 62,34,0
MA 106,164,74
RT 64,34,1024
MA 166,168,74
buffer_load_dwordx4 v19, s[44:47], s60 offen offset:1024 lds
MA 170,172,74
s_add_u32 s60, s60, 229376
WL 4
MA 110,160,54
RT 66,35,0
MA 114,164,54
RT 68,35,1024
MA 174,168,54
buffer_load_dwordx4 a[96:99], v16, s[40:43], s63 offen offset:0 sc1
MA 178,172,54
WL 4
MA 118,160,58
RT 70,28,8192
MA 122,164,58
RT 72,28,9216
MA 182,168,58
buffer_load_dwordx4 a[100:103], v16, s[40:43], s63 offen offset:1024 sc1
MA 186,172,58
s_add_u32 s63, s63, 4096
WL 4
MA 126,160,62
RT 74,29,8192
MA 130,164,62
RT 76,29,9216
MA 190,168,62
buffer_load_dwordx4 a[104:107], v17, s[40:43], s86 offen offset:0 sc1
MA 194,172,62
WL 4
MA 134,160,66
RT 54,30,8192
MA 138,164,66
RT 56,30,9216
MA 198,168,66
buffer_load_dwordx4 a[108:111], v17, s[40:43], s86 offen offset:1024 sc1
MA 202,172,66
s_add_u32 s86, s86, 4096
s_waitcnt vmcnt(32) lgkmcnt(4)
MA 78,176,70
RT 58,31,8192
MA 82,180,70
RT 60,31,9216
s_add_u32 m0, s52, 131072
MA 142,184,70
buffer_load_dwordx4 v18, s[44:47], s60 offen offset:0 lds
MA 146,188,70
WL 4
MA 86,176,74
RT 62,32,8192
MA 90,180,74
RT 64,32,9216
MA 150,184,74
buffer_load_dwordx4 v19, s[44:47], s60 offen offset:1024 lds
MA 154,188,74
s_add_u32 s60, s60, 229376
WL 4
MA 94,176,54
RT 66,33,8192
MA 98,180,54
RT 68,33,9216
MA 158,184,54
buffer_load_dwordx4 a[112:115], v16, s[40:43], s63 offen offset:0 sc1
MA 162,188,54
WL 4
MA 102,176,58
RT 70,34,8192
MA 106,180,58
RT 72,34,9216
MA 166,184,58
buffer_load_dwordx4 a[116:119], v16, s[40:43], s63 offen offset:1024 sc1
MA 170,188,58
s_add_u32 s63, s63, 4096
WL 4
MA 110,176,62
RT 74,35,8192
MA 114,180,62
RT 76,35,9216
MA 174,184,62
buffer_load_dwordx4 a[120:123], v17, s[40:43], s86 offen offset:0 sc1
MA 178,188,62
WL 4
MA 118,176,66
s_waitcnt vmcnt(29)
s_barrier
RT 54,28,16384
MA 122,180,66
RT 56,28,17408
MA 182,184,66
buffer_load_dwordx4 a[124:127], v17, s[40:43], s86 offen offset:1024 sc1
MA 186,188,66
s_add_u32 s86, s86, 4096
WL 4
MA 126,176,70
RT 58,29,16384
MA 130,180,70
RT 60,29,17408
s_add_u32 m0, s52, 139264
MA 190,184,70
buffer_load_dwordx4 v18, s[44:47], s60 offen offset:0 lds
MA 194,188,70
WL 4
MA 134,176,74
RT 62,30,16384
MA 138,180,74
RT 64,30,17408
MA 198,184,74
buffer_load_dwordx4 v19, s[44:47], s60 offen offset:1024 lds
MA 202,188,74
s_add_u32 s60, s60, 229376
WL 4
MA 78,32,54
RT 66,31,16384
MA 82,36,54
RT 68,31,17408
s_add_u32 m0, s52, 147456
MA 142,40,54
buffer_load_dwordx4 v18, s[44:47], s60 offen offset:0 lds
MA 146,44,54
WL 4
MA 86,32,58
RT 70,32,16384
MA 90,36,58
RT 72,32,17408
MA 150,40,58
buffer_load_dwordx4 v19, s[44:47], s60 offen offset:1024 lds
MA 154,44,58
s_add_u32 s60, s60, 229376
WL 4
MA 94,32,62
RT 74,33,16384
MA 98,36,62
RT 76,33,17408
MA 158,40,62
buffer_load_dwordx4 a[128:131], v17, s[40:43], s86 offen offset:0 sc1
MA 162,44,62
WL 4
MA 102,32,66
RT 54,34,16384
MA 106,36,66
RT 56,34,17408
MA 166,40,66
buffer_load_dwordx4 a[132:135], v17, s[40:43], s86 offen offset:1024 sc1
MA 170,44,66
s_add_u32 s86, s86, 4096
WL 4
MA 110,32,70
RT 58,35,16384
MA 114,36,70
RT 60,35,17408
s_add_u32 m0, s52, 155648
MA 174,40,70
buffer_load_dwordx4 v18, s[44:47], s60 offen offset:0 lds
MA 178,44,70
WL 4
MA 118,32,74
RT 62,28,24576
MA 122,36,74
RT 64,28,25600
MA 182,40,74
buffer_load_dwordx4 v19, s[44:47], s60 offen offset:1024 lds
MA 186,44,74
s_add_u32 s60, s60, 229376
WL 4
MA 126,32,54
RT 66,29,24576
MA 130,36,54
RT 68,29,25600
MA 190,40,54
buffer_load_dwordx4 a[136:139], v17, s[40:43], s86 offen offset:0 sc1
MA 194,44,54
WL 4
MA 134,32,58
RT 70,30,24576
MA 138,36,58
RT 72,30,25600
MA 198,40,58
buffer_load_dwordx4 a[140:143], v17, s[40:43], s86 offen offset:1024 sc1
MA 202,44,58
s_add_u32 s86, s86, 4096
s_waitcnt vmcnt(34) lgkmcnt(4)
MA 78,48,62
RT 74,31,24576
MA 82,52,62
RT 76,31,25600
s_add_u32 m0, s52, 0
MA 142,56,62
buffer_load_dwordx4 v18, s[44:47], s60 offen offset:0 lds
MA 146,60,62
WL 4
MA 86,48,66
RT 54,32,24576
MA 90,52,66
RT 56,32,25600
MA 150,56,66
buffer_load_dwordx4 v19, s[44:47], s60 offen offset:1024 lds
MA 154,60,66
s_add_u32 s60, s60, 229376
WL 4
MA 94,48,70
RT 58,33,24576
MA 98,52,70
RT 60,33,25600
MA 158,56,70
buffer_load_dwordx4 a[144:147], v17, s[40:43], s86 offen offset:0 sc1
MA 162,60,70
WL 4
MA 102,48,74
RT 62,34,24576
MA 106,52,74
RT 64,34,25600
MA 166,56,74
buffer_load_dwordx4 a[148:151], v17, s[40:43], s86 offen offset:1024 sc1
MA 170,60,74
s_add_u32 s86, s86, 4096
WL 4
MA 110,48,54
RT 66,35,24576
MA 114,52,54
RT 68,35,25600
s_add_u32 m0, s52, 8192
MA 174,56,54
buffer_load_dwordx4 v18, s[44:47], s60 offen offset:0 lds
MA 178,60,54
WL 4
MA 118,48,58
s_waitcnt vmcnt(37)
s_barrier
RT 70,28,32768
MA 122,52,58
RT 72,28,33792
MA 182,56,58
buffer_load_dwordx4 v19, s[44:47], s60 offen offset:1024 lds
MA 186,60,58
s_add_u32 s60, s60, 229376
WL 4
MA 126,48,62
RT 74,29,32768
MA 130,52,62
RT 76,29,33792
MA 190,56,62
buffer_load_dwordx4 a[152:155], v17, s[40:43], s86 offen offset:0 sc1
MA 194,60,62
WL 4
MA 134,48,66
RT 54,30,32768
MA 138,52,66
RT 56,30,33792
MA 198,56,66
buffer_load_dwordx4 a[156:159], v17, s[40:43], s86 offen offset:1024 sc1
MA 202,60,66
s_add_u32 s86, s86, 4096
s_waitcnt vmcnt(36) lgkmcnt(4)
MA 78,64,70
RT 58,31,32768
MA 82,68,70
RT 60,31,33792
s_add_u32 m0, s52, 16384
MA 142,72,70
buffer_load_dwordx4 v18, s[44:47], s60 offen offset:0 lds
MA 146,76,70
WL 4
MA 86,64,74
RT 62,32,32768
MA 90,68,74
RT 64,32,33792
MA 150,72,74
buffer_load_dwordx4 v19, s[44:47], s60 offen offset:1024 lds
MA 154,76,74
s_add_u32 s60, s60, 229376
WL 4
MA 94,64,54
RT 66,33,32768
MA 98,68,54
RT 68,33,33792
MA 158,72,54
buffer_load_dwordx4 a[160:163], v17, s[40:43], s86 offen offset:0 sc1
MA 162,76,54
WL 4
MA 102,64,58
RT 70,34,32768
MA 106,68,58
RT 72,34,33792
MA 166,72,58
buffer_load_dwordx4 a[164:167], v17, s[40:43], s86 offen offset:1024 sc1
MA 170,76,58
s_add_u32 s86, s86, 4096
WL 4
MA 110,64,62
RT 74,35,32768
MA 114,68,62
RT 76,35,33792
s_add_u32 m0, s52, 24576
MA 174,72,62
buffer_load_dwordx4 v18, s[44:47], s60 offen offset:0 lds
MA 178,76,62
WL 4
MA 118,64,66
RT 54,28,40960
MA 122,68,66
RT 56,28,41984
MA 182,72,66
buffer_load_dwordx4 v19, s[44:47], s60 offen offset:1024 lds
MA 186,76,66
s_add_u32 s60, s60, 229376
WL 4
MA 126,64,70
RT 58,29,40960
MA 130,68,70
RT 60,29,41984
MA 190,72,70
)"
R"(buffer_load_dwordx4 a[168:171], v17, s[40:43], s86 offen offset:0 sc1
MA 194,76,70
WL 4
MA 134,64,74
RT 62,30,40960
MA 138,68,74
RT 64,30,41984
MA 198,72,74
buffer_load_dwordx4 a[172:175], v17, s[40:43], s86 offen offset:1024 sc1
MA 202,76,74
s_add_u32 s86, s86, 4096
s_waitcnt vmcnt(38) lgkmcnt(4)
MA 78,80,54
RT 66,31,40960
MA 82,84,54
RT 68,31,41984
s_add_u32 m0, s52, 32768
MA 142,88,54
buffer_load_dwordx4 v18, s[44:47], s60 offen offset:0 lds
MA 146,92,54
WL 4
MA 86,80,58
RT 70,32,40960
MA 90,84,58
RT 72,32,41984
MA 150,88,58
buffer_load_dwordx4 v19, s[44:47], s60 offen offset:1024 lds
MA 154,92,58
s_add_u32 s60, s60, 229376
WL 4
MA 94,80,62
RT 74,33,40960
MA 98,84,62
RT 76,33,41984
MA 158,88,62
buffer_load_dwordx4 a[176:179], v17, s[40:43], s86 offen offset:0 sc1
MA 162,92,62
WL 4
MA 102,80,66
RT 54,34,40960
MA 106,84,66
RT 56,34,41984
MA 166,88,66
buffer_load_dwordx4 a[180:183], v17, s[40:43], s86 offen offset:1024 sc1
MA 170,92,66
s_add_u32 s86, s86, 4096
WL 4
MA 110,80,70
RT 58,35,40960
MA 114,84,70
RT 60,35,41984
s_add_u32 m0, s52, 40960
MA 174,88,70
buffer_load_dwordx4 v18, s[44:47], s60 offen offset:0 lds
MA 178,92,70
WL 4
MA 118,80,74
s_waitcnt vmcnt(41)
s_barrier
RT 62,28,49152
MA 122,84,74
RT 64,28,50176
MA 182,88,74
buffer_load_dwordx4 v19, s[44:47], s60 offen offset:1024 lds
MA 186,92,74
s_add_u32 s60, s60, 229376
WL 4
MA 126,80,54
RT 66,29,49152
MA 130,84,54
RT 68,29,50176
MA 190,88,54
buffer_load_dwordx4 a[184:187], v17, s[40:43], s86 offen offset:0 sc1
MA 194,92,54
WL 4
MA 134,80,58
RT 70,30,49152
MA 138,84,58
RT 72,30,50176
MA 198,88,58
buffer_load_dwordx4 a[188:191], v17, s[40:43], s86 offen offset:1024 sc1
MA 202,92,58
s_add_u32 s86, s86, 4096
s_waitcnt vmcnt(40) lgkmcnt(4)
MA 78,96,62
RT 74,31,49152
MA 82,100,62
RT 76,31,50176
s_add_u32 m0, s52, 49152
MA 142,104,62
buffer_load_dwordx4 v18, s[44:47], s60 offen offset:0 lds
MA 146,108,62
WL 4
MA 86,96,66
RT 54,32,49152
MA 90,100,66
RT 56,32,50176
MA 150,104,66
buffer_load_dwordx4 v19, s[44:47], s60 offen offset:1024 lds
MA 154,108,66
s_add_u32 s60, s60, 229376
WL 4
MA 94,96,70
RT 58,33,49152
MA 98,100,70
RT 60,33,50176
MA 158,104,70
buffer_load_dwordx4 a[32:35], v17, s[40:43], s86 offen offset:0 sc1
MA 162,108,70
WL 4
MA 102,96,74
RT 62,34,49152
MA 106,100,74
RT 64,34,50176
MA 166,104,74
buffer_load_dwordx4 a[36:39], v17, s[40:43], s86 offen offset:1024 sc1
MA 170,108,74
s_add_u32 s86, s86, 4096
WL 4
MA 110,96,54
RT 66,35,49152
MA 114,100,54
RT 68,35,50176
s_add_u32 m0, s52, 57344
MA 174,104,54
buffer_load_dwordx4 v18, s[44:47], s60 offen offset:0 lds
MA 178,108,54
WL 4
MA 118,96,58
RT 70,28,57344
MA 122,100,58
RT 72,28,58368
MA 182,104,58
buffer_load_dwordx4 v19, s[44:47], s60 offen offset:1024 lds
MA 186,108,58
s_add_u32 s60, s60, 229376
WL 4
MA 126,96,62
RT 74,29,57344
MA 130,100,62
RT 76,29,58368
MA 190,104,62
buffer_load_dwordx4 a[40:43], v17, s[40:43], s86 offen offset:0 sc1
MA 194,108,62
WL 4
MA 134,96,66
RT 54,30,57344
MA 138,100,66
RT 56,30,58368
MA 198,104,66
buffer_load_dwordx4 a[44:47], v17, s[40:43], s86 offen offset:1024 sc1
MA 202,108,66
s_add_u32 s86, s86, 4096
s_waitcnt vmcnt(42) lgkmcnt(4)
MA 78,112,70
RT 58,31,57344
MA 82,116,70
RT 60,31,58368
s_add_u32 m0, s52, 65536
MA 142,120,70
buffer_load_dwordx4 v18, s[44:47], s60 offen offset:0 lds
MA 146,124,70
WL 4
MA 86,112,74
RT 62,32,57344
MA 90,116,74
RT 64,32,58368
MA 150,120,74
buffer_load_dwordx4 v19, s[44:47], s60 offen offset:1024 lds
MA 154,124,74
s_add_u32 s60, s60, 229376
WL 4
MA 94,112,54
RT 66,33,57344
MA 98,116,54
RT 68,33,58368
MA 158,120,54
buffer_load_dwordx4 a[48:51], v17, s[40:43], s86 offen offset:0 sc1
MA 162,124,54
WL 4
MA 102,112,58
RT 70,34,57344
MA 106,116,58
RT 72,34,58368
MA 166,120,58
buffer_load_dwordx4 a[52:55], v17, s[40:43], s86 offen offset:1024 sc1
MA 170,124,58
s_add_u32 s86, s86, 4096
WL 4
MA 110,112,62
RT 74,35,57344
MA 114,116,62
RT 76,35,58368
s_add_u32 m0, s52, 73728
MA 174,120,62
buffer_load_dwordx4 v18, s[44:47], s60 offen offset:0 lds
MA 178,124,62
WL 4
MA 118,112,66
s_waitcnt vmcnt(39)
s_barrier
RT 54,36,0
MA 122,116,66
RT 56,36,1024
MA 182,120,66
buffer_load_dwordx4 v19, s[44:47], s60 offen offset:1024 lds
MA 186,124,66
s_add_u32 s60, s60, 229376
WL 4
MA 126,112,70
RT 58,37,0
MA 130,116,70
RT 60,37,1024
MA 190,120,70
buffer_load_dwordx4 a[56:59], v17, s[40:43], s86 offen offset:0 sc1
MA 194,124,70
WL 4
MA 134,112,74
RT 62,38,0
MA 138,116,74
RT 64,38,1024
MA 198,120,74
buffer_load_dwordx4 a[60:63], v17, s[40:43], s86 offen offset:1024 sc1
MA 202,124,74
s_add_u32 s86, s86, 4096
WL 4
MA 142,128,54
RT 66,39,0
MA 146,132,54
RT 68,39,1024
WL 4
MA 150,128,58
RT 70,40,0
s_add_u32 m0, s52, 81920
MA 154,132,58
RT 72,40,1024
buffer_load_dwordx4 v18, s[44:47], s60 offen offset:0 lds
WL 4
MA 158,128,62
RT 74,41,0
MA 162,132,62
RT 76,41,1024
WL 4
MA 166,128,66
RT 54,42,0
MA 170,132,66
RT 56,42,1024
buffer_load_dwordx4 v19, s[44:47], s60 offen offset:1024 lds
WL 4
MA 174,128,70
RT 58,43,0
s_add_u32 s60, s60, 229376
MA 178,132,70
RT 60,43,1024
WL 4
MA 182,128,74
RT 62,36,8192
MA 186,132,74
RT 64,36,9216
buffer_load_dwordx4 a[64:67], v17, s[40:43], s86 offen offset:0 sc1
WL 4
MA 190,128,54
RT 66,37,8192
MA 194,132,54
RT 68,37,9216
WL 4
MA 198,128,58
RT 70,38,8192
MA 202,132,58
RT 72,38,9216
buffer_load_dwordx4 a[68:71], v17, s[40:43], s86 offen offset:1024 sc1
s_waitcnt vmcnt(44) lgkmcnt(4)
MA 142,136,62
RT 74,39,8192
s_add_u32 s86, s86, 4096
MA 146,140,62
RT 76,39,9216
WL 4
MA 150,136,66
RT 54,40,8192
s_add_u32 m0, s52, 90112
MA 154,140,66
RT 56,40,9216
buffer_load_dwordx4 v18, s[44:47], s60 offen offset:0 lds
WL 4
MA 158,136,70
RT 58,41,8192
MA 162,140,70
RT 60,41,9216
WL 4
MA 166,136,74
RT 62,42,8192
MA 170,140,74
RT 64,42,9216
buffer_load_dwordx4 v19, s[44:47], s60 offen offset:1024 lds
WL 4
MA 174,136,54
RT 66,43,8192
s_add_u32 s60, s60, 229376
MA 178,140,54
RT 68,43,9216
WL 4
MA 182,136,58
RT 70,36,16384
MA 186,140,58
RT 72,36,17408
buffer_load_dwordx4 a[72:75], v17, s[40:43], s86 offen offset:0 sc1
WL 4
MA 190,136,62
RT 74,37,16384
MA 194,140,62
RT 76,37,17408
WL 4
MA 198,136,66
RT 54,38,16384
MA 202,140,66
RT 56,38,17408
buffer_load_dwordx4 a[76:79], v17, s[40:43], s86 offen offset:1024 sc1
s_waitcnt vmcnt(44) lgkmcnt(4)
MA 142,144,70
RT 58,39,16384
s_add_u32 s86, s86, 4096
MA 146,148,70
RT 60,39,17408
WL 4
MA 150,144,74
RT 62,40,16384
s_add_u32 m0, s52, 98304
MA 154,148,74
RT 64,40,17408
buffer_load_dwordx4 v18, s[44:47], s60 offen offset:0 lds
WL 4
MA 158,144,54
RT 66,41,16384
MA 162,148,54
RT 68,41,17408
WL 4
MA 166,144,58
RT 70,42,16384
MA 170,148,58
RT 72,42,17408
buffer_load_dwordx4 v19, s[44:47], s60 offen offset:1024 lds
WL 4
MA 174,144,62
RT 74,43,16384
s_add_u32 s60, s60, 229376
MA 178,148,62
RT 76,43,17408
WL 4
MA 182,144,66
RT 54,36,24576
MA 186,148,66
RT 56,36,25600
buffer_load_dwordx4 a[80:83], v17, s[40:43], s86 offen offset:0 sc1
WL 4
MA 190,144,70
RT 58,37,24576
MA 194,148,70
RT 60,37,25600
WL 4
MA 198,144,74
RT 62,38,24576
MA 202,148,74
RT 64,38,25600
buffer_load_dwordx4 a[84:87], v17, s[40:43], s86 offen offset:1024 sc1
s_waitcnt vmcnt(44) lgkmcnt(4)
MA 142,152,54
RT 66,39,24576
s_add_u32 s86, s86, 4096
MA 146,156,54
RT 68,39,25600
WL 4
MA 150,152,58
RT 70,40,24576
s_add_u32 m0, s52, 106496
MA 154,156,58
RT 72,40,25600
buffer_load_dwordx4 v18, s[44:47], s60 offen offset:0 lds
WL 4
MA 158,152,62
RT 74,41,24576
MA 162,156,62
RT 76,41,25600
WL 4
MA 166,152,66
RT 54,42,24576
MA 170,156,66
RT 56,42,25600
buffer_load_dwordx4 v19, s[44:47], s60 offen offset:1024 lds
WL 4
MA 174,152,70
RT 58,43,24576
s_add_u32 s60, s60, 229376
MA 178,156,70
RT 60,43,25600
WL 4
MA 182,152,74
s_waitcnt vmcnt(40)
s_barrier
RT 62,20,0
)"
R"(MA 186,156,74
RT 64,20,1024
buffer_load_dwordx4 a[88:91], v17, s[40:43], s86 offen offset:0 sc1
WL 4
MA 190,152,54
RT 66,21,0
MA 194,156,54
RT 68,21,1024
WL 4
MA 198,152,58
RT 70,22,0
MA 202,156,58
RT 72,22,1024
buffer_load_dwordx4 a[92:95], v17, s[40:43], s86 offen offset:1024 sc1
WL 4
MA 142,160,62
RT 74,23,0
s_add_u32 s86, s86, 4096
MA 146,164,62
RT 76,23,1024
WL 4
MA 150,160,66
RT 54,24,0
s_add_u32 m0, s52, 114688
MA 154,164,66
RT 56,24,1024
buffer_load_dwordx4 v18, s[44:47], s60 offen offset:0 lds
WL 4
MA 158,160,70
RT 58,25,0
MA 162,164,70
RT 60,25,1024
WL 4
MA 166,160,74
RT 62,26,0
MA 170,164,74
RT 64,26,1024
buffer_load_dwordx4 v19, s[44:47], s60 offen offset:1024 lds
WL 4
MA 174,160,54
RT 66,27,0
s_add_u32 s60, s60, 229376
MA 178,164,54
RT 68,27,1024
WL 4
MA 182,160,58
RT 70,20,8192
MA 186,164,58
RT 72,20,9216
buffer_load_dwordx4 a[96:99], v17, s[40:43], s86 offen offset:0 sc1
WL 4
MA 190,160,62
RT 74,21,8192
MA 194,164,62
RT 76,21,9216
WL 4
MA 198,160,66
RT 54,22,8192
MA 202,164,66
RT 56,22,9216
buffer_load_dwordx4 a[100:103], v17, s[40:43], s86 offen offset:1024 sc1
s_waitcnt vmcnt(44) lgkmcnt(4)
MA 142,168,70
RT 58,23,8192
s_add_u32 s86, s86, 4096
MA 146,172,70
RT 60,23,9216
WL 4
MA 150,168,74
RT 62,24,8192
s_add_u32 m0, s52, 122880
MA 154,172,74
RT 64,24,9216
buffer_load_dwordx4 v18, s[44:47], s60 offen offset:0 lds
WL 4
MA 158,168,54
RT 66,25,8192
MA 162,172,54
RT 68,25,9216
WL 4
MA 166,168,58
RT 70,26,8192
MA 170,172,58
RT 72,26,9216
buffer_load_dwordx4 v19, s[44:47], s60 offen offset:1024 lds
WL 4
MA 174,168,62
RT 74,27,8192
s_add_u32 s60, s60, 229376
MA 178,172,62
RT 76,27,9216
WL 4
MA 182,168,66
RT 54,20,16384
MA 186,172,66
RT 56,20,17408
buffer_load_dwordx4 a[104:107], v17, s[40:43], s86 offen offset:0 sc1
WL 4
MA 190,168,70
RT 58,21,16384
MA 194,172,70
RT 60,21,17408
WL 4
MA 198,168,74
RT 62,22,16384
MA 202,172,74
RT 64,22,17408
buffer_load_dwordx4 a[108:111], v17, s[40:43], s86 offen offset:1024 sc1
s_waitcnt vmcnt(44) lgkmcnt(4)
MA 142,176,54
RT 66,23,16384
s_add_u32 s86, s86, 4096
MA 146,180,54
RT 68,23,17408
WL 4
MA 150,176,58
RT 70,24,16384
MA 154,180,58
RT 72,24,17408
buffer_load_dwordx4 a[112:115], v17, s[40:43], s86 offen offset:0 sc1
WL 4
MA 158,176,62
RT 74,25,16384
MA 162,180,62
RT 76,25,17408
WL 4
MA 166,176,66
RT 54,26,16384
MA 170,180,66
RT 56,26,17408
buffer_load_dwordx4 a[116:119], v17, s[40:43], s86 offen offset:1024 sc1
WL 4
MA 174,176,70
RT 58,27,16384
s_add_u32 s86, s86, 4096
MA 178,180,70
RT 60,27,17408
WL 4
MA 182,176,74
RT 62,20,24576
MA 186,180,74
RT 64,20,25600
WL 4
MA 190,176,54
RT 66,21,24576
MA 194,180,54
RT 68,21,25600
WL 4
MA 198,176,58
RT 70,22,24576
MA 202,180,58
RT 72,22,25600
s_waitcnt vmcnt(42) lgkmcnt(4)
MA 142,184,62
RT 74,23,24576
MA 146,188,62
RT 76,23,25600
buffer_load_dwordx4 a[120:123], v17, s[40:43], s86 offen offset:0 sc1
WL 4
MA 150,184,66
RT 54,24,24576
MA 154,188,66
RT 56,24,25600
WL 4
MA 158,184,70
RT 58,25,24576
MA 162,188,70
RT 60,25,25600
buffer_load_dwordx4 a[124:127], v17, s[40:43], s86 offen offset:1024 sc1
WL 4
MA 166,184,74
RT 62,26,24576
s_add_u32 s86, s86, 4096
MA 170,188,74
RT 64,26,25600
WL 4
MA 174,184,54
RT 66,27,24576
MA 178,188,54
RT 68,27,25600
WL 4
MA 182,184,58
s_waitcnt vmcnt(38)
s_barrier
RT 70,20,32768
MA 186,188,58
RT 72,20,33792
WL 4
MA 190,184,62
RT 74,21,32768
MA 194,188,62
RT 76,21,33792
WL 4
MA 198,184,66
RT 54,22,32768
MA 202,188,66
RT 56,22,33792
WL 4
MA 142,32,70
RT 58,23,32768
MA 146,36,70
RT 60,23,33792
buffer_load_dwordx4 a[128:131], v16, s[40:43], s63 offen offset:0 sc1
WL 4
MA 150,32,74
RT 62,24,32768
MA 154,36,74
RT 64,24,33792
WL 4
MA 158,32,54
RT 66,25,32768
MA 162,36,54
RT 68,25,33792
buffer_load_dwordx4 a[132:135], v16, s[40:43], s63 offen offset:1024 sc1
WL 4
MA 166,32,58
RT 70,26,32768
s_add_u32 s63, s63, 4096
MA 170,36,58
RT 72,26,33792
WL 4
MA 174,32,62
RT 74,27,32768
MA 178,36,62
RT 76,27,33792
WL 4
MA 182,32,66
RT 54,20,40960
MA 186,36,66
RT 56,20,41984
WL 4
MA 190,32,70
RT 58,21,40960
MA 194,36,70
RT 60,21,41984
WL 4
MA 198,32,74
RT 62,22,40960
MA 202,36,74
RT 64,22,41984
s_waitcnt vmcnt(38) lgkmcnt(4)
MA 142,40,54
RT 66,23,40960
MA 146,44,54
RT 68,23,41984
buffer_load_dwordx4 a[136:139], v16, s[40:43], s63 offen offset:0 sc1
WL 4
MA 150,40,58
RT 70,24,40960
MA 154,44,58
RT 72,24,41984
WL 4
MA 158,40,62
RT 74,25,40960
MA 162,44,62
RT 76,25,41984
buffer_load_dwordx4 a[140:143], v16, s[40:43], s63 offen offset:1024 sc1
WL 4
MA 166,40,66
RT 54,26,40960
s_add_u32 s63, s63, 4096
MA 170,44,66
RT 56,26,41984
WL 4
MA 174,40,70
RT 58,27,40960
MA 178,44,70
RT 60,27,41984
WL 4
MA 182,40,74
RT 62,20,49152
MA 186,44,74
RT 64,20,50176
WL 4
MA 190,40,54
RT 66,21,49152
MA 194,44,54
RT 68,21,50176
WL 4
MA 198,40,58
RT 70,22,49152
MA 202,44,58
RT 72,22,50176
s_waitcnt vmcnt(36) lgkmcnt(4)
MA 142,48,62
RT 74,23,49152
MA 146,52,62
RT 76,23,50176
buffer_load_dwordx4 a[144:147], v16, s[40:43], s63 offen offset:0 sc1
WL 4
MA 150,48,66
RT 54,24,49152
MA 154,52,66
RT 56,24,50176
WL 4
MA 158,48,70
RT 58,25,49152
MA 162,52,70
RT 60,25,50176
buffer_load_dwordx4 a[148:151], v16, s[40:43], s63 offen offset:1024 sc1
WL 4
MA 166,48,74
RT 62,26,49152
s_add_u32 s63, s63, 4096
MA 170,52,74
RT 64,26,50176
WL 4
MA 174,48,54
RT 66,27,49152
MA 178,52,54
RT 68,27,50176
WL 4
MA 182,48,58
RT 70,20,57344
MA 186,52,58
RT 72,20,58368
WL 4
MA 190,48,62
RT 74,21,57344
MA 194,52,62
RT 76,21,58368
WL 4
MA 198,48,66
RT 54,22,57344
MA 202,52,66
RT 56,22,58368
s_branch L_join_%=
L_slowa_%=:
s_mov_b32 s54, 0x40000
L_spa_%=:
buffer_load_dwordx2 v[44:45], v47, s[92:95], 0 offen offset:0 sc1
s_waitcnt vmcnt(0)
v_cmp_ne_u64 vcc, s[98:99], v[44:45]
s_sub_u32 s54, s54, 1
s_cbranch_scc1 L_spax_%=
s_cmp_eq_u64 vcc, 0
s_cbranch_scc1 L_spax_%=
s_sleep 1
s_branch L_spa_%=
L_spax_%=:
s_branch L_backa_%=
L_slowgB0_%=:
s_mov_b32 s54, 0x40000
L_spgB0_%=:
buffer_load_dwordx2 v[44:45], v47, s[92:95], 0 offen offset:256 sc1
s_waitcnt vmcnt(0)
v_cmp_ne_u64 vcc, s[98:99], v[44:45]
s_sub_u32 s54, s54, 1
s_cbranch_scc1 L_spgB0x_%=
s_cmp_eq_u64 vcc, 0
s_cbranch_scc1 L_spgB0x_%=
s_sleep 1
s_branch L_spgB0_%=
L_spgB0x_%=:
s_branch L_backgB0_%=
L_slowx_%=:
s_mov_b32 s54, 0x40000
L_spx_%=:
buffer_load_dwordx2 v[44:45], v46, s[92:95], 0 offen offset:0 sc1
s_waitcnt vmcnt(0)
v_cmp_ne_u64 vcc, s[98:99], v[44:45]
s_sub_u32 s54, s54, 1
s_cbranch_scc1 L_spxx_%=
s_cmp_eq_u64 vcc, 0
s_cbranch_scc1 L_spxx_%=
s_sleep 1
s_branch L_spx_%=
L_spxx_%=:
buffer_load_dwordx4 a[0:3], v3, s[92:95], s57 offen offset:0 sc1
buffer_load_dwordx4 a[4:7], v3, s[92:95], s57 offen offset:1024 sc1
buffer_load_dwordx4 a[8:11], v3, s[92:95], s57 offen offset:2048 sc1
buffer_load_dwordx4 a[12:15], v3, s[92:95], s57 offen offset:3072 sc1
s_add_u32 s57, s57, 4096
buffer_load_dwordx4 a[16:19], v3, s[92:95], s57 offen offset:0 sc1
buffer_load_dwordx4 a[20:23], v3, s[92:95], s57 offen offset:1024 sc1
buffer_load_dwordx4 a[24:27], v3, s[92:95], s57 offen offset:2048 sc1
buffer_load_dwordx4 a[28:31], v3, s[92:95], s57 offen offset:3072 sc1
s_branch L_backx_%=
L_slowgX_%=:
s_mov_b32 s54, 0x40000
L_spgX_%=:
buffer_load_dwordx2 v[52:53], v48, s[92:95], 0 offen offset:0 sc1
s_waitcnt vmcnt(0)
v_cmp_ne_u64 vcc, s[98:99], v[52:53]
s_sub_u32 s54, s54, 1
s_cbranch_scc1 L_spgXx_%=
s_cmp_eq_u64 vcc, 0
s_cbranch_scc1 L_spgXx_%=
s_sleep 1
s_branch L_spgX_%=
L_spgXx_%=:
s_branch L_backgX_%=
L_slowgB1_%=:
s_mov_b32 s54, 0x40000
L_spgB1_%=:
buffer_load_dwordx2 v[44:45], v47, s[92:95], 0 offen offset:256 sc1
s_waitcnt vmcnt(0)
v_cmp_ne_u64 vcc, s[98:99], v[44:45]
s_sub_u32 s54, s54, 1
s_cbranch_scc1 L_spgB1x_%=
)"
R"(s_cmp_eq_u64 vcc, 0
s_cbranch_scc1 L_spgB1x_%=
s_sleep 1
s_branch L_spgB1_%=
L_spgB1x_%=:
s_branch L_backgB1_%=
L_slowgB2_%=:
s_mov_b32 s54, 0x40000
L_spgB2_%=:
buffer_load_dwordx2 v[44:45], v47, s[92:95], 0 offen offset:256 sc1
s_waitcnt vmcnt(0)
v_cmp_ne_u64 vcc, s[98:99], v[44:45]
s_sub_u32 s54, s54, 1
s_cbranch_scc1 L_spgB2x_%=
s_cmp_eq_u64 vcc, 0
s_cbranch_scc1 L_spgB2x_%=
s_sleep 1
s_branch L_spgB2_%=
L_spgB2x_%=:
s_branch L_backgB2_%=
L_slowgB3_%=:
s_mov_b32 s54, 0x40000
L_spgB3_%=:
buffer_load_dwordx2 v[44:45], v47, s[92:95], 0 offen offset:256 sc1
s_waitcnt vmcnt(0)
v_cmp_ne_u64 vcc, s[98:99], v[44:45]
s_sub_u32 s54, s54, 1
s_cbranch_scc1 L_spgB3x_%=
s_cmp_eq_u64 vcc, 0
s_cbranch_scc1 L_spgB3x_%=
s_sleep 1
s_branch L_spgB3_%=
L_spgB3x_%=:
s_branch L_backgB3_%=
L_g_%=:
s_and_b32 s24, %1, 7
s_lshr_b32 s25, %1, 3
s_sub_u32 s25, s25, 28
s_lshl_b32 s26, s24, 2
s_add_u32 s26, s26, s7
s_lshl_b32 s56, s7, 14
s_lshl_b32 s27, s25, 9
s_add_u32 s56, s56, s27
s_lshl_b32 s52, s7, 11
v_lshrrev_b32 v50, 4, v1
v_and_b32 v51, 15, v1
s_and_b32 s53, s7, 1
s_lshl_b32 s53, s53, 2
v_or_b32 v52, s53, v50
v_lshlrev_b32 v52, 1, v52
v_xor_b32 v52, v52, v51
v_lshlrev_b32 v53, 11, v50
v_lshl_add_u32 v20, v52, 4, v53
v_add_u32 v21, 7168, v20
v_add_u32 v22, 256, v20
v_add_u32 v23, 256, v21
s_lshl_b32 s66, s26, 17
s_add_u32 s67, s66, 0x10000
s_lshl_b32 s65, s7, 14
s_add_u32 s65, s65, 0x18000
v_lshrrev_b32 v51, 3, v1
v_lshlrev_b32 v51, 11, v51
v_or_b32 v52, 0, v50
v_xor_b32 v52, v52, v1
v_and_b32 v52, 7, v52
v_lshl_add_u32 v16, v52, 4, v51
v_or_b32 v52, 4, v50
v_xor_b32 v52, v52, v1
v_and_b32 v52, 7, v52
v_lshl_add_u32 v17, v52, 4, v51
v_add_u32 v18, 30720, v16
v_add_u32 v19, 46080, v17
v_add_u32 v17, 15360, v17
s_mov_b32 s42, 0x400000
s_mov_b32 s43, 0x20000
s_mov_b32 s46, 0x200000
s_mov_b32 s47, 0x20000
s_mov_b32 s31, 0x3020000
WL 0
s_mov_b64 s[40:41], s[8:9]
s_mov_b64 s[44:45], s[10:11]
s_add_u32 m0, s52, 0
s_nop 0
buffer_load_dwordx4 v20, s[44:47], s56 offen offset:0 lds
buffer_load_dwordx4 v21, s[44:47], s56 offen offset:1024 lds
s_add_u32 m0, s52, 8192
s_nop 0
buffer_load_dwordx4 v22, s[44:47], s56 offen offset:0 lds
buffer_load_dwordx4 v23, s[44:47], s56 offen offset:1024 lds
s_add_u32 s56, s56, 65536
s_add_u32 m0, s65, 0
s_nop 0
buffer_load_dwordx4 v16, s[40:43], s66 offen offset:0 lds
buffer_load_dwordx4 v17, s[40:43], s66 offen offset:1024 lds
buffer_load_dwordx4 v18, s[40:43], s66 offen offset:2048 lds
buffer_load_dwordx4 v19, s[40:43], s66 offen offset:3072 lds
s_add_u32 s66, s66, 128
s_load_dwordx2 s[98:99], s[20:21], s31
s_add_u32 m0, s65, 4096
s_nop 0
buffer_load_dwordx4 v16, s[40:43], s67 offen offset:0 lds
buffer_load_dwordx4 v17, s[40:43], s67 offen offset:1024 lds
buffer_load_dwordx4 v18, s[40:43], s67 offen offset:2048 lds
buffer_load_dwordx4 v19, s[40:43], s67 offen offset:3072 lds
s_add_u32 s67, s67, 128
s_add_u32 m0, s52, 16384
s_nop 0
buffer_load_dwordx4 v20, s[44:47], s56 offen offset:0 lds
buffer_load_dwordx4 v21, s[44:47], s56 offen offset:1024 lds
s_add_u32 m0, s52, 24576
s_nop 0
buffer_load_dwordx4 v22, s[44:47], s56 offen offset:0 lds
buffer_load_dwordx4 v23, s[44:47], s56 offen offset:1024 lds
s_add_u32 s56, s56, 65536
v_lshrrev_b32 v50, 4, v1
v_bfe_u32 v51, v1, 2, 2
v_and_b32 v52, 1, v50
v_lshlrev_b32 v52, 2, v52
v_or_b32 v52, v52, v51
v_lshlrev_b32 v53, 3, v50
v_add_u32 v53, v53, v51
v_lshlrev_b32 v53, 8, v53
v_and_b32 v54, 3, v1
v_lshlrev_b32 v54, 3, v54
v_add_u32 v53, v53, v54
v_add_u32 v53, 0, v53
v_xor_b32 v54, 0, v52
v_lshlrev_b32 v54, 5, v54
v_add_u32 v24, v53, v54
v_xor_b32 v54, 1, v52
v_lshlrev_b32 v54, 5, v54
v_add_u32 v25, v53, v54
v_xor_b32 v54, 2, v52
v_lshlrev_b32 v54, 5, v54
v_add_u32 v26, v53, v54
v_xor_b32 v54, 3, v52
v_lshlrev_b32 v54, 5, v54
v_add_u32 v27, v53, v54
v_xor_b32 v54, 4, v52
v_lshlrev_b32 v54, 5, v54
v_add_u32 v28, v53, v54
v_xor_b32 v54, 5, v52
v_lshlrev_b32 v54, 5, v54
v_add_u32 v29, v53, v54
v_xor_b32 v54, 6, v52
v_lshlrev_b32 v54, 5, v54
v_add_u32 v30, v53, v54
v_xor_b32 v54, 7, v52
v_lshlrev_b32 v54, 5, v54
v_add_u32 v31, v53, v54
v_add_u32 v32, 65536, v24
v_add_u32 v33, 65536, v25
v_add_u32 v34, 65536, v26
v_add_u32 v35, 65536, v27
v_add_u32 v36, 65536, v28
v_add_u32 v37, 65536, v29
v_add_u32 v38, 65536, v30
v_add_u32 v39, 65536, v31
v_and_b32 v51, 15, v1
v_lshrrev_b32 v52, 1, v51
v_lshrrev_b32 v50, 4, v1
v_xor_b32 v52, v52, v50
v_lshlrev_b32 v52, 4, v52
v_lshlrev_b32 v51, 7, v51
v_add3_u32 v40, v51, v52, s65
v_xor_b32 v41, 64, v40
s_lshl_b32 s30, s25, 2
s_add_u32 s30, s30, s7
s_and_b32 s53, s24, 1
s_mul_i32 s53, s53, 448
s_add_u32 s53, s53, s30
s_mul_i32 s53, s53, 28672
s_lshr_b32 s55, s24, 1
s_mul_i32 s55, s55, 1792
s_add_u32 s53, s53, s55
v_mul_u32_u24 v50, 37, v1
v_lshrrev_b32 v50, 9, v50
v_mul_u32_u24 v51, 5376, v50
v_lshl_add_u32 v51, v1, 7, v51
v_add_u32 v51, s53, v51
v_cmp_gt_u32 vcc, 56, v1
v_bfrev_b32 v50, 1
s_nop 1
v_cndmask_b32 v48, v50, v51, vcc
s_mov_b32 s60, s18
s_mov_b32 s61, s19
s_mov_b32 s62, 0x1880000
s_mov_b32 s63, 0x20000
s_mov_b32 s57, 1376256
s_add_u32 s48, s20, 0x800000
s_addc_u32 s49, s21, 0
s_mov_b32 s50, 0x400000
s_mov_b32 s51, 0x20000
s_add_u32 s92, s20, 0x2000000
s_addc_u32 s93, s21, 0
s_mov_b32 s94, 0x1040000
s_mov_b32 s95, 0x20000
s_lshl_b32 s64, s26, 2
s_add_u32 s64, s64, s25
s_lshl_b32 s64, s64, 15
s_lshr_b32 s58, s26, 1
s_lshl_b32 s58, s58, 8
s_lshl_b32 s27, s25, 4
s_add_u32 s58, s58, s27
s_and_b32 s27, s7, 1
s_lshl_b32 s27, s27, 3
s_add_u32 s58, s58, s27
s_add_u32 s58, s58, 0x1001000
WL 0
s_add_u32 s98, s98, 1
s_addc_u32 s99, s99, 0
v_mov_b32 v44, s98
v_mov_b32 v45, s99
s_mov_b32 s68, s64
s_waitcnt vmcnt(4)
ds_read_b128 a[128:131], v40 offset:0
ds_read_b128 a[132:135], v40 offset:2048
ds_read_b128 a[136:139], v40 offset:4096
ds_read_b128 a[140:143], v40 offset:6144
ds_read_b128 a[144:147], v41 offset:0
ds_read_b128 a[148:151], v41 offset:2048
ds_read_b128 a[152:155], v41 offset:4096
ds_read_b128 a[156:159], v41 offset:6144
s_barrier
RT 50,24,0
RT 52,24,1024
RT 54,25,0
RT 56,25,1024
RT 58,26,0
RT 60,26,1024
WL 4
v_mfma_f32_16x16x32_bf16 v[82:85], v[50:53], a[128:131], 0
RT 62,27,0
v_mfma_f32_16x16x32_bf16 v[146:149], v[50:53], a[132:135], 0
RT 64,27,1024
s_add_u32 m0, s52, 32768
v_mfma_f32_16x16x32_bf16 a[0:3], v[50:53], a[136:139], 0
buffer_load_dwordx4 v20, s[44:47], s56 offen offset:0 lds
v_mfma_f32_16x16x32_bf16 a[64:67], v[50:53], a[140:143], 0
WL 4
v_mfma_f32_16x16x32_bf16 v[86:89], v[54:57], a[128:131], 0
RT 66,28,0
v_mfma_f32_16x16x32_bf16 v[150:153], v[54:57], a[132:135], 0
RT 68,28,1024
v_mfma_f32_16x16x32_bf16 a[4:7], v[54:57], a[136:139], 0
buffer_load_dwordx4 v21, s[44:47], s56 offen offset:1024 lds
v_mfma_f32_16x16x32_bf16 a[68:71], v[54:57], a[140:143], 0
WL 4
v_mfma_f32_16x16x32_bf16 v[90:93], v[58:61], a[128:131], 0
RT 70,29,0
v_mfma_f32_16x16x32_bf16 v[154:157], v[58:61], a[132:135], 0
RT 72,29,1024
s_add_u32 m0, s52, 40960
v_mfma_f32_16x16x32_bf16 a[8:11], v[58:61], a[136:139], 0
buffer_load_dwordx4 v22, s[44:47], s56 offen offset:0 lds
v_mfma_f32_16x16x32_bf16 a[72:75], v[58:61], a[140:143], 0
WL 4
v_mfma_f32_16x16x32_bf16 v[94:97], v[62:65], a[128:131], 0
RT 74,30,0
v_mfma_f32_16x16x32_bf16 v[158:161], v[62:65], a[132:135], 0
RT 76,30,1024
v_mfma_f32_16x16x32_bf16 a[12:15], v[62:65], a[136:139], 0
buffer_load_dwordx4 v23, s[44:47], s56 offen offset:1024 lds
v_mfma_f32_16x16x32_bf16 a[76:79], v[62:65], a[140:143], 0
s_add_u32 s56, s56, 65536
WL 4
v_mfma_f32_16x16x32_bf16 v[98:101], v[66:69], a[128:131], 0
RT 78,31,0
v_mfma_f32_16x16x32_bf16 v[162:165], v[66:69], a[132:135], 0
RT 80,31,1024
s_add_u32 m0, s65, 8192
v_mfma_f32_16x16x32_bf16 a[16:19], v[66:69], a[136:139], 0
buffer_load_dwordx4 v16, s[40:43], s66 offen offset:0 lds
v_mfma_f32_16x16x32_bf16 a[80:83], v[66:69], a[140:143], 0
WL 4
v_mfma_f32_16x16x32_bf16 v[102:105], v[70:73], a[128:131], 0
RT 50,24,8192
v_mfma_f32_16x16x32_bf16 v[166:169], v[70:73], a[132:135], 0
RT 52,24,9216
v_mfma_f32_16x16x32_bf16 a[20:23], v[70:73], a[136:139], 0
buffer_load_dwordx4 v17, s[40:43], s66 offen offset:1024 lds
v_mfma_f32_16x16x32_bf16 a[84:87], v[70:73], a[140:143], 0
WL 4
v_mfma_f32_16x16x32_bf16 v[106:109], v[74:77], a[128:131], 0
RT 54,25,8192
v_mfma_f32_16x16x32_bf16 v[170:173], v[74:77], a[132:135], 0
RT 56,25,9216
v_mfma_f32_16x16x32_bf16 a[24:27], v[74:77], a[136:139], 0
buffer_load_dwordx4 v18, s[40:43], s66 offen offset:2048 lds
v_mfma_f32_16x16x32_bf16 a[88:91], v[74:77], a[140:143], 0
WL 4
v_mfma_f32_16x16x32_bf16 v[110:113], v[78:81], a[128:131], 0
RT 58,26,8192
v_mfma_f32_16x16x32_bf16 v[174:177], v[78:81], a[132:135], 0
RT 60,26,9216
v_mfma_f32_16x16x32_bf16 a[28:31], v[78:81], a[136:139], 0
buffer_load_dwordx4 v19, s[40:43], s66 offen offset:3072 lds
v_mfma_f32_16x16x32_bf16 a[92:95], v[78:81], a[140:143], 0
s_add_u32 s66, s66, 128
WL 4
v_mfma_f32_16x16x32_bf16 v[114:117], v[50:53], a[128:131], 0
RT 62,27,8192
v_mfma_f32_16x16x32_bf16 v[178:181], v[50:53], a[132:135], 0
RT 64,27,9216
s_add_u32 m0, s65, 12288
v_mfma_f32_16x16x32_bf16 a[32:35], v[50:53], a[136:139], 0
buffer_load_dwordx4 v16, s[40:43], s67 offen offset:0 lds
v_mfma_f32_16x16x32_bf16 a[96:99], v[50:53], a[140:143], 0
WL 4
v_mfma_f32_16x16x32_bf16 v[118:121], v[54:57], a[128:131], 0
RT 66,28,8192
v_mfma_f32_16x16x32_bf16 v[182:185], v[54:57], a[132:135], 0
RT 68,28,9216
v_mfma_f32_16x16x32_bf16 a[36:39], v[54:57], a[136:139], 0
buffer_load_dwordx4 v17, s[40:43], s67 offen offset:1024 lds
v_mfma_f32_16x16x32_bf16 a[100:103], v[54:57], a[140:143], 0
WL 4
v_mfma_f32_16x16x32_bf16 v[122:125], v[58:61], a[128:131], 0
RT 70,29,8192
v_mfma_f32_16x16x32_bf16 v[186:189], v[58:61], a[132:135], 0
RT 72,29,9216
v_mfma_f32_16x16x32_bf16 a[40:43], v[58:61], a[136:139], 0
buffer_load_dwordx4 v18, s[40:43], s67 offen offset:2048 lds
v_mfma_f32_16x16x32_bf16 a[104:107], v[58:61], a[140:143], 0
WL 4
v_mfma_f32_16x16x32_bf16 v[126:129], v[62:65], a[128:131], 0
RT 74,30,8192
v_mfma_f32_16x16x32_bf16 v[190:193], v[62:65], a[132:135], 0
RT 76,30,9216
v_mfma_f32_16x16x32_bf16 a[44:47], v[62:65], a[136:139], 0
buffer_load_dwordx4 v19, s[40:43], s67 offen offset:3072 lds
v_mfma_f32_16x16x32_bf16 a[108:111], v[62:65], a[140:143], 0
s_add_u32 s67, s67, 128
WL 4
v_mfma_f32_16x16x32_bf16 v[130:133], v[66:69], a[128:131], 0
RT 78,31,8192
v_mfma_f32_16x16x32_bf16 v[194:197], v[66:69], a[132:135], 0
RT 80,31,9216
s_add_u32 m0, s52, 49152
v_mfma_f32_16x16x32_bf16 a[48:51], v[66:69], a[136:139], 0
buffer_load_dwordx4 v20, s[44:47], s56 offen offset:0 lds
v_mfma_f32_16x16x32_bf16 a[112:115], v[66:69], a[140:143], 0
WL 4
v_mfma_f32_16x16x32_bf16 v[134:137], v[70:73], a[128:131], 0
s_waitcnt vmcnt(13)
s_barrier
RT 50,24,16384
v_mfma_f32_16x16x32_bf16 v[198:201], v[70:73], a[132:135], 0
RT 52,24,17408
v_mfma_f32_16x16x32_bf16 a[52:55], v[70:73], a[136:139], 0
buffer_load_dwordx4 v21, s[44:47], s56 offen offset:1024 lds
v_mfma_f32_16x16x32_bf16 a[116:119], v[70:73], a[140:143], 0
WL 4
v_mfma_f32_16x16x32_bf16 v[138:141], v[74:77], a[128:131], 0
RT 54,25,16384
v_mfma_f32_16x16x32_bf16 v[202:205], v[74:77], a[132:135], 0
RT 56,25,17408
s_add_u32 m0, s52, 57344
v_mfma_f32_16x16x32_bf16 a[56:59], v[74:77], a[136:139], 0
buffer_load_dwordx4 v22, s[44:47], s56 offen offset:0 lds
v_mfma_f32_16x16x32_bf16 a[120:123], v[74:77], a[140:143], 0
WL 4
v_mfma_f32_16x16x32_bf16 v[142:145], v[78:81], a[128:131], 0
RT 58,26,16384
v_mfma_f32_16x16x32_bf16 v[206:209], v[78:81], a[132:135], 0
RT 60,26,17408
v_mfma_f32_16x16x32_bf16 a[60:63], v[78:81], a[136:139], 0
buffer_load_dwordx4 v23, s[44:47], s56 offen offset:1024 lds
v_mfma_f32_16x16x32_bf16 a[124:127], v[78:81], a[140:143], 0
s_add_u32 s56, s56, 65536
WL 4
MV 82,50,144
RT 62,27,16384
MV 146,50,148
RT 64,27,17408
s_add_u32 m0, s65, 0
MC 0,50,152
buffer_load_dwordx4 v16, s[40:43], s66 offen offset:0 lds
MC 64,50,156
WL 4
MV 86,54,144
RT 66,28,16384
MV 150,54,148
RT 68,28,17408
MC 4,54,152
buffer_load_dwordx4 v17, s[40:43], s66 offen offset:1024 lds
MC 68,54,156
WL 4
MV 90,58,144
RT 70,29,16384
MV 154,58,148
RT 72,29,17408
MC 8,58,152
buffer_load_dwordx4 v18, s[40:43], s66 offen offset:2048 lds
MC 72,58,156
WL 4
MV 94,62,144
RT 74,30,16384
MV 158,62,148
RT 76,30,17408
)"
R"(MC 12,62,152
buffer_load_dwordx4 v19, s[40:43], s66 offen offset:3072 lds
MC 76,62,156
s_add_u32 s66, s66, 128
WL 4
MV 98,66,144
RT 78,31,16384
MV 162,66,148
RT 80,31,17408
s_add_u32 m0, s65, 4096
MC 16,66,152
buffer_load_dwordx4 v16, s[40:43], s67 offen offset:0 lds
MC 80,66,156
WL 4
MV 102,70,144
RT 50,24,24576
MV 166,70,148
RT 52,24,25600
MC 20,70,152
buffer_load_dwordx4 v17, s[40:43], s67 offen offset:1024 lds
MC 84,70,156
WL 4
MV 106,74,144
RT 54,25,24576
MV 170,74,148
RT 56,25,25600
MC 24,74,152
buffer_load_dwordx4 v18, s[40:43], s67 offen offset:2048 lds
MC 88,74,156
WL 4
MV 110,78,144
RT 58,26,24576
MV 174,78,148
RT 60,26,25600
MC 28,78,152
buffer_load_dwordx4 v19, s[40:43], s67 offen offset:3072 lds
MC 92,78,156
s_add_u32 s67, s67, 128
s_waitcnt vmcnt(12)
ds_read_b128 a[160:163], v40 offset:8192
WL 5
MV 114,50,144
RT 62,27,24576
MV 178,50,148
RT 64,27,25600
MC 32,50,152
MC 96,50,156
ds_read_b128 a[164:167], v40 offset:10240
WL 6
MV 118,54,144
RT 66,28,24576
MV 182,54,148
RT 68,28,25600
MC 36,54,152
MC 100,54,156
ds_read_b128 a[168:171], v40 offset:12288
WL 7
MV 122,58,144
RT 70,29,24576
MV 186,58,148
RT 72,29,25600
MC 40,58,152
MC 104,58,156
ds_read_b128 a[172:175], v40 offset:14336
WL 7
MV 126,62,144
RT 74,30,24576
MV 190,62,148
RT 76,30,25600
MC 44,62,152
MC 108,62,156
ds_read_b128 a[176:179], v41 offset:8192
WL 7
MV 130,66,144
RT 78,31,24576
MV 194,66,148
RT 80,31,25600
MC 48,66,152
MC 112,66,156
ds_read_b128 a[180:183], v41 offset:10240
WL 7
MV 134,70,144
s_barrier
RT 50,24,32768
MV 198,70,148
RT 52,24,33792
s_add_u32 m0, s52, 0
MC 52,70,152
buffer_load_dwordx4 v20, s[44:47], s56 offen offset:0 lds
MC 116,70,156
ds_read_b128 a[184:187], v41 offset:12288
WL 7
MV 138,74,144
RT 54,25,32768
MV 202,74,148
RT 56,25,33792
MC 56,74,152
buffer_load_dwordx4 v21, s[44:47], s56 offen offset:1024 lds
MC 120,74,156
ds_read_b128 a[188:191], v41 offset:14336
WL 7
MV 142,78,144
RT 58,26,32768
MV 206,78,148
RT 60,26,33792
s_add_u32 m0, s52, 8192
MC 60,78,152
buffer_load_dwordx4 v22, s[44:47], s56 offen offset:0 lds
MC 124,78,156
WL 2
MV 82,50,160
RT 62,27,32768
MV 146,50,164
RT 64,27,33792
MC 0,50,168
buffer_load_dwordx4 v23, s[44:47], s56 offen offset:1024 lds
MC 64,50,172
s_add_u32 s56, s56, 65536
MV 86,54,160
RT 66,28,32768
MV 150,54,164
RT 68,28,33792
s_add_u32 m0, s65, 8192
MC 4,54,168
buffer_load_dwordx4 v16, s[40:43], s66 offen offset:0 lds
MC 68,54,172
WL 4
MV 90,58,160
RT 70,29,32768
MV 154,58,164
RT 72,29,33792
MC 8,58,168
buffer_load_dwordx4 v17, s[40:43], s66 offen offset:1024 lds
MC 72,58,172
WL 4
MV 94,62,160
RT 74,30,32768
MV 158,62,164
RT 76,30,33792
MC 12,62,168
buffer_load_dwordx4 v18, s[40:43], s66 offen offset:2048 lds
MC 76,62,172
WL 4
MV 98,66,160
RT 78,31,32768
MV 162,66,164
RT 80,31,33792
MC 16,66,168
buffer_load_dwordx4 v19, s[40:43], s66 offen offset:3072 lds
MC 80,66,172
s_add_u32 s66, s66, 128
WL 4
MV 102,70,160
RT 50,24,40960
MV 166,70,164
RT 52,24,41984
s_add_u32 m0, s65, 12288
MC 20,70,168
buffer_load_dwordx4 v16, s[40:43], s67 offen offset:0 lds
MC 84,70,172
WL 4
MV 106,74,160
RT 54,25,40960
MV 170,74,164
RT 56,25,41984
MC 24,74,168
buffer_load_dwordx4 v17, s[40:43], s67 offen offset:1024 lds
MC 88,74,172
WL 4
MV 110,78,160
RT 58,26,40960
MV 174,78,164
RT 60,26,41984
MC 28,78,168
buffer_load_dwordx4 v18, s[40:43], s67 offen offset:2048 lds
MC 92,78,172
WL 4
MV 114,50,160
RT 62,27,40960
MV 178,50,164
RT 64,27,41984
MC 32,50,168
buffer_load_dwordx4 v19, s[40:43], s67 offen offset:3072 lds
MC 96,50,172
s_add_u32 s67, s67, 128
WL 4
MV 118,54,160
RT 66,28,40960
MV 182,54,164
RT 68,28,41984
MC 36,54,168
MC 100,54,172
WL 4
MV 122,58,160
RT 70,29,40960
MV 186,58,164
RT 72,29,41984
MC 40,58,168
MC 104,58,172
WL 4
MV 126,62,160
RT 74,30,40960
MV 190,62,164
RT 76,30,41984
MC 44,62,168
MC 108,62,172
WL 4
MV 130,66,160
RT 78,31,40960
MV 194,66,164
RT 80,31,41984
MC 48,66,168
MC 112,66,172
WL 4
MV 134,70,160
s_waitcnt vmcnt(20)
s_barrier
RT 50,24,49152
MV 198,70,164
RT 52,24,50176
s_add_u32 m0, s52, 16384
MC 52,70,168
buffer_load_dwordx4 v20, s[44:47], s56 offen offset:0 lds
MC 116,70,172
WL 4
MV 138,74,160
RT 54,25,49152
MV 202,74,164
RT 56,25,50176
MC 56,74,168
buffer_load_dwordx4 v21, s[44:47], s56 offen offset:1024 lds
MC 120,74,172
WL 4
MV 142,78,160
RT 58,26,49152
MV 206,78,164
RT 60,26,50176
s_add_u32 m0, s52, 24576
MC 60,78,168
buffer_load_dwordx4 v22, s[44:47], s56 offen offset:0 lds
MC 124,78,172
WL 4
MV 82,50,176
RT 62,27,49152
MV 146,50,180
RT 64,27,50176
MC 0,50,184
buffer_load_dwordx4 v23, s[44:47], s56 offen offset:1024 lds
MC 64,50,188
s_add_u32 s56, s56, 65536
WL 4
MV 86,54,176
RT 66,28,49152
MV 150,54,180
RT 68,28,50176
MC 4,54,184
MC 68,54,188
WL 4
MV 90,58,176
RT 70,29,49152
MV 154,58,180
RT 72,29,50176
MC 8,58,184
MC 72,58,188
WL 4
MV 94,62,176
RT 74,30,49152
MV 158,62,180
RT 76,30,50176
MC 12,62,184
MC 76,62,188
WL 4
MV 98,66,176
RT 78,31,49152
MV 162,66,180
RT 80,31,50176
MC 16,66,184
MC 80,66,188
WL 4
MV 102,70,176
RT 50,24,57344
MV 166,70,180
RT 52,24,58368
MC 20,70,184
MC 84,70,188
WL 4
MV 106,74,176
RT 54,25,57344
MV 170,74,180
RT 56,25,58368
MC 24,74,184
MC 88,74,188
WL 4
MV 110,78,176
RT 58,26,57344
MV 174,78,180
RT 60,26,58368
MC 28,78,184
MC 92,78,188
s_waitcnt vmcnt(16)
ds_read_b128 a[128:131], v40 offset:0
WL 5
MV 114,50,176
RT 62,27,57344
MV 178,50,180
RT 64,27,58368
MC 32,50,184
MC 96,50,188
ds_read_b128 a[132:135], v40 offset:2048
WL 6
MV 118,54,176
RT 66,28,57344
MV 182,54,180
RT 68,28,58368
MC 36,54,184
MC 100,54,188
ds_read_b128 a[136:139], v40 offset:4096
WL 7
MV 122,58,176
RT 70,29,57344
MV 186,58,180
RT 72,29,58368
MC 40,58,184
MC 104,58,188
ds_read_b128 a[140:143], v40 offset:6144
WL 7
MV 126,62,176
RT 74,30,57344
MV 190,62,180
RT 76,30,58368
MC 44,62,184
MC 108,62,188
ds_read_b128 a[144:147], v41 offset:0
WL 7
MV 130,66,176
RT 78,31,57344
MV 194,66,180
RT 80,31,58368
MC 48,66,184
MC 112,66,188
ds_read_b128 a[148:151], v41 offset:2048
WL 7
MV 134,70,176
s_waitcnt vmcnt(12)
s_barrier
RT 50,24,0
MV 198,70,180
RT 52,24,1024
s_add_u32 m0, s52, 32768
MC 52,70,184
buffer_load_dwordx4 v20, s[44:47], s56 offen offset:0 lds
MC 116,70,188
ds_read_b128 a[152:155], v41 offset:4096
WL 7
MV 138,74,176
RT 54,25,0
MV 202,74,180
RT 56,25,1024
MC 56,74,184
buffer_load_dwordx4 v21, s[44:47], s56 offen offset:1024 lds
MC 120,74,188
ds_read_b128 a[156:159], v41 offset:6144
WL 7
MV 142,78,176
RT 58,26,0
MV 206,78,180
RT 60,26,1024
s_add_u32 m0, s52, 40960
MC 60,78,184
buffer_load_dwordx4 v22, s[44:47], s56 offen offset:0 lds
MC 124,78,188
s_mov_b32 s69, 6
L_groll_%=:
WL 2
MV 82,50,128
RT 62,27,0
MV 146,50,132
RT 64,27,1024
MC 0,50,136
buffer_load_dwordx4 v23, s[44:47], s56 offen offset:1024 lds
MC 64,50,140
s_add_u32 s56, s56, 65536
MV 86,54,128
RT 66,28,0
MV 150,54,132
RT 68,28,1024
s_add_u32 m0, s65, 0
MC 4,54,136
buffer_load_dwordx4 v16, s[40:43], s66 offen offset:0 lds
MC 68,54,140
WL 4
MV 90,58,128
RT 70,29,0
MV 154,58,132
RT 72,29,1024
MC 8,58,136
buffer_load_dwordx4 v17, s[40:43], s66 offen offset:1024 lds
MC 72,58,140
WL 4
MV 94,62,128
)"
R"(RT 74,30,0
MV 158,62,132
RT 76,30,1024
MC 12,62,136
buffer_load_dwordx4 v18, s[40:43], s66 offen offset:2048 lds
MC 76,62,140
WL 4
MV 98,66,128
RT 78,31,0
MV 162,66,132
RT 80,31,1024
MC 16,66,136
buffer_load_dwordx4 v19, s[40:43], s66 offen offset:3072 lds
MC 80,66,140
s_add_u32 s66, s66, 128
WL 4
MV 102,70,128
RT 50,24,8192
MV 166,70,132
RT 52,24,9216
s_add_u32 m0, s65, 4096
MC 20,70,136
buffer_load_dwordx4 v16, s[40:43], s67 offen offset:0 lds
MC 84,70,140
WL 4
MV 106,74,128
RT 54,25,8192
MV 170,74,132
RT 56,25,9216
MC 24,74,136
buffer_load_dwordx4 v17, s[40:43], s67 offen offset:1024 lds
MC 88,74,140
WL 4
MV 110,78,128
RT 58,26,8192
MV 174,78,132
RT 60,26,9216
MC 28,78,136
buffer_load_dwordx4 v18, s[40:43], s67 offen offset:2048 lds
MC 92,78,140
WL 4
MV 114,50,128
RT 62,27,8192
MV 178,50,132
RT 64,27,9216
MC 32,50,136
buffer_load_dwordx4 v19, s[40:43], s67 offen offset:3072 lds
MC 96,50,140
s_add_u32 s67, s67, 128
WL 4
MV 118,54,128
RT 66,28,8192
MV 182,54,132
RT 68,28,9216
MC 36,54,136
MC 100,54,140
WL 4
MV 122,58,128
RT 70,29,8192
MV 186,58,132
RT 72,29,9216
MC 40,58,136
MC 104,58,140
WL 4
MV 126,62,128
RT 74,30,8192
MV 190,62,132
RT 76,30,9216
MC 44,62,136
MC 108,62,140
WL 4
MV 130,66,128
RT 78,31,8192
MV 194,66,132
RT 80,31,9216
MC 48,66,136
MC 112,66,140
WL 4
MV 134,70,128
s_waitcnt vmcnt(12)
s_barrier
RT 50,24,16384
MV 198,70,132
RT 52,24,17408
s_add_u32 m0, s52, 49152
MC 52,70,136
buffer_load_dwordx4 v20, s[44:47], s56 offen offset:0 lds
MC 116,70,140
WL 4
MV 138,74,128
RT 54,25,16384
MV 202,74,132
RT 56,25,17408
MC 56,74,136
buffer_load_dwordx4 v21, s[44:47], s56 offen offset:1024 lds
MC 120,74,140
WL 4
MV 142,78,128
RT 58,26,16384
MV 206,78,132
RT 60,26,17408
s_add_u32 m0, s52, 57344
MC 60,78,136
buffer_load_dwordx4 v22, s[44:47], s56 offen offset:0 lds
MC 124,78,140
WL 4
MV 82,50,144
RT 62,27,16384
MV 146,50,148
RT 64,27,17408
MC 0,50,152
buffer_load_dwordx4 v23, s[44:47], s56 offen offset:1024 lds
MC 64,50,156
s_add_u32 s56, s56, 65536
WL 4
MV 86,54,144
RT 66,28,16384
MV 150,54,148
RT 68,28,17408
MC 4,54,152
MC 68,54,156
WL 4
MV 90,58,144
RT 70,29,16384
MV 154,58,148
RT 72,29,17408
MC 8,58,152
MC 72,58,156
WL 4
MV 94,62,144
RT 74,30,16384
MV 158,62,148
RT 76,30,17408
MC 12,62,152
MC 76,62,156
WL 4
MV 98,66,144
RT 78,31,16384
MV 162,66,148
RT 80,31,17408
MC 16,66,152
MC 80,66,156
WL 4
MV 102,70,144
RT 50,24,24576
MV 166,70,148
RT 52,24,25600
MC 20,70,152
MC 84,70,156
WL 4
MV 106,74,144
RT 54,25,24576
MV 170,74,148
RT 56,25,25600
MC 24,74,152
MC 88,74,156
WL 4
MV 110,78,144
RT 58,26,24576
MV 174,78,148
RT 60,26,25600
MC 28,78,152
MC 92,78,156
ds_read_b128 a[160:163], v40 offset:8192
WL 5
MV 114,50,144
RT 62,27,24576
MV 178,50,148
RT 64,27,25600
MC 32,50,152
MC 96,50,156
ds_read_b128 a[164:167], v40 offset:10240
WL 6
MV 118,54,144
RT 66,28,24576
MV 182,54,148
RT 68,28,25600
MC 36,54,152
MC 100,54,156
ds_read_b128 a[168:171], v40 offset:12288
WL 7
MV 122,58,144
RT 70,29,24576
MV 186,58,148
RT 72,29,25600
MC 40,58,152
MC 104,58,156
ds_read_b128 a[172:175], v40 offset:14336
WL 7
MV 126,62,144
RT 74,30,24576
MV 190,62,148
RT 76,30,25600
MC 44,62,152
MC 108,62,156
ds_read_b128 a[176:179], v41 offset:8192
WL 7
MV 130,66,144
RT 78,31,24576
MV 194,66,148
RT 80,31,25600
MC 48,66,152
MC 112,66,156
ds_read_b128 a[180:183], v41 offset:10240
WL 7
MV 134,70,144
s_waitcnt vmcnt(12)
s_barrier
RT 50,24,32768
MV 198,70,148
RT 52,24,33792
s_add_u32 m0, s52, 0
MC 52,70,152
buffer_load_dwordx4 v20, s[44:47], s56 offen offset:0 lds
MC 116,70,156
ds_read_b128 a[184:187], v41 offset:12288
WL 7
MV 138,74,144
RT 54,25,32768
MV 202,74,148
RT 56,25,33792
MC 56,74,152
buffer_load_dwordx4 v21, s[44:47], s56 offen offset:1024 lds
MC 120,74,156
ds_read_b128 a[188:191], v41 offset:14336
WL 7
MV 142,78,144
RT 58,26,32768
MV 206,78,148
RT 60,26,33792
s_add_u32 m0, s52, 8192
MC 60,78,152
buffer_load_dwordx4 v22, s[44:47], s56 offen offset:0 lds
MC 124,78,156
WL 2
MV 82,50,160
RT 62,27,32768
MV 146,50,164
RT 64,27,33792
MC 0,50,168
buffer_load_dwordx4 v23, s[44:47], s56 offen offset:1024 lds
MC 64,50,172
s_add_u32 s56, s56, 65536
MV 86,54,160
RT 66,28,32768
MV 150,54,164
RT 68,28,33792
s_add_u32 m0, s65, 8192
MC 4,54,168
buffer_load_dwordx4 v16, s[40:43], s66 offen offset:0 lds
MC 68,54,172
WL 4
MV 90,58,160
RT 70,29,32768
MV 154,58,164
RT 72,29,33792
MC 8,58,168
buffer_load_dwordx4 v17, s[40:43], s66 offen offset:1024 lds
MC 72,58,172
WL 4
MV 94,62,160
RT 74,30,32768
MV 158,62,164
RT 76,30,33792
MC 12,62,168
buffer_load_dwordx4 v18, s[40:43], s66 offen offset:2048 lds
MC 76,62,172
WL 4
MV 98,66,160
RT 78,31,32768
MV 162,66,164
RT 80,31,33792
MC 16,66,168
buffer_load_dwordx4 v19, s[40:43], s66 offen offset:3072 lds
MC 80,66,172
s_add_u32 s66, s66, 128
WL 4
MV 102,70,160
RT 50,24,40960
MV 166,70,164
RT 52,24,41984
s_add_u32 m0, s65, 12288
MC 20,70,168
buffer_load_dwordx4 v16, s[40:43], s67 offen offset:0 lds
MC 84,70,172
WL 4
MV 106,74,160
RT 54,25,40960
MV 170,74,164
RT 56,25,41984
MC 24,74,168
buffer_load_dwordx4 v17, s[40:43], s67 offen offset:1024 lds
MC 88,74,172
WL 4
MV 110,78,160
RT 58,26,40960
MV 174,78,164
RT 60,26,41984
MC 28,78,168
buffer_load_dwordx4 v18, s[40:43], s67 offen offset:2048 lds
MC 92,78,172
WL 4
MV 114,50,160
RT 62,27,40960
MV 178,50,164
RT 64,27,41984
MC 32,50,168
buffer_load_dwordx4 v19, s[40:43], s67 offen offset:3072 lds
MC 96,50,172
s_add_u32 s67, s67, 128
WL 4
MV 118,54,160
RT 66,28,40960
MV 182,54,164
RT 68,28,41984
MC 36,54,168
MC 100,54,172
WL 4
MV 122,58,160
RT 70,29,40960
MV 186,58,164
RT 72,29,41984
MC 40,58,168
MC 104,58,172
WL 4
MV 126,62,160
RT 74,30,40960
MV 190,62,164
RT 76,30,41984
MC 44,62,168
MC 108,62,172
WL 4
MV 130,66,160
RT 78,31,40960
MV 194,66,164
RT 80,31,41984
MC 48,66,168
MC 112,66,172
WL 4
MV 134,70,160
s_waitcnt vmcnt(12)
s_barrier
RT 50,24,49152
MV 198,70,164
RT 52,24,50176
s_add_u32 m0, s52, 16384
MC 52,70,168
buffer_load_dwordx4 v20, s[44:47], s56 offen offset:0 lds
MC 116,70,172
WL 4
MV 138,74,160
RT 54,25,49152
MV 202,74,164
RT 56,25,50176
MC 56,74,168
buffer_load_dwordx4 v21, s[44:47], s56 offen offset:1024 lds
MC 120,74,172
WL 4
MV 142,78,160
RT 58,26,49152
MV 206,78,164
RT 60,26,50176
s_add_u32 m0, s52, 24576
MC 60,78,168
buffer_load_dwordx4 v22, s[44:47], s56 offen offset:0 lds
MC 124,78,172
WL 4
MV 82,50,176
RT 62,27,49152
MV 146,50,180
RT 64,27,50176
MC 0,50,184
buffer_load_dwordx4 v23, s[44:47], s56 offen offset:1024 lds
MC 64,50,188
s_add_u32 s56, s56, 65536
WL 4
MV 86,54,176
RT 66,28,49152
MV 150,54,180
RT 68,28,50176
MC 4,54,184
MC 68,54,188
WL 4
MV 90,58,176
RT 70,29,49152
MV 154,58,180
RT 72,29,50176
MC 8,58,184
MC 72,58,188
WL 4
MV 94,62,176
RT 74,30,49152
MV 158,62,180
RT 76,30,50176
MC 12,62,184
MC 76,62,188
WL 4
MV 98,66,176
RT 78,31,49152
MV 162,66,180
)"
R"(RT 80,31,50176
MC 16,66,184
MC 80,66,188
WL 4
MV 102,70,176
RT 50,24,57344
MV 166,70,180
RT 52,24,58368
MC 20,70,184
MC 84,70,188
WL 4
MV 106,74,176
RT 54,25,57344
MV 170,74,180
RT 56,25,58368
MC 24,74,184
MC 88,74,188
WL 4
MV 110,78,176
RT 58,26,57344
MV 174,78,180
RT 60,26,58368
MC 28,78,184
MC 92,78,188
ds_read_b128 a[128:131], v40 offset:0
WL 5
MV 114,50,176
RT 62,27,57344
MV 178,50,180
RT 64,27,58368
MC 32,50,184
MC 96,50,188
ds_read_b128 a[132:135], v40 offset:2048
WL 6
MV 118,54,176
RT 66,28,57344
MV 182,54,180
RT 68,28,58368
MC 36,54,184
MC 100,54,188
ds_read_b128 a[136:139], v40 offset:4096
WL 7
MV 122,58,176
RT 70,29,57344
MV 186,58,180
RT 72,29,58368
MC 40,58,184
MC 104,58,188
ds_read_b128 a[140:143], v40 offset:6144
WL 7
MV 126,62,176
RT 74,30,57344
MV 190,62,180
RT 76,30,58368
MC 44,62,184
MC 108,62,188
ds_read_b128 a[144:147], v41 offset:0
WL 7
MV 130,66,176
RT 78,31,57344
MV 194,66,180
RT 80,31,58368
MC 48,66,184
MC 112,66,188
ds_read_b128 a[148:151], v41 offset:2048
WL 7
MV 134,70,176
s_waitcnt vmcnt(12)
s_barrier
RT 50,24,0
MV 198,70,180
RT 52,24,1024
s_add_u32 m0, s52, 32768
MC 52,70,184
buffer_load_dwordx4 v20, s[44:47], s56 offen offset:0 lds
MC 116,70,188
ds_read_b128 a[152:155], v41 offset:4096
WL 7
MV 138,74,176
RT 54,25,0
MV 202,74,180
RT 56,25,1024
MC 56,74,184
buffer_load_dwordx4 v21, s[44:47], s56 offen offset:1024 lds
MC 120,74,188
ds_read_b128 a[156:159], v41 offset:6144
WL 7
MV 142,78,176
RT 58,26,0
MV 206,78,180
RT 60,26,1024
s_add_u32 m0, s52, 40960
MC 60,78,184
buffer_load_dwordx4 v22, s[44:47], s56 offen offset:0 lds
MC 124,78,188
s_sub_u32 s69, s69, 1
s_cmp_lg_u32 s69, 0
s_cbranch_scc1 L_groll_%=
WL 2
MC 0,50,136
RT 62,27,0
MC 64,50,140
RT 64,27,1024
MC 4,54,136
RT 66,28,0
MC 68,54,140
RT 68,28,1024
buffer_load_dwordx4 v23, s[44:47], s56 offen offset:1024 lds
WL 4
MC 8,58,136
RT 70,29,0
s_add_u32 s56, s56, 65536
MC 72,58,140
RT 72,29,1024
WL 4
MC 12,62,136
RT 74,30,0
MC 76,62,140
RT 76,30,1024
WL 4
MC 16,66,136
RT 78,31,0
MC 80,66,140
RT 80,31,1024
WL 4
MC 20,70,136
RT 50,24,8192
MC 84,70,140
RT 52,24,9216
WL 4
MC 24,74,136
RT 54,25,8192
MC 88,74,140
RT 56,25,9216
WL 4
MC 28,78,136
RT 58,26,8192
MC 92,78,140
RT 60,26,9216
WL 4
MC 32,50,136
RT 62,27,8192
MC 96,50,140
RT 64,27,9216
WL 4
MC 36,54,136
RT 66,28,8192
MC 100,54,140
RT 68,28,9216
WL 4
MC 40,58,136
RT 70,29,8192
MC 104,58,140
RT 72,29,9216
WL 4
MC 44,62,136
RT 74,30,8192
MC 108,62,140
RT 76,30,9216
WL 4
MC 48,66,136
RT 78,31,8192
MC 112,66,140
RT 80,31,9216
WL 4
MC 52,70,136
s_waitcnt vmcnt(4)
s_barrier
RT 50,24,16384
s_add_u32 m0, s52, 49152
MC 116,70,140
RT 52,24,17408
buffer_load_dwordx4 v20, s[44:47], s56 offen offset:0 lds
WL 4
MC 56,74,136
RT 54,25,16384
MC 120,74,140
RT 56,25,17408
WL 4
MC 60,78,136
RT 58,26,16384
MC 124,78,140
RT 60,26,17408
buffer_load_dwordx4 v21, s[44:47], s56 offen offset:1024 lds
WL 4
MC 0,50,152
RT 62,27,16384
MC 64,50,156
RT 64,27,17408
WL 4
MC 4,54,152
RT 66,28,16384
s_add_u32 m0, s52, 57344
MC 68,54,156
RT 68,28,17408
buffer_load_dwordx4 v22, s[44:47], s56 offen offset:0 lds
WL 4
MC 8,58,152
RT 70,29,16384
MC 72,58,156
RT 72,29,17408
WL 4
MC 12,62,152
RT 74,30,16384
MC 76,62,156
RT 76,30,17408
buffer_load_dwordx4 v23, s[44:47], s56 offen offset:1024 lds
WL 4
MC 16,66,152
RT 78,31,16384
s_add_u32 s56, s56, 65536
MC 80,66,156
RT 80,31,17408
WL 4
MC 20,70,152
RT 50,24,24576
MC 84,70,156
RT 52,24,25600
WL 4
MC 24,74,152
RT 54,25,24576
MC 88,74,156
RT 56,25,25600
WL 4
MC 28,78,152
RT 58,26,24576
MC 92,78,156
RT 60,26,25600
ds_read_b128 a[160:163], v40 offset:8192
WL 5
MC 32,50,152
RT 62,27,24576
MC 96,50,156
RT 64,27,25600
ds_read_b128 a[164:167], v40 offset:10240
WL 6
MC 36,54,152
RT 66,28,24576
MC 100,54,156
RT 68,28,25600
ds_read_b128 a[168:171], v40 offset:12288
WL 7
MC 40,58,152
RT 70,29,24576
MC 104,58,156
RT 72,29,25600
ds_read_b128 a[172:175], v40 offset:14336
WL 7
MC 44,62,152
RT 74,30,24576
MC 108,62,156
RT 76,30,25600
ds_read_b128 a[176:179], v41 offset:8192
WL 7
MC 48,66,152
RT 78,31,24576
MC 112,66,156
RT 80,31,25600
ds_read_b128 a[180:183], v41 offset:10240
WL 7
MC 52,70,152
s_waitcnt vmcnt(4)
s_barrier
RT 50,24,32768
MC 116,70,156
RT 52,24,33792
ds_read_b128 a[184:187], v41 offset:12288
WL 7
MC 56,74,152
RT 54,25,32768
MC 120,74,156
RT 56,25,33792
ds_read_b128 a[188:191], v41 offset:14336
WL 7
MC 60,78,152
RT 58,26,32768
MC 124,78,156
RT 60,26,33792
WL 2
MC 0,50,168
RT 62,27,32768
MC 64,50,172
RT 64,27,33792
MC 4,54,168
RT 66,28,32768
MC 68,54,172
RT 68,28,33792
WL 4
MC 8,58,168
RT 70,29,32768
MC 72,58,172
RT 72,29,33792
WL 4
MC 12,62,168
RT 74,30,32768
MC 76,62,172
RT 76,30,33792
WL 4
MC 16,66,168
RT 78,31,32768
MC 80,66,172
RT 80,31,33792
WL 4
MC 20,70,168
RT 50,24,40960
MC 84,70,172
RT 52,24,41984
WL 4
MC 24,74,168
RT 54,25,40960
MC 88,74,172
RT 56,25,41984
WL 4
MC 28,78,168
RT 58,26,40960
MC 92,78,172
RT 60,26,41984
WL 4
MC 32,50,168
RT 62,27,40960
MC 96,50,172
RT 64,27,41984
WL 4
MC 36,54,168
RT 66,28,40960
MC 100,54,172
RT 68,28,41984
WL 4
MC 40,58,168
RT 70,29,40960
MC 104,58,172
RT 72,29,41984
WL 4
MC 44,62,168
RT 74,30,40960
MC 108,62,172
RT 76,30,41984
WL 4
MC 48,66,168
RT 78,31,40960
MC 112,66,172
RT 80,31,41984
WL 4
MC 52,70,168
s_waitcnt vmcnt(0)
s_barrier
RT 50,24,49152
MC 116,70,172
RT 52,24,50176
WL 4
MC 56,74,168
RT 54,25,49152
MC 120,74,172
RT 56,25,50176
WL 4
MC 60,78,168
RT 58,26,49152
MC 124,78,172
RT 60,26,50176
WL 4
MC 0,50,184
RT 62,27,49152
MC 64,50,188
RT 64,27,50176
WL 4
MC 4,54,184
RT 66,28,49152
MC 68,54,188
RT 68,28,50176
WL 4
MC 8,58,184
RT 70,29,49152
MC 72,58,188
RT 72,29,50176
WL 4
MC 12,62,184
RT 74,30,49152
MC 76,62,188
RT 76,30,50176
WL 4
MC 16,66,184
RT 78,31,49152
MC 80,66,188
RT 80,31,50176
WL 4
MC 20,70,184
RT 50,24,57344
MC 84,70,188
RT 52,24,58368
WL 4
MC 24,74,184
RT 54,25,57344
MC 88,74,188
RT 56,25,58368
WL 4
MC 28,78,184
RT 58,26,57344
MC 92,78,188
RT 60,26,58368
WL 4
)"
R"(MC 32,50,184
RT 62,27,57344
MC 96,50,188
RT 64,27,58368
WL 4
MC 36,54,184
RT 66,28,57344
MC 100,54,188
RT 68,28,58368
WL 4
MC 40,58,184
RT 70,29,57344
MC 104,58,188
RT 72,29,58368
WL 4
MC 44,62,184
RT 74,30,57344
MC 108,62,188
RT 76,30,58368
WL 4
MC 48,66,184
RT 78,31,57344
MC 112,66,188
RT 80,31,58368
WL 4
MC 52,70,184
RT 50,24,0
MC 116,70,188
RT 52,24,1024
WL 4
MC 56,74,184
RT 54,25,0
MC 120,74,188
RT 56,25,1024
WL 4
MC 60,78,184
RT 58,26,0
MC 124,78,188
RT 60,26,1024
WL 4
MV 82,50,128
RT 62,27,0
v_accvgpr_read_b32 v210, a0
v_accvgpr_read_b32 v211, a1
MV 146,50,132
RT 64,27,1024
v_accvgpr_read_b32 v212, a2
v_accvgpr_read_b32 v213, a3
WL 4
MV 86,54,128
RT 66,28,0
v_accvgpr_read_b32 v214, a4
v_accvgpr_read_b32 v215, a5
MV 150,54,132
RT 68,28,1024
v_accvgpr_read_b32 v216, a6
v_accvgpr_read_b32 v217, a7
WL 4
MV 90,58,128
RT 70,29,0
v_cvt_pk_bf16_f32 v226, v210, v211
v_cvt_pk_bf16_f32 v227, v212, v213
MV 154,58,132
RT 72,29,1024
v_cvt_pk_bf16_f32 v228, v214, v215
v_cvt_pk_bf16_f32 v229, v216, v217
WL 4
MV 94,62,128
RT 74,30,0
MV 158,62,132
RT 76,30,1024
buffer_store_dwordx4 v[226:229], v3, s[48:51], s68 offen offset:2048 sc1
v_accvgpr_read_b32 v218, a64
v_accvgpr_read_b32 v219, a65
WL 4
MV 98,66,128
RT 78,31,0
v_accvgpr_read_b32 v220, a66
v_accvgpr_read_b32 v221, a67
MV 162,66,132
RT 80,31,1024
v_accvgpr_read_b32 v222, a68
v_accvgpr_read_b32 v223, a69
WL 4
MV 102,70,128
RT 50,24,8192
v_accvgpr_read_b32 v224, a70
v_accvgpr_read_b32 v225, a71
MV 166,70,132
RT 52,24,9216
v_cvt_pk_bf16_f32 v230, v218, v219
v_cvt_pk_bf16_f32 v231, v220, v221
WL 4
MV 106,74,128
RT 54,25,8192
v_cvt_pk_bf16_f32 v232, v222, v223
v_cvt_pk_bf16_f32 v233, v224, v225
MV 170,74,132
RT 56,25,9216
buffer_store_dwordx4 v[230:233], v3, s[48:51], s68 offen offset:3072 sc1
s_add_u32 s68, s68, 4096
v_accvgpr_read_b32 v210, a8
WL 4
MV 110,78,128
RT 58,26,8192
v_accvgpr_read_b32 v211, a9
v_accvgpr_read_b32 v212, a10
MV 174,78,132
RT 60,26,9216
v_accvgpr_read_b32 v213, a11
v_accvgpr_read_b32 v214, a12
WL 4
MV 114,50,128
RT 62,27,8192
v_accvgpr_read_b32 v215, a13
v_accvgpr_read_b32 v216, a14
MV 178,50,132
RT 64,27,9216
v_accvgpr_read_b32 v217, a15
v_cvt_pk_bf16_f32 v234, v210, v211
WL 4
MV 118,54,128
RT 66,28,8192
v_cvt_pk_bf16_f32 v235, v212, v213
v_cvt_pk_bf16_f32 v236, v214, v215
MV 182,54,132
RT 68,28,9216
v_cvt_pk_bf16_f32 v237, v216, v217
WL 4
MV 122,58,128
RT 70,29,8192
MV 186,58,132
RT 72,29,9216
buffer_store_dwordx4 v[234:237], v3, s[48:51], s68 offen offset:2048 sc1
v_accvgpr_read_b32 v218, a72
v_accvgpr_read_b32 v219, a73
WL 4
MV 126,62,128
RT 74,30,8192
v_accvgpr_read_b32 v220, a74
v_accvgpr_read_b32 v221, a75
MV 190,62,132
RT 76,30,9216
v_accvgpr_read_b32 v222, a76
v_accvgpr_read_b32 v223, a77
WL 4
MV 130,66,128
RT 78,31,8192
v_accvgpr_read_b32 v224, a78
v_accvgpr_read_b32 v225, a79
MV 194,66,132
RT 80,31,9216
v_cvt_pk_bf16_f32 v238, v218, v219
v_cvt_pk_bf16_f32 v239, v220, v221
WL 4
MV 134,70,128
RT 50,24,16384
v_cvt_pk_bf16_f32 v240, v222, v223
v_cvt_pk_bf16_f32 v241, v224, v225
MV 198,70,132
RT 52,24,17408
buffer_store_dwordx4 v[238:241], v3, s[48:51], s68 offen offset:3072 sc1
s_add_u32 s68, s68, 4096
v_accvgpr_read_b32 v210, a16
WL 4
MV 138,74,128
RT 54,25,16384
v_accvgpr_read_b32 v211, a17
v_accvgpr_read_b32 v212, a18
MV 202,74,132
RT 56,25,17408
v_accvgpr_read_b32 v213, a19
v_accvgpr_read_b32 v214, a20
WL 4
MV 142,78,128
RT 58,26,16384
v_accvgpr_read_b32 v215, a21
v_accvgpr_read_b32 v216, a22
MV 206,78,132
RT 60,26,17408
v_accvgpr_read_b32 v217, a23
v_cvt_pk_bf16_f32 v226, v210, v211
WL 4
MV 82,50,144
RT 62,27,16384
v_cvt_pk_bf16_f32 v227, v212, v213
v_cvt_pk_bf16_f32 v228, v214, v215
MV 146,50,148
RT 64,27,17408
v_cvt_pk_bf16_f32 v229, v216, v217
WL 4
MV 86,54,144
RT 66,28,16384
MV 150,54,148
RT 68,28,17408
buffer_store_dwordx4 v[226:229], v3, s[48:51], s68 offen offset:2048 sc1
v_accvgpr_read_b32 v218, a80
v_accvgpr_read_b32 v219, a81
WL 4
MV 90,58,144
RT 70,29,16384
v_accvgpr_read_b32 v220, a82
v_accvgpr_read_b32 v221, a83
MV 154,58,148
RT 72,29,17408
v_accvgpr_read_b32 v222, a84
v_accvgpr_read_b32 v223, a85
WL 4
MV 94,62,144
RT 74,30,16384
v_accvgpr_read_b32 v224, a86
v_accvgpr_read_b32 v225, a87
MV 158,62,148
RT 76,30,17408
v_cvt_pk_bf16_f32 v230, v218, v219
v_cvt_pk_bf16_f32 v231, v220, v221
WL 4
MV 98,66,144
RT 78,31,16384
v_cvt_pk_bf16_f32 v232, v222, v223
v_cvt_pk_bf16_f32 v233, v224, v225
MV 162,66,148
RT 80,31,17408
buffer_store_dwordx4 v[230:233], v3, s[48:51], s68 offen offset:3072 sc1
s_add_u32 s68, s68, 4096
v_accvgpr_read_b32 v210, a24
WL 4
MV 102,70,144
RT 50,24,24576
v_accvgpr_read_b32 v211, a25
v_accvgpr_read_b32 v212, a26
MV 166,70,148
RT 52,24,25600
v_accvgpr_read_b32 v213, a27
v_accvgpr_read_b32 v214, a28
WL 4
MV 106,74,144
RT 54,25,24576
v_accvgpr_read_b32 v215, a29
v_accvgpr_read_b32 v216, a30
MV 170,74,148
RT 56,25,25600
v_accvgpr_read_b32 v217, a31
v_cvt_pk_bf16_f32 v234, v210, v211
WL 4
MV 110,78,144
RT 58,26,24576
v_cvt_pk_bf16_f32 v235, v212, v213
v_cvt_pk_bf16_f32 v236, v214, v215
MV 174,78,148
RT 60,26,25600
v_cvt_pk_bf16_f32 v237, v216, v217
WL 4
MV 114,50,144
RT 62,27,24576
MV 178,50,148
RT 64,27,25600
buffer_store_dwordx4 v[234:237], v3, s[48:51], s68 offen offset:2048 sc1
v_accvgpr_read_b32 v218, a88
v_accvgpr_read_b32 v219, a89
WL 4
MV 118,54,144
RT 66,28,24576
v_accvgpr_read_b32 v220, a90
v_accvgpr_read_b32 v221, a91
MV 182,54,148
RT 68,28,25600
v_accvgpr_read_b32 v222, a92
v_accvgpr_read_b32 v223, a93
WL 4
MV 122,58,144
RT 70,29,24576
v_accvgpr_read_b32 v224, a94
v_accvgpr_read_b32 v225, a95
MV 186,58,148
RT 72,29,25600
v_cvt_pk_bf16_f32 v238, v218, v219
v_cvt_pk_bf16_f32 v239, v220, v221
WL 4
MV 126,62,144
RT 74,30,24576
v_cvt_pk_bf16_f32 v240, v222, v223
v_cvt_pk_bf16_f32 v241, v224, v225
MV 190,62,148
RT 76,30,25600
buffer_store_dwordx4 v[238:241], v3, s[48:51], s68 offen offset:3072 sc1
s_add_u32 s68, s68, 4096
v_accvgpr_read_b32 v210, a32
WL 4
MV 130,66,144
RT 78,31,24576
v_accvgpr_read_b32 v211, a33
v_accvgpr_read_b32 v212, a34
MV 194,66,148
RT 80,31,25600
v_accvgpr_read_b32 v213, a35
v_accvgpr_read_b32 v214, a36
WL 4
MV 134,70,144
RT 50,24,32768
v_accvgpr_read_b32 v215, a37
v_accvgpr_read_b32 v216, a38
MV 198,70,148
RT 52,24,33792
v_accvgpr_read_b32 v217, a39
v_cvt_pk_bf16_f32 v226, v210, v211
WL 4
MV 138,74,144
RT 54,25,32768
v_cvt_pk_bf16_f32 v227, v212, v213
v_cvt_pk_bf16_f32 v228, v214, v215
MV 202,74,148
RT 56,25,33792
v_cvt_pk_bf16_f32 v229, v216, v217
WL 4
MV 142,78,144
RT 58,26,32768
MV 206,78,148
RT 60,26,33792
buffer_store_dwordx4 v[226:229], v3, s[48:51], s68 offen offset:2048 sc1
v_accvgpr_read_b32 v218, a96
v_accvgpr_read_b32 v219, a97
WL 4
MV 82,50,160
RT 62,27,32768
v_accvgpr_read_b32 v220, a98
v_accvgpr_read_b32 v221, a99
MV 146,50,164
RT 64,27,33792
v_accvgpr_read_b32 v222, a100
v_accvgpr_read_b32 v223, a101
WL 4
MV 86,54,160
RT 66,28,32768
v_accvgpr_read_b32 v224, a102
v_accvgpr_read_b32 v225, a103
MV 150,54,164
RT 68,28,33792
v_cvt_pk_bf16_f32 v230, v218, v219
v_cvt_pk_bf16_f32 v231, v220, v221
WL 4
MV 90,58,160
RT 70,29,32768
v_cvt_pk_bf16_f32 v232, v222, v223
v_cvt_pk_bf16_f32 v233, v224, v225
MV 154,58,164
RT 72,29,33792
buffer_store_dwordx4 v[230:233], v3, s[48:51], s68 offen offset:3072 sc1
s_add_u32 s68, s68, 4096
v_accvgpr_read_b32 v210, a40
WL 4
MV 94,62,160
RT 74,30,32768
v_accvgpr_read_b32 v211, a41
v_accvgpr_read_b32 v212, a42
MV 158,62,164
RT 76,30,33792
v_accvgpr_read_b32 v213, a43
v_accvgpr_read_b32 v214, a44
WL 4
MV 98,66,160
RT 78,31,32768
v_accvgpr_read_b32 v215, a45
v_accvgpr_read_b32 v216, a46
MV 162,66,164
RT 80,31,33792
v_accvgpr_read_b32 v217, a47
v_cvt_pk_bf16_f32 v234, v210, v211
WL 4
MV 102,70,160
RT 50,24,40960
v_cvt_pk_bf16_f32 v235, v212, v213
v_cvt_pk_bf16_f32 v236, v214, v215
MV 166,70,164
RT 52,24,41984
v_cvt_pk_bf16_f32 v237, v216, v217
WL 4
MV 106,74,160
RT 54,25,40960
MV 170,74,164
RT 56,25,41984
buffer_store_dwordx4 v[234:237], v3, s[48:51], s68 offen offset:2048 sc1
v_accvgpr_read_b32 v218, a104
v_accvgpr_read_b32 v219, a105
WL 4
MV 110,78,160
RT 58,26,40960
v_accvgpr_read_b32 v220, a106
v_accvgpr_read_b32 v221, a107
MV 174,78,164
RT 60,26,41984
v_accvgpr_read_b32 v222, a108
v_accvgpr_read_b32 v223, a109
WL 4
MV 114,50,160
RT 62,27,40960
v_accvgpr_read_b32 v224, a110
v_accvgpr_read_b32 v225, a111
MV 178,50,164
RT 64,27,41984
)"
R"(v_cvt_pk_bf16_f32 v238, v218, v219
v_cvt_pk_bf16_f32 v239, v220, v221
WL 4
MV 118,54,160
RT 66,28,40960
v_cvt_pk_bf16_f32 v240, v222, v223
v_cvt_pk_bf16_f32 v241, v224, v225
MV 182,54,164
RT 68,28,41984
buffer_store_dwordx4 v[238:241], v3, s[48:51], s68 offen offset:3072 sc1
s_add_u32 s68, s68, 4096
v_accvgpr_read_b32 v210, a48
WL 4
MV 122,58,160
RT 70,29,40960
v_accvgpr_read_b32 v211, a49
v_accvgpr_read_b32 v212, a50
MV 186,58,164
RT 72,29,41984
v_accvgpr_read_b32 v213, a51
v_accvgpr_read_b32 v214, a52
WL 4
MV 126,62,160
RT 74,30,40960
v_accvgpr_read_b32 v215, a53
v_accvgpr_read_b32 v216, a54
MV 190,62,164
RT 76,30,41984
v_accvgpr_read_b32 v217, a55
v_cvt_pk_bf16_f32 v226, v210, v211
WL 4
MV 130,66,160
RT 78,31,40960
v_cvt_pk_bf16_f32 v227, v212, v213
v_cvt_pk_bf16_f32 v228, v214, v215
MV 194,66,164
RT 80,31,41984
v_cvt_pk_bf16_f32 v229, v216, v217
WL 4
MV 134,70,160
RT 50,24,49152
MV 198,70,164
RT 52,24,50176
buffer_store_dwordx4 v[226:229], v3, s[48:51], s68 offen offset:2048 sc1
v_accvgpr_read_b32 v218, a112
v_accvgpr_read_b32 v219, a113
WL 4
MV 138,74,160
RT 54,25,49152
v_accvgpr_read_b32 v220, a114
v_accvgpr_read_b32 v221, a115
MV 202,74,164
RT 56,25,50176
v_accvgpr_read_b32 v222, a116
v_accvgpr_read_b32 v223, a117
WL 4
MV 142,78,160
RT 58,26,49152
v_accvgpr_read_b32 v224, a118
v_accvgpr_read_b32 v225, a119
MV 206,78,164
RT 60,26,50176
v_cvt_pk_bf16_f32 v230, v218, v219
v_cvt_pk_bf16_f32 v231, v220, v221
WL 4
MV 82,50,176
RT 62,27,49152
v_cvt_pk_bf16_f32 v232, v222, v223
v_cvt_pk_bf16_f32 v233, v224, v225
MV 146,50,180
RT 64,27,50176
buffer_store_dwordx4 v[230:233], v3, s[48:51], s68 offen offset:3072 sc1
s_add_u32 s68, s68, 4096
v_accvgpr_read_b32 v210, a56
WL 4
MV 86,54,176
RT 66,28,49152
v_accvgpr_read_b32 v211, a57
v_accvgpr_read_b32 v212, a58
MV 150,54,180
RT 68,28,50176
v_accvgpr_read_b32 v213, a59
v_accvgpr_read_b32 v214, a60
WL 4
MV 90,58,176
RT 70,29,49152
v_accvgpr_read_b32 v215, a61
v_accvgpr_read_b32 v216, a62
MV 154,58,180
RT 72,29,50176
v_accvgpr_read_b32 v217, a63
v_cvt_pk_bf16_f32 v234, v210, v211
WL 4
MV 94,62,176
RT 74,30,49152
v_cvt_pk_bf16_f32 v235, v212, v213
v_cvt_pk_bf16_f32 v236, v214, v215
MV 158,62,180
RT 76,30,50176
v_cvt_pk_bf16_f32 v237, v216, v217
WL 4
MV 98,66,176
RT 78,31,49152
MV 162,66,180
RT 80,31,50176
buffer_store_dwordx4 v[234:237], v3, s[48:51], s68 offen offset:2048 sc1
v_accvgpr_read_b32 v218, a120
v_accvgpr_read_b32 v219, a121
WL 4
MV 102,70,176
RT 50,24,57344
v_accvgpr_read_b32 v220, a122
v_accvgpr_read_b32 v221, a123
MV 166,70,180
RT 52,24,58368
v_accvgpr_read_b32 v222, a124
v_accvgpr_read_b32 v223, a125
WL 4
MV 106,74,176
RT 54,25,57344
v_accvgpr_read_b32 v224, a126
v_accvgpr_read_b32 v225, a127
MV 170,74,180
RT 56,25,58368
v_cvt_pk_bf16_f32 v238, v218, v219
v_cvt_pk_bf16_f32 v239, v220, v221
WL 4
MV 110,78,176
RT 58,26,57344
v_cvt_pk_bf16_f32 v240, v222, v223
v_cvt_pk_bf16_f32 v241, v224, v225
MV 174,78,180
RT 60,26,58368
buffer_store_dwordx4 v[238:241], v3, s[48:51], s68 offen offset:3072 sc1
v_cvt_pk_bf16_f32 v226, v82, v83
v_cvt_pk_bf16_f32 v227, v84, v85
WL 4
MV 114,50,176
RT 62,27,57344
v_cvt_pk_bf16_f32 v228, v86, v87
v_cvt_pk_bf16_f32 v229, v88, v89
MV 178,50,180
RT 64,27,58368
buffer_store_dwordx4 v[226:229], v3, s[48:51], s64 offen offset:0 sc1
v_cvt_pk_bf16_f32 v230, v146, v147
v_cvt_pk_bf16_f32 v231, v148, v149
WL 4
MV 118,54,176
RT 66,28,57344
v_cvt_pk_bf16_f32 v232, v150, v151
v_cvt_pk_bf16_f32 v233, v152, v153
MV 182,54,180
RT 68,28,58368
buffer_store_dwordx4 v[230:233], v3, s[48:51], s64 offen offset:1024 sc1
s_add_u32 s64, s64, 4096
v_cvt_pk_bf16_f32 v234, v90, v91
WL 4
MV 122,58,176
RT 70,29,57344
v_cvt_pk_bf16_f32 v235, v92, v93
v_cvt_pk_bf16_f32 v236, v94, v95
MV 186,58,180
RT 72,29,58368
v_cvt_pk_bf16_f32 v237, v96, v97
WL 4
MV 126,62,176
RT 74,30,57344
MV 190,62,180
RT 76,30,58368
buffer_store_dwordx4 v[234:237], v3, s[48:51], s64 offen offset:0 sc1
v_cvt_pk_bf16_f32 v238, v154, v155
v_cvt_pk_bf16_f32 v239, v156, v157
WL 4
MV 130,66,176
RT 78,31,57344
v_cvt_pk_bf16_f32 v240, v158, v159
v_cvt_pk_bf16_f32 v241, v160, v161
MV 194,66,180
RT 80,31,58368
buffer_store_dwordx4 v[238:241], v3, s[48:51], s64 offen offset:1024 sc1
s_add_u32 s64, s64, 4096
v_cvt_pk_bf16_f32 v226, v98, v99
WL 4
MV 134,70,176
v_cvt_pk_bf16_f32 v227, v100, v101
v_cvt_pk_bf16_f32 v228, v102, v103
MV 198,70,180
v_cvt_pk_bf16_f32 v229, v104, v105
WL 2
MV 138,74,176
MV 202,74,180
buffer_store_dwordx4 v[226:229], v3, s[48:51], s64 offen offset:0 sc1
v_cvt_pk_bf16_f32 v230, v162, v163
v_cvt_pk_bf16_f32 v231, v164, v165
WL 0
MV 142,78,176
v_cvt_pk_bf16_f32 v232, v166, v167
v_cvt_pk_bf16_f32 v233, v168, v169
MV 206,78,180
buffer_store_dwordx4 v[230:233], v3, s[48:51], s64 offen offset:1024 sc1
s_add_u32 s64, s64, 4096
v_cvt_pk_bf16_f32 v234, v106, v107
s_nop 7
s_nop 1
v_cvt_pk_bf16_f32 v235, v108, v109
v_cvt_pk_bf16_f32 v236, v110, v111
v_cvt_pk_bf16_f32 v237, v112, v113
buffer_store_dwordx4 v[234:237], v3, s[48:51], s64 offen offset:0 sc1
v_cvt_pk_bf16_f32 v238, v170, v171
v_cvt_pk_bf16_f32 v239, v172, v173
v_cvt_pk_bf16_f32 v240, v174, v175
v_cvt_pk_bf16_f32 v241, v176, v177
buffer_store_dwordx4 v[238:241], v3, s[48:51], s64 offen offset:1024 sc1
s_add_u32 s64, s64, 4096
v_cvt_pk_bf16_f32 v226, v114, v115
v_cvt_pk_bf16_f32 v227, v116, v117
v_cvt_pk_bf16_f32 v228, v118, v119
v_cvt_pk_bf16_f32 v229, v120, v121
buffer_store_dwordx4 v[226:229], v3, s[48:51], s64 offen offset:0 sc1
v_cvt_pk_bf16_f32 v230, v178, v179
v_cvt_pk_bf16_f32 v231, v180, v181
v_cvt_pk_bf16_f32 v232, v182, v183
v_cvt_pk_bf16_f32 v233, v184, v185
buffer_store_dwordx4 v[230:233], v3, s[48:51], s64 offen offset:1024 sc1
s_add_u32 s64, s64, 4096
v_cvt_pk_bf16_f32 v234, v122, v123
v_cvt_pk_bf16_f32 v235, v124, v125
v_cvt_pk_bf16_f32 v236, v126, v127
v_cvt_pk_bf16_f32 v237, v128, v129
buffer_store_dwordx4 v[234:237], v3, s[48:51], s64 offen offset:0 sc1
v_cvt_pk_bf16_f32 v238, v186, v187
v_cvt_pk_bf16_f32 v239, v188, v189
v_cvt_pk_bf16_f32 v240, v190, v191
v_cvt_pk_bf16_f32 v241, v192, v193
buffer_store_dwordx4 v[238:241], v3, s[48:51], s64 offen offset:1024 sc1
s_add_u32 s64, s64, 4096
v_cvt_pk_bf16_f32 v226, v130, v131
v_cvt_pk_bf16_f32 v227, v132, v133
v_cvt_pk_bf16_f32 v228, v134, v135
v_cvt_pk_bf16_f32 v229, v136, v137
buffer_store_dwordx4 v[226:229], v3, s[48:51], s64 offen offset:0 sc1
v_cvt_pk_bf16_f32 v230, v194, v195
v_cvt_pk_bf16_f32 v231, v196, v197
v_cvt_pk_bf16_f32 v232, v198, v199
v_cvt_pk_bf16_f32 v233, v200, v201
buffer_store_dwordx4 v[230:233], v3, s[48:51], s64 offen offset:1024 sc1
s_add_u32 s64, s64, 4096
v_cvt_pk_bf16_f32 v234, v138, v139
v_cvt_pk_bf16_f32 v235, v140, v141
v_cvt_pk_bf16_f32 v236, v142, v143
v_cvt_pk_bf16_f32 v237, v144, v145
buffer_store_dwordx4 v[234:237], v3, s[48:51], s64 offen offset:0 sc1
v_cvt_pk_bf16_f32 v238, v202, v203
v_cvt_pk_bf16_f32 v239, v204, v205
v_cvt_pk_bf16_f32 v240, v206, v207
v_cvt_pk_bf16_f32 v241, v208, v209
buffer_store_dwordx4 v[238:241], v3, s[48:51], s64 offen offset:1024 sc1
s_waitcnt vmcnt(0)
s_mov_b64 exec, 1
buffer_store_dwordx2 v[44:45], off, s[92:95], s58 offset:0 sc1
s_mov_b64 exec, -1
s_cmp_lg_u32 %1, 224
s_cbranch_scc1 L_gna_%=
v_lshrrev_b32 v50, 3, v1
v_and_b32 v51, 7, v1
v_lshlrev_b32 v50, 8, v50
v_lshl_add_u32 v50, v51, 3, v50
v_add_u32 v46, 0x1001000, v50
s_mov_b32 s54, 0x40000
L_ga_%=:
buffer_load_dwordx2 v[42:43], v46, s[92:95], 0 offen offset:0 sc1
s_waitcnt vmcnt(0)
v_cmp_ne_u64 vcc, s[98:99], v[42:43]
s_sub_u32 s54, s54, 1
s_cbranch_scc1 L_gax_%=
s_cmp_eq_u64 vcc, 0
s_cbranch_scc1 L_gax_%=
s_sleep 1
s_branch L_ga_%=
L_gax_%=:
s_mov_b32 s54, 0x40000
L_gb_%=:
buffer_load_dwordx2 v[42:43], v46, s[92:95], 0 offen offset:2048 sc1
s_waitcnt vmcnt(0)
v_cmp_ne_u64 vcc, s[98:99], v[42:43]
s_sub_u32 s54, s54, 1
s_cbranch_scc1 L_gbx_%=
s_cmp_eq_u64 vcc, 0
s_cbranch_scc1 L_gbx_%=
s_sleep 1
s_branch L_gb_%=
L_gbx_%=:
L_gna_%=:
s_and_b32 s31, %1, 1
s_lshl_b32 s31, s31, 3
s_bfe_u32 s30, %1, 0x30003
s_add_u32 s31, s31, s30
s_lshl_b32 s31, s31, 8
s_add_u32 s31, s31, 0x1001000
v_min_u32 v51, 7, v1
v_lshl_add_u32 v46, v51, 3, s31
s_mov_b32 s54, 0x40000
L_gx_%=:
buffer_load_dwordx2 v[42:43], v46, s[92:95], 0 offen offset:0 sc1
s_waitcnt vmcnt(0)
v_cmp_ne_u64 vcc, s[98:99], v[42:43]
s_sub_u32 s54, s54, 1
s_cbranch_scc1 L_gxx_%=
s_cmp_eq_u64 vcc, 0
s_cbranch_scc1 L_gxx_%=
s_sleep 1
s_branch L_gx_%=
L_gxx_%=:
s_and_b32 s8, %1, 1
s_lshl_b32 s8, s8, 3
s_bfe_u32 s9, %1, 0x30003
s_add_u32 s8, s8, s9
s_lshl_b32 s8, s8, 1
s_lshr_b32 s9, s7, 1
s_add_u32 s8, s8, s9
s_lshl_b32 s8, s8, 17
s_and_b32 s9, s7, 1
s_lshl_b32 s9, s9, 11
s_add_u32 s8, s8, s9
v_add_u32 v2, s8, v3
s_mov_b32 s35, 0
s_add_u32 s36, s20, 0x800000
s_addc_u32 s37, s21, 0
s_mov_b32 s38, 0x400000
s_mov_b32 s39, 0x20000
buffer_load_dwordx4 a[192:195], v2, s[36:39], s35 offen offset:0 sc1
buffer_load_dwordx4 a[196:199], v2, s[36:39], s35 offen offset:1024 sc1
s_add_u32 s35, s35, 4096
buffer_load_dwordx4 a[200:203], v2, s[36:39], s35 offen offset:0 sc1
buffer_load_dwordx4 a[204:207], v2, s[36:39], s35 offen offset:1024 sc1
s_add_u32 s35, s35, 4096
buffer_load_dwordx4 a[208:211], v2, s[36:39], s35 offen offset:0 sc1
buffer_load_dwordx4 a[212:215], v2, s[36:39], s35 offen offset:1024 sc1
s_add_u32 s35, s35, 4096
buffer_load_dwordx4 a[216:219], v2, s[36:39], s35 offen offset:0 sc1
buffer_load_dwordx4 a[220:223], v2, s[36:39], s35 offen offset:1024 sc1
s_add_u32 s35, s35, 4096
buffer_load_dwordx4 a[224:227], v2, s[36:39], s35 offen offset:0 sc1
buffer_load_dwordx4 a[228:231], v2, s[36:39], s35 offen offset:1024 sc1
s_add_u32 s35, s35, 4096
buffer_load_dwordx4 a[232:235], v2, s[36:39], s35 offen offset:0 sc1
buffer_load_dwordx4 a[236:239], v2, s[36:39], s35 offen offset:1024 sc1
s_add_u32 s35, s35, 4096
buffer_load_dwordx4 a[240:243], v2, s[36:39], s35 offen offset:0 sc1
buffer_load_dwordx4 a[244:247], v2, s[36:39], s35 offen offset:1024 sc1
s_add_u32 s35, s35, 4096
buffer_load_dwordx4 a[248:251], v2, s[36:39], s35 offen offset:0 sc1
buffer_load_dwordx4 a[252:255], v2, s[36:39], s35 offen offset:1024 sc1
s_add_u32 s35, s35, 4096
s_barrier
s_branch .Lcstart
L_cls2_%=:
s_waitcnt vmcnt(12) lgkmcnt(4)
v_mov_b32 v206, s98
v_mov_b32 v207, s99
s_mov_b64 exec, 1
buffer_store_dwordx2 v[206:207], off, s[92:95], s89 offset:256 sc1
s_mov_b64 exec, -1
v_mfma_f32_16x16x32_bf16 v[78:81], a[32:35], v[54:57], 0
RT 66,23,0
v_mfma_f32_16x16x32_bf16 v[82:85], a[36:39], v[54:57], 0
RT 68,23,1024
v_mfma_f32_16x16x32_bf16 v[142:145], a[40:43], v[54:57], 0
buffer_load_dwordx4 a[96:99], v16, s[40:43], s63 offen offset:0 sc1
v_mfma_f32_16x16x32_bf16 v[146:149], a[44:47], v[54:57], 0
WL 4
v_mfma_f32_16x16x32_bf16 v[86:89], a[32:35], v[58:61], 0
RT 70,24,0
v_mfma_f32_16x16x32_bf16 v[90:93], a[36:39], v[58:61], 0
RT 72,24,1024
v_mfma_f32_16x16x32_bf16 v[150:153], a[40:43], v[58:61], 0
buffer_load_dwordx4 a[100:103], v16, s[40:43], s63 offen offset:1024 sc1
v_mfma_f32_16x16x32_bf16 v[154:157], a[44:47], v[58:61], 0
s_add_u32 s63, s63, 4096
WL 4
v_mfma_f32_16x16x32_bf16 v[94:97], a[32:35], v[62:65], 0
RT 74,25,0
v_mfma_f32_16x16x32_bf16 v[98:101], a[36:39], v[62:65], 0
RT 76,25,1024
v_mfma_f32_16x16x32_bf16 v[158:161], a[40:43], v[62:65], 0
buffer_load_dwordx4 a[104:107], v17, s[40:43], s86 offen offset:0 sc1
v_mfma_f32_16x16x32_bf16 v[162:165], a[44:47], v[62:65], 0
WL 4
v_mfma_f32_16x16x32_bf16 v[102:105], a[32:35], v[66:69], 0
RT 54,26,0
v_mfma_f32_16x16x32_bf16 v[106:109], a[36:39], v[66:69], 0
RT 56,26,1024
v_mfma_f32_16x16x32_bf16 v[166:169], a[40:43], v[66:69], 0
buffer_load_dwordx4 a[108:111], v17, s[40:43], s86 offen offset:1024 sc1
v_mfma_f32_16x16x32_bf16 v[170:173], a[44:47], v[66:69], 0
s_add_u32 s86, s86, 4096
WL 4
v_mfma_f32_16x16x32_bf16 v[110:113], a[32:35], v[70:73], 0
RT 58,27,0
v_mfma_f32_16x16x32_bf16 v[114:117], a[36:39], v[70:73], 0
)"
R"(RT 60,27,1024
v_mfma_f32_16x16x32_bf16 v[174:177], a[40:43], v[70:73], 0
buffer_load_dwordx4 a[112:115], v16, s[40:43], s63 offen offset:0 sc1
v_mfma_f32_16x16x32_bf16 v[178:181], a[44:47], v[70:73], 0
WL 4
v_mfma_f32_16x16x32_bf16 v[118:121], a[32:35], v[74:77], 0
RT 62,20,8192
v_mfma_f32_16x16x32_bf16 v[122:125], a[36:39], v[74:77], 0
RT 64,20,9216
v_mfma_f32_16x16x32_bf16 v[182:185], a[40:43], v[74:77], 0
buffer_load_dwordx4 a[116:119], v16, s[40:43], s63 offen offset:1024 sc1
v_mfma_f32_16x16x32_bf16 v[186:189], a[44:47], v[74:77], 0
s_add_u32 s63, s63, 4096
WL 4
v_mfma_f32_16x16x32_bf16 v[126:129], a[32:35], v[54:57], 0
RT 66,21,8192
v_mfma_f32_16x16x32_bf16 v[130:133], a[36:39], v[54:57], 0
RT 68,21,9216
v_mfma_f32_16x16x32_bf16 v[190:193], a[40:43], v[54:57], 0
buffer_load_dwordx4 a[120:123], v17, s[40:43], s86 offen offset:0 sc1
v_mfma_f32_16x16x32_bf16 v[194:197], a[44:47], v[54:57], 0
WL 4
v_mfma_f32_16x16x32_bf16 v[134:137], a[32:35], v[58:61], 0
RT 70,22,8192
v_mfma_f32_16x16x32_bf16 v[138:141], a[36:39], v[58:61], 0
RT 72,22,9216
v_mfma_f32_16x16x32_bf16 v[198:201], a[40:43], v[58:61], 0
buffer_load_dwordx4 a[124:127], v17, s[40:43], s86 offen offset:1024 sc1
v_mfma_f32_16x16x32_bf16 v[202:205], a[44:47], v[58:61], 0
s_add_u32 s86, s86, 4096
s_waitcnt vmcnt(17) lgkmcnt(4)
MA 78,48,62
RT 74,23,8192
MA 82,52,62
RT 76,23,9216
MA 142,56,62
buffer_load_dwordx4 a[128:131], v16, s[40:43], s63 offen offset:0 sc1
MA 146,60,62
WL 4
MA 86,48,66
RT 54,24,8192
MA 90,52,66
RT 56,24,9216
MA 150,56,66
buffer_load_dwordx4 a[132:135], v16, s[40:43], s63 offen offset:1024 sc1
MA 154,60,66
s_add_u32 s63, s63, 4096
WL 4
MA 94,48,70
RT 58,25,8192
MA 98,52,70
RT 60,25,9216
MA 158,56,70
buffer_load_dwordx4 a[136:139], v17, s[40:43], s86 offen offset:0 sc1
MA 162,60,70
WL 4
MA 102,48,74
RT 62,26,8192
MA 106,52,74
RT 64,26,9216
MA 166,56,74
buffer_load_dwordx4 a[140:143], v17, s[40:43], s86 offen offset:1024 sc1
MA 170,60,74
s_add_u32 s86, s86, 4096
WL 4
MA 110,48,54
RT 66,27,8192
MA 114,52,54
RT 68,27,9216
s_add_u32 m0, s52, 65536
MA 174,56,54
buffer_load_dwordx4 v18, s[44:47], s60 offen offset:0 lds
MA 178,60,54
WL 4
MA 118,48,58
s_barrier
RT 70,20,16384
MA 122,52,58
RT 72,20,17408
MA 182,56,58
buffer_load_dwordx4 v19, s[44:47], s60 offen offset:1024 lds
MA 186,60,58
s_add_u32 s60, s60, 229376
WL 4
MA 126,48,62
RT 74,21,16384
MA 130,52,62
RT 76,21,17408
MA 190,56,62
buffer_load_dwordx4 a[144:147], v16, s[40:43], s63 offen offset:0 sc1
MA 194,60,62
WL 4
MA 134,48,66
RT 54,22,16384
MA 138,52,66
RT 56,22,17408
MA 198,56,66
buffer_load_dwordx4 a[148:151], v16, s[40:43], s63 offen offset:1024 sc1
MA 202,60,66
s_add_u32 s63, s63, 4096
s_waitcnt vmcnt(21) lgkmcnt(4)
MA 78,64,70
RT 58,23,16384
MA 82,68,70
RT 60,23,17408
MA 142,72,70
buffer_load_dwordx4 a[152:155], v17, s[40:43], s86 offen offset:0 sc1
MA 146,76,70
WL 4
MA 86,64,74
RT 62,24,16384
MA 90,68,74
RT 64,24,17408
MA 150,72,74
buffer_load_dwordx4 a[156:159], v17, s[40:43], s86 offen offset:1024 sc1
MA 154,76,74
s_add_u32 s86, s86, 4096
WL 4
MA 94,64,54
RT 66,25,16384
MA 98,68,54
RT 68,25,17408
s_add_u32 m0, s52, 73728
MA 158,72,54
buffer_load_dwordx4 v18, s[44:47], s60 offen offset:0 lds
MA 162,76,54
WL 4
MA 102,64,58
RT 70,26,16384
MA 106,68,58
RT 72,26,17408
MA 166,72,58
buffer_load_dwordx4 v19, s[44:47], s60 offen offset:1024 lds
MA 170,76,58
s_add_u32 s60, s60, 229376
WL 4
MA 110,64,62
RT 74,27,16384
MA 114,68,62
RT 76,27,17408
MA 174,72,62
buffer_load_dword v50, v49, s[44:47], s58 offen
MA 178,76,62
s_add_u32 s58, s58, 3670016
s_cmp_ge_u32 s58, s61
s_cselect_b32 s58, 0x40000000, s58
WL 4
MA 118,64,66
RT 54,20,24576
MA 122,68,66
RT 56,20,25600
MA 182,72,66
MA 186,76,66
WL 4
MA 126,64,70
RT 58,21,24576
MA 130,68,70
RT 60,21,25600
MA 190,72,70
MA 194,76,70
WL 4
MA 134,64,74
RT 62,22,24576
MA 138,68,74
RT 64,22,25600
MA 198,72,74
MA 202,76,74
buffer_load_dwordx2 v[44:45], v47, s[92:95], 0 offen offset:256 sc1
s_waitcnt vmcnt(23) lgkmcnt(4)
MA 78,80,54
RT 66,23,24576
MA 82,84,54
RT 68,23,25600
MA 142,88,54
MA 146,92,54
WL 4
MA 86,80,58
RT 70,24,24576
MA 90,84,58
RT 72,24,25600
MA 150,88,58
MA 154,92,58
WL 4
MA 94,80,62
RT 74,25,24576
MA 98,84,62
RT 76,25,25600
MA 158,88,62
MA 162,92,62
WL 4
MA 102,80,66
RT 54,26,24576
MA 106,84,66
RT 56,26,25600
MA 166,88,66
MA 170,92,66
WL 4
MA 110,80,70
RT 58,27,24576
MA 114,84,70
RT 60,27,25600
MA 174,88,70
MA 178,92,70
WL 4
MA 118,80,74
s_barrier
RT 62,20,32768
MA 122,84,74
RT 64,20,33792
MA 182,88,74
MA 186,92,74
WL 4
MA 126,80,54
RT 66,21,32768
MA 130,84,54
RT 68,21,33792
MA 190,88,54
MA 194,92,54
WL 4
MA 134,80,58
RT 70,22,32768
MA 138,84,58
RT 72,22,33792
MA 198,88,58
MA 202,92,58
s_waitcnt vmcnt(18) lgkmcnt(4)
MA 78,96,62
RT 74,23,32768
MA 82,100,62
RT 76,23,33792
MA 142,104,62
buffer_load_dwordx4 a[160:163], v16, s[40:43], s63 offen offset:0 sc1
MA 146,108,62
WL 4
MA 86,96,66
RT 54,24,32768
MA 90,100,66
RT 56,24,33792
MA 150,104,66
buffer_load_dwordx4 a[164:167], v16, s[40:43], s63 offen offset:1024 sc1
MA 154,108,66
s_add_u32 s63, s63, 4096
WL 4
MA 94,96,70
RT 58,25,32768
MA 98,100,70
RT 60,25,33792
MA 158,104,70
buffer_load_dwordx4 a[168:171], v17, s[40:43], s86 offen offset:0 sc1
MA 162,108,70
WL 4
MA 102,96,74
RT 62,26,32768
MA 106,100,74
RT 64,26,33792
MA 166,104,74
buffer_load_dwordx4 a[172:175], v17, s[40:43], s86 offen offset:1024 sc1
MA 170,108,74
s_add_u32 s86, s86, 4096
WL 4
MA 110,96,54
RT 66,27,32768
MA 114,100,54
RT 68,27,33792
MA 174,104,54
buffer_load_dwordx4 a[176:179], v16, s[40:43], s63 offen offset:0 sc1
MA 178,108,54
WL 4
MA 118,96,58
RT 70,20,40960
MA 122,100,58
RT 72,20,41984
MA 182,104,58
buffer_load_dwordx4 a[180:183], v16, s[40:43], s63 offen offset:1024 sc1
MA 186,108,58
s_add_u32 s63, s63, 4096
WL 4
MA 126,96,62
RT 74,21,40960
MA 130,100,62
RT 76,21,41984
MA 190,104,62
buffer_load_dwordx4 a[184:187], v17, s[40:43], s86 offen offset:0 sc1
MA 194,108,62
WL 4
MA 134,96,66
RT 54,22,40960
MA 138,100,66
RT 56,22,41984
MA 198,104,66
buffer_load_dwordx4 a[188:191], v17, s[40:43], s86 offen offset:1024 sc1
MA 202,108,66
s_add_u32 s86, s86, 4096
s_waitcnt vmcnt(22) lgkmcnt(4)
MA 78,112,70
RT 58,23,40960
MA 82,116,70
RT 60,23,41984
s_add_u32 m0, s52, 81920
MA 142,120,70
buffer_load_dwordx4 v18, s[44:47], s60 offen offset:0 lds
MA 146,124,70
WL 4
MA 86,112,74
RT 62,24,40960
MA 90,116,74
RT 64,24,41984
MA 150,120,74
buffer_load_dwordx4 v19, s[44:47], s60 offen offset:1024 lds
MA 154,124,74
s_add_u32 s60, s60, 229376
WL 4
MA 94,112,54
RT 66,25,40960
MA 98,116,54
RT 68,25,41984
MA 158,120,54
buffer_load_dwordx4 a[32:35], v16, s[40:43], s63 offen offset:0 sc1
MA 162,124,54
WL 4
MA 102,112,58
RT 70,26,40960
MA 106,116,58
RT 72,26,41984
MA 166,120,58
buffer_load_dwordx4 a[36:39], v16, s[40:43], s63 offen offset:1024 sc1
MA 170,124,58
s_add_u32 s63, s63, 4096
WL 4
MA 110,112,62
RT 74,27,40960
MA 114,116,62
RT 76,27,41984
MA 174,120,62
buffer_load_dwordx4 a[40:43], v17, s[40:43], s86 offen offset:0 sc1
MA 178,124,62
WL 4
MA 118,112,66
s_barrier
RT 54,20,49152
MA 122,116,66
RT 56,20,50176
MA 182,120,66
buffer_load_dwordx4 a[44:47], v17, s[40:43], s86 offen offset:1024 sc1
MA 186,124,66
s_add_u32 s86, s86, 4096
WL 4
MA 126,112,70
RT 58,21,49152
MA 130,116,70
RT 60,21,50176
s_add_u32 m0, s52, 90112
MA 190,120,70
buffer_load_dwordx4 v18, s[44:47], s60 offen offset:0 lds
MA 194,124,70
WL 4
MA 134,112,74
RT 62,22,49152
MA 138,116,74
RT 64,22,50176
MA 198,120,74
buffer_load_dwordx4 v19, s[44:47], s60 offen offset:1024 lds
MA 202,124,74
s_add_u32 s60, s60, 229376
s_waitcnt vmcnt(26) lgkmcnt(4)
MA 78,128,54
RT 66,23,49152
MA 82,132,54
RT 68,23,50176
s_add_u32 m0, s52, 98304
MA 142,136,54
buffer_load_dwordx4 v18, s[44:47], s60 offen offset:0 lds
MA 146,140,54
WL 4
MA 86,128,58
RT 70,24,49152
MA 90,132,58
RT 72,24,50176
MA 150,136,58
buffer_load_dwordx4 v19, s[44:47], s60 offen offset:1024 lds
MA 154,140,58
s_add_u32 s60, s60, 229376
WL 4
MA 94,128,62
RT 74,25,49152
MA 98,132,62
RT 76,25,50176
MA 158,136,62
buffer_load_dwordx4 a[48:51], v16, s[40:43], s63 offen offset:0 sc1
MA 162,140,62
WL 4
MA 102,128,66
RT 54,26,49152
MA 106,132,66
RT 56,26,50176
MA 166,136,66
buffer_load_dwordx4 a[52:55], v16, s[40:43], s63 offen offset:1024 sc1
MA 170,140,66
s_add_u32 s63, s63, 4096
WL 4
)"
R"(MA 110,128,70
RT 58,27,49152
MA 114,132,70
RT 60,27,50176
MA 174,136,70
buffer_load_dwordx4 a[56:59], v17, s[40:43], s86 offen offset:0 sc1
MA 178,140,70
WL 4
MA 118,128,74
RT 62,20,57344
MA 122,132,74
RT 64,20,58368
MA 182,136,74
buffer_load_dwordx4 a[60:63], v17, s[40:43], s86 offen offset:1024 sc1
MA 186,140,74
s_add_u32 s86, s86, 4096
WL 4
MA 126,128,54
RT 66,21,57344
MA 130,132,54
RT 68,21,58368
s_add_u32 m0, s52, 106496
MA 190,136,54
buffer_load_dwordx4 v18, s[44:47], s60 offen offset:0 lds
MA 194,140,54
WL 4
MA 134,128,58
RT 70,22,57344
MA 138,132,58
RT 72,22,58368
MA 198,136,58
buffer_load_dwordx4 v19, s[44:47], s60 offen offset:1024 lds
MA 202,140,58
s_add_u32 s60, s60, 229376
s_waitcnt vmcnt(28) lgkmcnt(4)
MA 78,144,62
RT 74,23,57344
MA 82,148,62
RT 76,23,58368
MA 142,152,62
buffer_load_dwordx4 a[64:67], v16, s[40:43], s63 offen offset:0 sc1
MA 146,156,62
WL 4
MA 86,144,66
RT 54,24,57344
MA 90,148,66
RT 56,24,58368
MA 150,152,66
buffer_load_dwordx4 a[68:71], v16, s[40:43], s63 offen offset:1024 sc1
MA 154,156,66
s_add_u32 s63, s63, 4096
WL 4
MA 94,144,70
RT 58,25,57344
MA 98,148,70
RT 60,25,58368
MA 158,152,70
buffer_load_dwordx4 a[72:75], v17, s[40:43], s86 offen offset:0 sc1
MA 162,156,70
WL 4
MA 102,144,74
RT 62,26,57344
MA 106,148,74
RT 64,26,58368
MA 166,152,74
buffer_load_dwordx4 a[76:79], v17, s[40:43], s86 offen offset:1024 sc1
MA 170,156,74
s_add_u32 s86, s86, 4096
WL 4
MA 110,144,54
RT 66,27,57344
MA 114,148,54
RT 68,27,58368
s_add_u32 m0, s52, 114688
MA 174,152,54
buffer_load_dwordx4 v18, s[44:47], s60 offen offset:0 lds
MA 178,156,54
WL 4
MA 118,144,58
s_waitcnt vmcnt(31)
s_barrier
RT 70,28,0
MA 122,148,58
RT 72,28,1024
MA 182,152,58
buffer_load_dwordx4 v19, s[44:47], s60 offen offset:1024 lds
MA 186,156,58
s_add_u32 s60, s60, 229376
WL 4
MA 126,144,62
RT 74,29,0
MA 130,148,62
RT 76,29,1024
MA 190,152,62
buffer_load_dwordx4 a[80:83], v16, s[40:43], s63 offen offset:0 sc1
MA 194,156,62
WL 4
MA 134,144,66
RT 54,30,0
MA 138,148,66
RT 56,30,1024
MA 198,152,66
buffer_load_dwordx4 a[84:87], v16, s[40:43], s63 offen offset:1024 sc1
MA 202,156,66
s_add_u32 s63, s63, 4096
s_waitcnt vmcnt(32)
v_cmp_ne_u64 vcc, s[98:99], v[44:45]
s_cmp_eq_u64 vcc, 0
s_cbranch_scc0 L_slowgB2_%=
L_backgB2_%=:
s_waitcnt vmcnt(28) lgkmcnt(4)
MA 78,160,70
RT 58,31,0
MA 82,164,70
RT 60,31,1024
MA 142,168,70
buffer_load_dwordx4 a[88:91], v17, s[40:43], s86 offen offset:0 sc1
MA 146,172,70
WL 4
MA 86,160,74
RT 62,32,0
MA 90,164,74
RT 64,32,1024
MA 150,168,74
buffer_load_dwordx4 a[92:95], v17, s[40:43], s86 offen offset:1024 sc1
MA 154,172,74
s_add_u32 s86, s86, 4096
WL 4
MA 94,160,54
RT 66,33,0
MA 98,164,54
RT 68,33,1024
s_add_u32 m0, s52, 122880
MA 158,168,54
buffer_load_dwordx4 v18, s[44:47], s60 offen offset:0 lds
MA 162,172,54
WL 4
MA 102,160,58
RT 70,34,0
MA 106,164,58
RT 72,34,1024
MA 166,168,58
buffer_load_dwordx4 v19, s[44:47], s60 offen offset:1024 lds
MA 170,172,58
s_add_u32 s60, s60, 229376
WL 4
MA 110,160,62
RT 74,35,0
MA 114,164,62
RT 76,35,1024
MA 174,168,62
buffer_load_dwordx4 a[96:99], v16, s[40:43], s63 offen offset:0 sc1
MA 178,172,62
WL 4
MA 118,160,66
RT 54,28,8192
MA 122,164,66
RT 56,28,9216
MA 182,168,66
buffer_load_dwordx4 a[100:103], v16, s[40:43], s63 offen offset:1024 sc1
MA 186,172,66
s_add_u32 s63, s63, 4096
WL 4
MA 126,160,70
RT 58,29,8192
MA 130,164,70
RT 60,29,9216
MA 190,168,70
buffer_load_dwordx4 a[104:107], v17, s[40:43], s86 offen offset:0 sc1
MA 194,172,70
WL 4
MA 134,160,74
RT 62,30,8192
MA 138,164,74
RT 64,30,9216
MA 198,168,74
buffer_load_dwordx4 a[108:111], v17, s[40:43], s86 offen offset:1024 sc1
MA 202,172,74
s_add_u32 s86, s86, 4096
s_waitcnt vmcnt(32) lgkmcnt(4)
MA 78,176,54
RT 66,31,8192
MA 82,180,54
RT 68,31,9216
s_add_u32 m0, s52, 131072
MA 142,184,54
buffer_load_dwordx4 v18, s[44:47], s60 offen offset:0 lds
MA 146,188,54
WL 4
MA 86,176,58
RT 70,32,8192
MA 90,180,58
RT 72,32,9216
MA 150,184,58
buffer_load_dwordx4 v19, s[44:47], s60 offen offset:1024 lds
MA 154,188,58
s_add_u32 s60, s60, 229376
WL 4
MA 94,176,62
RT 74,33,8192
MA 98,180,62
RT 76,33,9216
MA 158,184,62
buffer_load_dwordx4 a[112:115], v16, s[40:43], s63 offen offset:0 sc1
MA 162,188,62
WL 4
MA 102,176,66
RT 54,34,8192
MA 106,180,66
RT 56,34,9216
MA 166,184,66
buffer_load_dwordx4 a[116:119], v16, s[40:43], s63 offen offset:1024 sc1
MA 170,188,66
s_add_u32 s63, s63, 4096
WL 4
MA 110,176,70
RT 58,35,8192
MA 114,180,70
RT 60,35,9216
MA 174,184,70
buffer_load_dwordx4 a[120:123], v17, s[40:43], s86 offen offset:0 sc1
MA 178,188,70
WL 4
MA 118,176,74
s_waitcnt vmcnt(29)
s_barrier
RT 62,28,16384
MA 122,180,74
RT 64,28,17408
MA 182,184,74
buffer_load_dwordx4 a[124:127], v17, s[40:43], s86 offen offset:1024 sc1
MA 186,188,74
s_add_u32 s86, s86, 4096
WL 4
MA 126,176,54
RT 66,29,16384
MA 130,180,54
RT 68,29,17408
s_add_u32 m0, s52, 139264
MA 190,184,54
buffer_load_dwordx4 v18, s[44:47], s60 offen offset:0 lds
MA 194,188,54
WL 4
MA 134,176,58
RT 70,30,16384
MA 138,180,58
RT 72,30,17408
MA 198,184,58
buffer_load_dwordx4 v19, s[44:47], s60 offen offset:1024 lds
MA 202,188,58
s_add_u32 s60, s60, 229376
WL 4
MA 78,32,62
RT 74,31,16384
MA 82,36,62
RT 76,31,17408
MA 142,40,62
buffer_load_dwordx4 a[128:131], v16, s[40:43], s63 offen offset:0 sc1
MA 146,44,62
WL 4
MA 86,32,66
RT 54,32,16384
MA 90,36,66
RT 56,32,17408
MA 150,40,66
buffer_load_dwordx4 a[132:135], v16, s[40:43], s63 offen offset:1024 sc1
MA 154,44,66
s_add_u32 s63, s63, 4096
WL 4
MA 94,32,70
RT 58,33,16384
MA 98,36,70
RT 60,33,17408
MA 158,40,70
buffer_load_dwordx4 a[136:139], v17, s[40:43], s86 offen offset:0 sc1
MA 162,44,70
WL 4
MA 102,32,74
RT 62,34,16384
MA 106,36,74
RT 64,34,17408
MA 166,40,74
buffer_load_dwordx4 a[140:143], v17, s[40:43], s86 offen offset:1024 sc1
MA 170,44,74
s_add_u32 s86, s86, 4096
WL 4
MA 110,32,54
RT 66,35,16384
MA 114,36,54
RT 68,35,17408
MA 174,40,54
MA 178,44,54
WL 4
MA 118,32,58
RT 70,28,24576
MA 122,36,58
RT 72,28,25600
s_add_u32 m0, s52, 147456
MA 182,40,58
buffer_load_dwordx4 v18, s[44:47], s60 offen offset:0 lds
MA 186,44,58
WL 4
MA 126,32,62
RT 74,29,24576
MA 130,36,62
RT 76,29,25600
MA 190,40,62
buffer_load_dwordx4 v19, s[44:47], s60 offen offset:1024 lds
MA 194,44,62
s_add_u32 s60, s60, 229376
WL 4
MA 134,32,66
RT 54,30,24576
MA 138,36,66
RT 56,30,25600
MA 198,40,66
MA 202,44,66
s_waitcnt vmcnt(32) lgkmcnt(4)
MA 78,48,70
RT 58,31,24576
MA 82,52,70
RT 60,31,25600
MA 142,56,70
buffer_load_dwordx4 a[144:147], v16, s[40:43], s63 offen offset:0 sc1
MA 146,60,70
WL 4
MA 86,48,74
RT 62,32,24576
MA 90,52,74
RT 64,32,25600
MA 150,56,74
buffer_load_dwordx4 a[148:151], v16, s[40:43], s63 offen offset:1024 sc1
MA 154,60,74
s_add_u32 s63, s63, 4096
WL 4
MA 94,48,54
RT 66,33,24576
MA 98,52,54
RT 68,33,25600
MA 158,56,54
buffer_load_dwordx4 a[152:155], v17, s[40:43], s86 offen offset:0 sc1
MA 162,60,54
WL 4
MA 102,48,58
RT 70,34,24576
MA 106,52,58
RT 72,34,25600
MA 166,56,58
buffer_load_dwordx4 a[156:159], v17, s[40:43], s86 offen offset:1024 sc1
MA 170,60,58
s_add_u32 s86, s86, 4096
WL 4
MA 110,48,62
RT 74,35,24576
MA 114,52,62
RT 76,35,25600
MA 174,56,62
MA 178,60,62
WL 4
MA 118,48,66
s_waitcnt vmcnt(34)
s_barrier
RT 54,28,32768
MA 122,52,66
RT 56,28,33792
s_add_u32 m0, s52, 155648
MA 182,56,66
buffer_load_dwordx4 v18, s[44:47], s60 offen offset:0 lds
MA 186,60,66
WL 4
MA 126,48,70
RT 58,29,32768
MA 130,52,70
RT 60,29,33792
MA 190,56,70
buffer_load_dwordx4 v19, s[44:47], s60 offen offset:1024 lds
MA 194,60,70
s_add_u32 s60, s60, 229376
WL 4
MA 134,48,74
RT 62,30,32768
MA 138,52,74
RT 64,30,33792
MA 198,56,74
MA 202,60,74
s_waitcnt vmcnt(32) lgkmcnt(4)
MA 78,64,54
RT 66,31,32768
MA 82,68,54
RT 68,31,33792
MA 142,72,54
buffer_load_dwordx4 a[160:163], v16, s[40:43], s63 offen offset:0 sc1
MA 146,76,54
WL 4
MA 86,64,58
RT 70,32,32768
MA 90,68,58
RT 72,32,33792
MA 150,72,58
buffer_load_dwordx4 a[164:167], v16, s[40:43], s63 offen offset:1024 sc1
)"
R"(MA 154,76,58
s_add_u32 s63, s63, 4096
WL 4
MA 94,64,62
RT 74,33,32768
MA 98,68,62
RT 76,33,33792
MA 158,72,62
buffer_load_dwordx4 a[168:171], v17, s[40:43], s86 offen offset:0 sc1
MA 162,76,62
WL 4
MA 102,64,66
RT 54,34,32768
MA 106,68,66
RT 56,34,33792
MA 166,72,66
buffer_load_dwordx4 a[172:175], v17, s[40:43], s86 offen offset:1024 sc1
MA 170,76,66
s_add_u32 s86, s86, 4096
WL 4
MA 110,64,70
RT 58,35,32768
MA 114,68,70
RT 60,35,33792
MA 174,72,70
MA 178,76,70
WL 4
MA 118,64,74
RT 62,28,40960
MA 122,68,74
RT 64,28,41984
s_add_u32 m0, s52, 0
MA 182,72,74
buffer_load_dwordx4 v18, s[44:47], s60 offen offset:0 lds
MA 186,76,74
WL 4
MA 126,64,54
RT 66,29,40960
MA 130,68,54
RT 68,29,41984
MA 190,72,54
buffer_load_dwordx4 v19, s[44:47], s60 offen offset:1024 lds
MA 194,76,54
s_add_u32 s60, s60, 229376
WL 4
MA 134,64,58
RT 70,30,40960
MA 138,68,58
RT 72,30,41984
MA 198,72,58
MA 202,76,58
s_waitcnt vmcnt(32) lgkmcnt(4)
MA 78,80,62
RT 74,31,40960
MA 82,84,62
RT 76,31,41984
MA 142,88,62
buffer_load_dwordx4 a[176:179], v16, s[40:43], s63 offen offset:0 sc1
MA 146,92,62
WL 4
MA 86,80,66
RT 54,32,40960
MA 90,84,66
RT 56,32,41984
MA 150,88,66
buffer_load_dwordx4 a[180:183], v16, s[40:43], s63 offen offset:1024 sc1
MA 154,92,66
s_add_u32 s63, s63, 4096
WL 4
MA 94,80,70
RT 58,33,40960
MA 98,84,70
RT 60,33,41984
MA 158,88,70
buffer_load_dwordx4 a[184:187], v17, s[40:43], s86 offen offset:0 sc1
MA 162,92,70
WL 4
MA 102,80,74
RT 62,34,40960
MA 106,84,74
RT 64,34,41984
MA 166,88,74
buffer_load_dwordx4 a[188:191], v17, s[40:43], s86 offen offset:1024 sc1
MA 170,92,74
s_add_u32 s86, s86, 4096
WL 4
MA 110,80,54
RT 66,35,40960
MA 114,84,54
RT 68,35,41984
MA 174,88,54
MA 178,92,54
WL 4
MA 118,80,58
s_waitcnt vmcnt(34)
s_barrier
RT 70,28,49152
MA 122,84,58
RT 72,28,50176
s_add_u32 m0, s52, 8192
MA 182,88,58
buffer_load_dwordx4 v18, s[44:47], s60 offen offset:0 lds
MA 186,92,58
WL 4
MA 126,80,62
RT 74,29,49152
MA 130,84,62
RT 76,29,50176
MA 190,88,62
buffer_load_dwordx4 v19, s[44:47], s60 offen offset:1024 lds
MA 194,92,62
s_add_u32 s60, s60, 229376
WL 4
MA 134,80,66
RT 54,30,49152
MA 138,84,66
RT 56,30,50176
MA 198,88,66
MA 202,92,66
s_waitcnt vmcnt(32) lgkmcnt(4)
MA 78,96,70
RT 58,31,49152
MA 82,100,70
RT 60,31,50176
MA 142,104,70
buffer_load_dword v50, v49, s[44:47], s58 offen
MA 146,108,70
s_add_u32 s58, s58, 3670016
s_cmp_ge_u32 s58, s61
s_cselect_b32 s58, 0x40000000, s58
WL 4
MA 86,96,74
RT 62,32,49152
MA 90,100,74
RT 64,32,50176
MA 150,104,74
MA 154,108,74
WL 4
MA 94,96,54
RT 66,33,49152
MA 98,100,54
RT 68,33,50176
MA 158,104,54
MA 162,108,54
WL 4
MA 102,96,58
RT 70,34,49152
MA 106,100,58
RT 72,34,50176
MA 166,104,58
MA 170,108,58
WL 4
MA 110,96,62
RT 74,35,49152
MA 114,100,62
RT 76,35,50176
MA 174,104,62
MA 178,108,62
WL 4
MA 118,96,66
RT 54,28,57344
MA 122,100,66
RT 56,28,58368
MA 182,104,66
MA 186,108,66
WL 4
MA 126,96,70
RT 58,29,57344
MA 130,100,70
RT 60,29,58368
MA 190,104,70
MA 194,108,70
WL 4
MA 134,96,74
RT 62,30,57344
MA 138,100,74
RT 64,30,58368
MA 198,104,74
MA 202,108,74
s_waitcnt vmcnt(27) lgkmcnt(4)
MA 78,112,54
RT 66,31,57344
MA 82,116,54
RT 68,31,58368
MA 142,120,54
MA 146,124,54
WL 4
MA 86,112,58
RT 70,32,57344
MA 90,116,58
RT 72,32,58368
MA 150,120,58
MA 154,124,58
WL 4
MA 94,112,62
RT 74,33,57344
MA 98,116,62
RT 76,33,58368
MA 158,120,62
MA 162,124,62
WL 4
MA 102,112,66
RT 54,34,57344
MA 106,116,66
RT 56,34,58368
MA 166,120,66
MA 170,124,66
WL 4
MA 110,112,70
RT 58,35,57344
MA 114,116,70
RT 60,35,58368
MA 174,120,70
MA 178,124,70
WL 4
MA 118,112,74
s_waitcnt vmcnt(25)
s_barrier
RT 62,36,0
MA 122,116,74
RT 64,36,1024
MA 182,120,74
MA 186,124,74
WL 4
MA 126,112,54
RT 66,37,0
MA 130,116,54
RT 68,37,1024
MA 190,120,54
MA 194,124,54
WL 4
MA 134,112,58
RT 70,38,0
MA 138,116,58
RT 72,38,1024
MA 198,120,58
MA 202,124,58
s_waitcnt vmcnt(21) lgkmcnt(4)
MA 78,128,62
RT 74,39,0
MA 82,132,62
RT 76,39,1024
MA 142,136,62
buffer_load_dwordx4 a[32:35], v16, s[40:43], s63 offen offset:0 sc1
MA 146,140,62
WL 4
MA 86,128,66
RT 54,40,0
MA 90,132,66
RT 56,40,1024
MA 150,136,66
buffer_load_dwordx4 a[36:39], v16, s[40:43], s63 offen offset:1024 sc1
MA 154,140,66
s_add_u32 s63, s63, 4096
WL 4
MA 94,128,70
RT 58,41,0
MA 98,132,70
RT 60,41,1024
MA 158,136,70
buffer_load_dwordx4 a[40:43], v17, s[40:43], s86 offen offset:0 sc1
MA 162,140,70
WL 4
MA 102,128,74
RT 62,42,0
MA 106,132,74
RT 64,42,1024
MA 166,136,74
buffer_load_dwordx4 a[44:47], v17, s[40:43], s86 offen offset:1024 sc1
MA 170,140,74
s_add_u32 s86, s86, 4096
WL 4
MA 110,128,54
RT 66,43,0
MA 114,132,54
RT 68,43,1024
MA 174,136,54
buffer_load_dwordx4 a[48:51], v16, s[40:43], s63 offen offset:0 sc1
MA 178,140,54
WL 4
MA 118,128,58
RT 70,36,8192
MA 122,132,58
RT 72,36,9216
MA 182,136,58
buffer_load_dwordx4 a[52:55], v16, s[40:43], s63 offen offset:1024 sc1
MA 186,140,58
s_add_u32 s63, s63, 4096
WL 4
MA 126,128,62
RT 74,37,8192
MA 130,132,62
RT 76,37,9216
MA 190,136,62
buffer_load_dwordx4 a[56:59], v17, s[40:43], s86 offen offset:0 sc1
MA 194,140,62
WL 4
MA 134,128,66
RT 54,38,8192
MA 138,132,66
RT 56,38,9216
MA 198,136,66
buffer_load_dwordx4 a[60:63], v17, s[40:43], s86 offen offset:1024 sc1
MA 202,140,66
s_add_u32 s86, s86, 4096
s_waitcnt vmcnt(23) lgkmcnt(4)
MA 78,144,70
RT 58,39,8192
MA 82,148,70
RT 60,39,9216
s_add_u32 m0, s52, 16384
MA 142,152,70
buffer_load_dwordx4 v18, s[44:47], s60 offen offset:0 lds
MA 146,156,70
WL 4
MA 86,144,74
RT 62,40,8192
MA 90,148,74
RT 64,40,9216
MA 150,152,74
buffer_load_dwordx4 v19, s[44:47], s60 offen offset:1024 lds
MA 154,156,74
s_add_u32 s60, s60, 229376
WL 4
MA 94,144,54
RT 66,41,8192
MA 98,148,54
RT 68,41,9216
MA 158,152,54
buffer_load_dwordx4 a[64:67], v16, s[40:43], s63 offen offset:0 sc1
MA 162,156,54
WL 4
MA 102,144,58
RT 70,42,8192
MA 106,148,58
RT 72,42,9216
MA 166,152,58
buffer_load_dwordx4 a[68:71], v16, s[40:43], s63 offen offset:1024 sc1
MA 170,156,58
s_add_u32 s63, s63, 4096
WL 4
MA 110,144,62
RT 74,43,8192
MA 114,148,62
RT 76,43,9216
MA 174,152,62
buffer_load_dwordx4 a[72:75], v17, s[40:43], s86 offen offset:0 sc1
MA 178,156,62
WL 4
MA 118,144,66
s_waitcnt vmcnt(26)
s_barrier
RT 54,36,16384
MA 122,148,66
RT 56,36,17408
MA 182,152,66
buffer_load_dwordx4 a[76:79], v17, s[40:43], s86 offen offset:1024 sc1
MA 186,156,66
s_add_u32 s86, s86, 4096
WL 4
MA 126,144,70
RT 58,37,16384
MA 130,148,70
RT 60,37,17408
s_add_u32 m0, s52, 24576
MA 190,152,70
buffer_load_dwordx4 v18, s[44:47], s60 offen offset:0 lds
MA 194,156,70
WL 4
MA 134,144,74
RT 62,38,16384
MA 138,148,74
RT 64,38,17408
MA 198,152,74
buffer_load_dwordx4 v19, s[44:47], s60 offen offset:1024 lds
MA 202,156,74
s_add_u32 s60, s60, 229376
s_waitcnt vmcnt(25) lgkmcnt(4)
MA 78,160,54
RT 66,39,16384
MA 82,164,54
RT 68,39,17408
s_add_u32 m0, s52, 32768
MA 142,168,54
buffer_load_dwordx4 v18, s[44:47], s60 offen offset:0 lds
MA 146,172,54
WL 4
MA 86,160,58
RT 70,40,16384
MA 90,164,58
RT 72,40,17408
MA 150,168,58
buffer_load_dwordx4 v19, s[44:47], s60 offen offset:1024 lds
MA 154,172,58
s_add_u32 s60, s60, 229376
WL 4
MA 94,160,62
RT 74,41,16384
MA 98,164,62
RT 76,41,17408
)"
R"(MA 158,168,62
buffer_load_dwordx4 a[80:83], v16, s[40:43], s63 offen offset:0 sc1
MA 162,172,62
WL 4
MA 102,160,66
RT 54,42,16384
MA 106,164,66
RT 56,42,17408
MA 166,168,66
buffer_load_dwordx4 a[84:87], v16, s[40:43], s63 offen offset:1024 sc1
MA 170,172,66
s_add_u32 s63, s63, 4096
WL 4
MA 110,160,70
RT 58,43,16384
MA 114,164,70
RT 60,43,17408
MA 174,168,70
buffer_load_dwordx4 a[88:91], v17, s[40:43], s86 offen offset:0 sc1
MA 178,172,70
WL 4
MA 118,160,74
RT 62,36,24576
MA 122,164,74
RT 64,36,25600
MA 182,168,74
buffer_load_dwordx4 a[92:95], v17, s[40:43], s86 offen offset:1024 sc1
MA 186,172,74
s_add_u32 s86, s86, 4096
WL 4
MA 126,160,54
RT 66,37,24576
MA 130,164,54
RT 68,37,25600
s_add_u32 m0, s52, 40960
MA 190,168,54
buffer_load_dwordx4 v18, s[44:47], s60 offen offset:0 lds
MA 194,172,54
WL 4
MA 134,160,58
RT 70,38,24576
MA 138,164,58
RT 72,38,25600
MA 198,168,58
buffer_load_dwordx4 v19, s[44:47], s60 offen offset:1024 lds
MA 202,172,58
s_add_u32 s60, s60, 229376
s_waitcnt vmcnt(27) lgkmcnt(4)
MA 78,176,62
RT 74,39,24576
MA 82,180,62
RT 76,39,25600
MA 142,184,62
buffer_load_dwordx4 a[96:99], v16, s[40:43], s63 offen offset:0 sc1
MA 146,188,62
WL 4
MA 86,176,66
RT 54,40,24576
MA 90,180,66
RT 56,40,25600
MA 150,184,66
buffer_load_dwordx4 a[100:103], v16, s[40:43], s63 offen offset:1024 sc1
MA 154,188,66
s_add_u32 s63, s63, 4096
WL 4
MA 94,176,70
RT 58,41,24576
MA 98,180,70
RT 60,41,25600
MA 158,184,70
buffer_load_dwordx4 a[104:107], v17, s[40:43], s86 offen offset:0 sc1
MA 162,188,70
WL 4
MA 102,176,74
RT 62,42,24576
MA 106,180,74
RT 64,42,25600
MA 166,184,74
buffer_load_dwordx4 a[108:111], v17, s[40:43], s86 offen offset:1024 sc1
MA 170,188,74
s_add_u32 s86, s86, 4096
WL 4
MA 110,176,54
RT 66,43,24576
MA 114,180,54
RT 68,43,25600
s_add_u32 m0, s52, 49152
MA 174,184,54
buffer_load_dwordx4 v18, s[44:47], s60 offen offset:0 lds
MA 178,188,54
WL 4
MA 118,176,58
s_waitcnt vmcnt(30)
s_barrier
RT 70,20,0
MA 122,180,58
RT 72,20,1024
MA 182,184,58
buffer_load_dwordx4 v19, s[44:47], s60 offen offset:1024 lds
MA 186,188,58
s_add_u32 s60, s60, 229376
WL 4
MA 126,176,62
RT 74,21,0
MA 130,180,62
RT 76,21,1024
MA 190,184,62
buffer_load_dwordx4 a[112:115], v16, s[40:43], s63 offen offset:0 sc1
MA 194,188,62
WL 4
MA 134,176,66
RT 54,22,0
MA 138,180,66
RT 56,22,1024
MA 198,184,66
buffer_load_dwordx4 a[116:119], v16, s[40:43], s63 offen offset:1024 sc1
MA 202,188,66
s_add_u32 s63, s63, 4096
s_waitcnt vmcnt(28) lgkmcnt(4)
MA 78,32,70
RT 58,23,0
MA 82,36,70
RT 60,23,1024
MA 142,40,70
buffer_load_dwordx4 a[120:123], v17, s[40:43], s86 offen offset:0 sc1
MA 146,44,70
WL 4
MA 86,32,74
RT 62,24,0
MA 90,36,74
RT 64,24,1024
MA 150,40,74
buffer_load_dwordx4 a[124:127], v17, s[40:43], s86 offen offset:1024 sc1
MA 154,44,74
s_add_u32 s86, s86, 4096
WL 4
MA 94,32,54
RT 66,25,0
MA 98,36,54
RT 68,25,1024
s_add_u32 m0, s52, 57344
MA 158,40,54
buffer_load_dwordx4 v18, s[44:47], s60 offen offset:0 lds
MA 162,44,54
WL 4
MA 102,32,58
RT 70,26,0
MA 106,36,58
RT 72,26,1024
MA 166,40,58
buffer_load_dwordx4 v19, s[44:47], s60 offen offset:1024 lds
MA 170,44,58
s_add_u32 s60, s60, 229376
WL 4
MA 110,32,62
RT 74,27,0
MA 114,36,62
RT 76,27,1024
MA 174,40,62
buffer_load_dwordx4 a[128:131], v16, s[40:43], s63 offen offset:0 sc1
MA 178,44,62
WL 4
MA 118,32,66
RT 54,20,8192
MA 122,36,66
RT 56,20,9216
MA 182,40,66
buffer_load_dwordx4 a[132:135], v16, s[40:43], s63 offen offset:1024 sc1
MA 186,44,66
s_add_u32 s63, s63, 4096
WL 4
MA 126,32,70
RT 58,21,8192
MA 130,36,70
RT 60,21,9216
MA 190,40,70
buffer_load_dwordx4 a[136:139], v17, s[40:43], s86 offen offset:0 sc1
MA 194,44,70
WL 4
MA 134,32,74
RT 62,22,8192
MA 138,36,74
RT 64,22,9216
MA 198,40,74
buffer_load_dwordx4 a[140:143], v17, s[40:43], s86 offen offset:1024 sc1
MA 202,44,74
s_add_u32 s86, s86, 4096
s_waitcnt vmcnt(32) lgkmcnt(4)
MA 78,48,54
RT 66,23,8192
MA 82,52,54
RT 68,23,9216
s_add_u32 m0, s52, 65536
MA 142,56,54
buffer_load_dwordx4 v18, s[44:47], s60 offen offset:0 lds
MA 146,60,54
WL 4
MA 86,48,58
RT 70,24,8192
MA 90,52,58
RT 72,24,9216
MA 150,56,58
buffer_load_dwordx4 v19, s[44:47], s60 offen offset:1024 lds
MA 154,60,58
s_add_u32 s60, s60, 229376
WL 4
MA 94,48,62
RT 74,25,8192
MA 98,52,62
RT 76,25,9216
MA 158,56,62
buffer_load_dwordx4 a[144:147], v16, s[40:43], s63 offen offset:0 sc1
MA 162,60,62
WL 4
MA 102,48,66
RT 54,26,8192
MA 106,52,66
RT 56,26,9216
MA 166,56,66
buffer_load_dwordx4 a[148:151], v16, s[40:43], s63 offen offset:1024 sc1
MA 170,60,66
s_add_u32 s63, s63, 4096
WL 4
MA 110,48,70
RT 58,27,8192
MA 114,52,70
RT 60,27,9216
MA 174,56,70
buffer_load_dwordx4 a[152:155], v17, s[40:43], s86 offen offset:0 sc1
MA 178,60,70
WL 4
MA 118,48,74
s_waitcnt vmcnt(29)
s_barrier
RT 62,20,16384
MA 122,52,74
RT 64,20,17408
MA 182,56,74
buffer_load_dwordx4 a[156:159], v17, s[40:43], s86 offen offset:1024 sc1
MA 186,60,74
s_add_u32 s86, s86, 4096
WL 4
MA 126,48,54
RT 66,21,16384
MA 130,52,54
RT 68,21,17408
s_add_u32 m0, s52, 73728
MA 190,56,54
buffer_load_dwordx4 v18, s[44:47], s60 offen offset:0 lds
MA 194,60,54
WL 4
MA 134,48,58
RT 70,22,16384
MA 138,52,58
RT 72,22,17408
MA 198,56,58
buffer_load_dwordx4 v19, s[44:47], s60 offen offset:1024 lds
MA 202,60,58
s_add_u32 s60, s60, 229376
WL 4
MA 78,64,62
RT 74,23,16384
MA 82,68,62
RT 76,23,17408
MA 142,72,62
buffer_load_dwordx4 a[160:163], v16, s[40:43], s63 offen offset:0 sc1
MA 146,76,62
WL 4
MA 86,64,66
RT 54,24,16384
MA 90,68,66
RT 56,24,17408
MA 150,72,66
buffer_load_dwordx4 a[164:167], v16, s[40:43], s63 offen offset:1024 sc1
MA 154,76,66
s_add_u32 s63, s63, 4096
WL 4
MA 94,64,70
RT 58,25,16384
MA 98,68,70
RT 60,25,17408
MA 158,72,70
buffer_load_dwordx4 a[168:171], v17, s[40:43], s86 offen offset:0 sc1
MA 162,76,70
WL 4
MA 102,64,74
RT 62,26,16384
MA 106,68,74
RT 64,26,17408
MA 166,72,74
buffer_load_dwordx4 a[172:175], v17, s[40:43], s86 offen offset:1024 sc1
MA 170,76,74
s_add_u32 s86, s86, 4096
WL 4
MA 110,64,54
RT 66,27,16384
MA 114,68,54
RT 68,27,17408
MA 174,72,54
MA 178,76,54
WL 4
MA 118,64,58
RT 70,20,24576
MA 122,68,58
RT 72,20,25600
s_add_u32 m0, s52, 81920
MA 182,72,58
buffer_load_dwordx4 v18, s[44:47], s60 offen offset:0 lds
MA 186,76,58
WL 4
MA 126,64,62
RT 74,21,24576
MA 130,68,62
RT 76,21,25600
MA 190,72,62
buffer_load_dwordx4 v19, s[44:47], s60 offen offset:1024 lds
MA 194,76,62
s_add_u32 s60, s60, 229376
WL 4
MA 134,64,66
RT 54,22,24576
MA 138,68,66
RT 56,22,25600
MA 198,72,66
MA 202,76,66
s_waitcnt vmcnt(32) lgkmcnt(4)
MA 78,80,70
RT 58,23,24576
MA 82,84,70
RT 60,23,25600
MA 142,88,70
buffer_load_dwordx4 a[176:179], v16, s[40:43], s63 offen offset:0 sc1
MA 146,92,70
WL 4
MA 86,80,74
RT 62,24,24576
MA 90,84,74
RT 64,24,25600
MA 150,88,74
buffer_load_dwordx4 a[180:183], v16, s[40:43], s63 offen offset:1024 sc1
MA 154,92,74
s_add_u32 s63, s63, 4096
WL 4
MA 94,80,54
RT 66,25,24576
MA 98,84,54
RT 68,25,25600
MA 158,88,54
buffer_load_dwordx4 a[184:187], v17, s[40:43], s86 offen offset:0 sc1
MA 162,92,54
WL 4
MA 102,80,58
RT 70,26,24576
MA 106,84,58
RT 72,26,25600
MA 166,88,58
buffer_load_dwordx4 a[188:191], v17, s[40:43], s86 offen offset:1024 sc1
MA 170,92,58
s_add_u32 s86, s86, 4096
WL 4
MA 110,80,62
RT 74,27,24576
MA 114,84,62
RT 76,27,25600
MA 174,88,62
MA 178,92,62
WL 4
MA 118,80,66
s_waitcnt vmcnt(34)
s_barrier
RT 54,20,32768
MA 122,84,66
RT 56,20,33792
s_add_u32 m0, s52, 90112
MA 182,88,66
buffer_load_dwordx4 v18, s[44:47], s60 offen offset:0 lds
MA 186,92,66
WL 4
MA 126,80,70
RT 58,21,32768
MA 130,84,70
RT 60,21,33792
MA 190,88,70
buffer_load_dwordx4 v19, s[44:47], s60 offen offset:1024 lds
MA 194,92,70
s_add_u32 s60, s60, 229376
WL 4
MA 134,80,74
RT 62,22,32768
MA 138,84,74
RT 64,22,33792
MA 198,88,74
MA 202,92,74
s_waitcnt vmcnt(32) lgkmcnt(4)
MA 78,96,54
RT 66,23,32768
MA 82,100,54
RT 68,23,33792
MA 142,104,54
buffer_load_dword v50, v49, s[44:47], s58 offen
)"
R"(MA 146,108,54
s_add_u32 s58, s58, 3670016
s_cmp_ge_u32 s58, s61
s_cselect_b32 s58, 0x40000000, s58
WL 4
MA 86,96,58
RT 70,24,32768
MA 90,100,58
RT 72,24,33792
MA 150,104,58
MA 154,108,58
WL 4
MA 94,96,62
RT 74,25,32768
MA 98,100,62
RT 76,25,33792
MA 158,104,62
MA 162,108,62
WL 4
MA 102,96,66
RT 54,26,32768
MA 106,100,66
RT 56,26,33792
MA 166,104,66
MA 170,108,66
WL 4
MA 110,96,70
RT 58,27,32768
MA 114,100,70
RT 60,27,33792
MA 174,104,70
MA 178,108,70
WL 4
MA 118,96,74
RT 62,20,40960
MA 122,100,74
RT 64,20,41984
MA 182,104,74
MA 186,108,74
WL 4
MA 126,96,54
RT 66,21,40960
MA 130,100,54
RT 68,21,41984
MA 190,104,54
MA 194,108,54
WL 4
MA 134,96,58
RT 70,22,40960
MA 138,100,58
RT 72,22,41984
MA 198,104,58
MA 202,108,58
s_waitcnt vmcnt(27) lgkmcnt(4)
MA 78,112,62
RT 74,23,40960
MA 82,116,62
RT 76,23,41984
MA 142,120,62
MA 146,124,62
WL 4
MA 86,112,66
RT 54,24,40960
MA 90,116,66
RT 56,24,41984
MA 150,120,66
MA 154,124,66
WL 4
MA 94,112,70
RT 58,25,40960
MA 98,116,70
RT 60,25,41984
MA 158,120,70
MA 162,124,70
WL 4
MA 102,112,74
RT 62,26,40960
MA 106,116,74
RT 64,26,41984
MA 166,120,74
MA 170,124,74
WL 4
MA 110,112,54
RT 66,27,40960
MA 114,116,54
RT 68,27,41984
MA 174,120,54
MA 178,124,54
WL 4
MA 118,112,58
s_waitcnt vmcnt(25)
s_barrier
RT 70,20,49152
MA 122,116,58
RT 72,20,50176
MA 182,120,58
MA 186,124,58
WL 4
MA 126,112,62
RT 74,21,49152
MA 130,116,62
RT 76,21,50176
MA 190,120,62
MA 194,124,62
WL 4
MA 134,112,66
RT 54,22,49152
MA 138,116,66
RT 56,22,50176
MA 198,120,66
MA 202,124,66
s_waitcnt vmcnt(21) lgkmcnt(4)
MA 78,128,70
RT 58,23,49152
MA 82,132,70
RT 60,23,50176
MA 142,136,70
buffer_load_dwordx4 a[32:35], v16, s[40:43], s63 offen offset:0 sc1
MA 146,140,70
WL 4
MA 86,128,74
RT 62,24,49152
MA 90,132,74
RT 64,24,50176
MA 150,136,74
buffer_load_dwordx4 a[36:39], v16, s[40:43], s63 offen offset:1024 sc1
MA 154,140,74
s_add_u32 s63, s63, 4096
WL 4
MA 94,128,54
RT 66,25,49152
MA 98,132,54
RT 68,25,50176
MA 158,136,54
buffer_load_dwordx4 a[40:43], v17, s[40:43], s86 offen offset:0 sc1
MA 162,140,54
WL 4
MA 102,128,58
RT 70,26,49152
MA 106,132,58
RT 72,26,50176
MA 166,136,58
buffer_load_dwordx4 a[44:47], v17, s[40:43], s86 offen offset:1024 sc1
MA 170,140,58
s_add_u32 s86, s86, 4096
WL 4
MA 110,128,62
RT 74,27,49152
MA 114,132,62
RT 76,27,50176
MA 174,136,62
buffer_load_dwordx4 a[48:51], v16, s[40:43], s63 offen offset:0 sc1
MA 178,140,62
WL 4
MA 118,128,66
RT 54,20,57344
MA 122,132,66
RT 56,20,58368
MA 182,136,66
buffer_load_dwordx4 a[52:55], v16, s[40:43], s63 offen offset:1024 sc1
MA 186,140,66
s_add_u32 s63, s63, 4096
WL 4
MA 126,128,70
RT 58,21,57344
MA 130,132,70
RT 60,21,58368
MA 190,136,70
buffer_load_dwordx4 a[56:59], v17, s[40:43], s86 offen offset:0 sc1
MA 194,140,70
WL 4
MA 134,128,74
RT 62,22,57344
MA 138,132,74
RT 64,22,58368
MA 198,136,74
buffer_load_dwordx4 a[60:63], v17, s[40:43], s86 offen offset:1024 sc1
MA 202,140,74
s_add_u32 s86, s86, 4096
s_waitcnt vmcnt(23) lgkmcnt(4)
MA 78,144,54
RT 66,23,57344
MA 82,148,54
RT 68,23,58368
s_add_u32 m0, s52, 98304
MA 142,152,54
buffer_load_dwordx4 v18, s[44:47], s60 offen offset:0 lds
MA 146,156,54
WL 4
MA 86,144,58
RT 70,24,57344
MA 90,148,58
RT 72,24,58368
MA 150,152,58
buffer_load_dwordx4 v19, s[44:47], s60 offen offset:1024 lds
MA 154,156,58
s_add_u32 s60, s60, 229376
WL 4
MA 94,144,62
RT 74,25,57344
MA 98,148,62
RT 76,25,58368
MA 158,152,62
buffer_load_dwordx4 a[64:67], v16, s[40:43], s63 offen offset:0 sc1
MA 162,156,62
WL 4
MA 102,144,66
RT 54,26,57344
MA 106,148,66
RT 56,26,58368
MA 166,152,66
buffer_load_dwordx4 a[68:71], v16, s[40:43], s63 offen offset:1024 sc1
MA 170,156,66
s_add_u32 s63, s63, 4096
WL 4
MA 110,144,70
RT 58,27,57344
MA 114,148,70
RT 60,27,58368
MA 174,152,70
buffer_load_dwordx4 a[72:75], v17, s[40:43], s86 offen offset:0 sc1
MA 178,156,70
WL 4
MA 118,144,74
s_waitcnt vmcnt(26)
s_barrier
RT 62,28,0
MA 122,148,74
RT 64,28,1024
MA 182,152,74
buffer_load_dwordx4 a[76:79], v17, s[40:43], s86 offen offset:1024 sc1
MA 186,156,74
s_add_u32 s86, s86, 4096
WL 4
MA 126,144,54
RT 66,29,0
MA 130,148,54
RT 68,29,1024
s_add_u32 m0, s52, 106496
MA 190,152,54
buffer_load_dwordx4 v18, s[44:47], s60 offen offset:0 lds
MA 194,156,54
WL 4
MA 134,144,58
RT 70,30,0
MA 138,148,58
RT 72,30,1024
MA 198,152,58
buffer_load_dwordx4 v19, s[44:47], s60 offen offset:1024 lds
MA 202,156,58
s_add_u32 s60, s60, 229376
s_waitcnt vmcnt(25) lgkmcnt(4)
MA 78,160,62
RT 74,31,0
MA 82,164,62
RT 76,31,1024
s_add_u32 m0, s52, 114688
MA 142,168,62
buffer_load_dwordx4 v18, s[44:47], s60 offen offset:0 lds
MA 146,172,62
WL 4
MA 86,160,66
RT 54,32,0
MA 90,164,66
RT 56,32,1024
MA 150,168,66
buffer_load_dwordx4 v19, s[44:47], s60 offen offset:1024 lds
MA 154,172,66
s_add_u32 s60, s60, 229376
WL 4
MA 94,160,70
RT 58,33,0
MA 98,164,70
RT 60,33,1024
MA 158,168,70
buffer_load_dwordx4 a[80:83], v16, s[40:43], s63 offen offset:0 sc1
MA 162,172,70
WL 4
MA 102,160,74
RT 62,34,0
MA 106,164,74
RT 64,34,1024
MA 166,168,74
buffer_load_dwordx4 a[84:87], v16, s[40:43], s63 offen offset:1024 sc1
MA 170,172,74
s_add_u32 s63, s63, 4096
WL 4
MA 110,160,54
RT 66,35,0
MA 114,164,54
RT 68,35,1024
MA 174,168,54
buffer_load_dwordx4 a[88:91], v17, s[40:43], s86 offen offset:0 sc1
MA 178,172,54
WL 4
MA 118,160,58
RT 70,28,8192
MA 122,164,58
RT 72,28,9216
MA 182,168,58
buffer_load_dwordx4 a[92:95], v17, s[40:43], s86 offen offset:1024 sc1
MA 186,172,58
s_add_u32 s86, s86, 4096
WL 4
MA 126,160,62
RT 74,29,8192
MA 130,164,62
RT 76,29,9216
s_add_u32 m0, s52, 122880
MA 190,168,62
buffer_load_dwordx4 v18, s[44:47], s60 offen offset:0 lds
MA 194,172,62
WL 4
MA 134,160,66
RT 54,30,8192
MA 138,164,66
RT 56,30,9216
MA 198,168,66
buffer_load_dwordx4 v19, s[44:47], s60 offen offset:1024 lds
MA 202,172,66
s_add_u32 s60, s60, 229376
s_waitcnt vmcnt(27) lgkmcnt(4)
MA 78,176,70
RT 58,31,8192
MA 82,180,70
RT 60,31,9216
MA 142,184,70
buffer_load_dwordx4 a[96:99], v16, s[40:43], s63 offen offset:0 sc1
MA 146,188,70
WL 4
MA 86,176,74
RT 62,32,8192
MA 90,180,74
RT 64,32,9216
MA 150,184,74
buffer_load_dwordx4 a[100:103], v16, s[40:43], s63 offen offset:1024 sc1
MA 154,188,74
s_add_u32 s63, s63, 4096
WL 4
MA 94,176,54
RT 66,33,8192
MA 98,180,54
RT 68,33,9216
MA 158,184,54
buffer_load_dwordx4 a[104:107], v17, s[40:43], s86 offen offset:0 sc1
MA 162,188,54
WL 4
MA 102,176,58
RT 70,34,8192
MA 106,180,58
RT 72,34,9216
MA 166,184,58
buffer_load_dwordx4 a[108:111], v17, s[40:43], s86 offen offset:1024 sc1
MA 170,188,58
s_add_u32 s86, s86, 4096
WL 4
MA 110,176,62
RT 74,35,8192
MA 114,180,62
RT 76,35,9216
s_add_u32 m0, s52, 131072
MA 174,184,62
buffer_load_dwordx4 v18, s[44:47], s60 offen offset:0 lds
MA 178,188,62
WL 4
MA 118,176,66
s_waitcnt vmcnt(30)
s_barrier
RT 54,28,16384
MA 122,180,66
RT 56,28,17408
MA 182,184,66
buffer_load_dwordx4 v19, s[44:47], s60 offen offset:1024 lds
MA 186,188,66
s_add_u32 s60, s60, 229376
WL 4
MA 126,176,70
RT 58,29,16384
MA 130,180,70
RT 60,29,17408
MA 190,184,70
buffer_load_dwordx4 a[112:115], v16, s[40:43], s63 offen offset:0 sc1
MA 194,188,70
WL 4
MA 134,176,74
RT 62,30,16384
MA 138,180,74
RT 64,30,17408
MA 198,184,74
buffer_load_dwordx4 a[116:119], v16, s[40:43], s63 offen offset:1024 sc1
MA 202,188,74
s_add_u32 s63, s63, 4096
s_waitcnt vmcnt(28) lgkmcnt(4)
MA 78,32,54
RT 66,31,16384
MA 82,36,54
RT 68,31,17408
MA 142,40,54
buffer_load_dwordx4 a[120:123], v17, s[40:43], s86 offen offset:0 sc1
MA 146,44,54
)"
R"(WL 4
MA 86,32,58
RT 70,32,16384
MA 90,36,58
RT 72,32,17408
MA 150,40,58
buffer_load_dwordx4 a[124:127], v17, s[40:43], s86 offen offset:1024 sc1
MA 154,44,58
s_add_u32 s86, s86, 4096
WL 4
MA 94,32,62
RT 74,33,16384
MA 98,36,62
RT 76,33,17408
s_add_u32 m0, s52, 139264
MA 158,40,62
buffer_load_dwordx4 v18, s[44:47], s60 offen offset:0 lds
MA 162,44,62
WL 4
MA 102,32,66
RT 54,34,16384
MA 106,36,66
RT 56,34,17408
MA 166,40,66
buffer_load_dwordx4 v19, s[44:47], s60 offen offset:1024 lds
MA 170,44,66
s_add_u32 s60, s60, 229376
WL 4
MA 110,32,70
RT 58,35,16384
MA 114,36,70
RT 60,35,17408
s_add_u32 m0, s52, 147456
MA 174,40,70
buffer_load_dwordx4 v18, s[44:47], s60 offen offset:0 lds
MA 178,44,70
WL 4
MA 118,32,74
RT 62,28,24576
MA 122,36,74
RT 64,28,25600
MA 182,40,74
buffer_load_dwordx4 v19, s[44:47], s60 offen offset:1024 lds
MA 186,44,74
s_add_u32 s60, s60, 229376
WL 4
MA 126,32,54
RT 66,29,24576
MA 130,36,54
RT 68,29,25600
MA 190,40,54
buffer_load_dwordx4 a[128:131], v17, s[40:43], s86 offen offset:0 sc1
MA 194,44,54
WL 4
MA 134,32,58
RT 70,30,24576
MA 138,36,58
RT 72,30,25600
MA 198,40,58
buffer_load_dwordx4 a[132:135], v17, s[40:43], s86 offen offset:1024 sc1
MA 202,44,58
s_add_u32 s86, s86, 4096
s_waitcnt vmcnt(32) lgkmcnt(4)
MA 78,48,62
RT 74,31,24576
MA 82,52,62
RT 76,31,25600
s_add_u32 m0, s52, 155648
MA 142,56,62
buffer_load_dwordx4 v18, s[44:47], s60 offen offset:0 lds
MA 146,60,62
WL 4
MA 86,48,66
RT 54,32,24576
MA 90,52,66
RT 56,32,25600
MA 150,56,66
buffer_load_dwordx4 v19, s[44:47], s60 offen offset:1024 lds
MA 154,60,66
s_add_u32 s60, s60, 229376
WL 4
MA 94,48,70
RT 58,33,24576
MA 98,52,70
RT 60,33,25600
MA 158,56,70
buffer_load_dwordx4 a[136:139], v17, s[40:43], s86 offen offset:0 sc1
MA 162,60,70
WL 4
MA 102,48,74
RT 62,34,24576
MA 106,52,74
RT 64,34,25600
MA 166,56,74
buffer_load_dwordx4 a[140:143], v17, s[40:43], s86 offen offset:1024 sc1
MA 170,60,74
s_add_u32 s86, s86, 4096
WL 4
MA 110,48,54
RT 66,35,24576
MA 114,52,54
RT 68,35,25600
s_add_u32 m0, s52, 0
MA 174,56,54
buffer_load_dwordx4 v18, s[44:47], s60 offen offset:0 lds
MA 178,60,54
WL 4
MA 118,48,58
s_waitcnt vmcnt(29)
s_barrier
RT 70,28,32768
MA 122,52,58
RT 72,28,33792
MA 182,56,58
buffer_load_dwordx4 v19, s[44:47], s60 offen offset:1024 lds
MA 186,60,58
s_add_u32 s60, s60, 229376
WL 4
MA 126,48,62
RT 74,29,32768
MA 130,52,62
RT 76,29,33792
MA 190,56,62
buffer_load_dwordx4 a[144:147], v17, s[40:43], s86 offen offset:0 sc1
MA 194,60,62
WL 4
MA 134,48,66
RT 54,30,32768
MA 138,52,66
RT 56,30,33792
MA 198,56,66
buffer_load_dwordx4 a[148:151], v17, s[40:43], s86 offen offset:1024 sc1
MA 202,60,66
s_add_u32 s86, s86, 4096
WL 4
MA 78,64,70
RT 58,31,32768
MA 82,68,70
RT 60,31,33792
s_add_u32 m0, s52, 8192
MA 142,72,70
buffer_load_dwordx4 v18, s[44:47], s60 offen offset:0 lds
MA 146,76,70
WL 4
MA 86,64,74
RT 62,32,32768
MA 90,68,74
RT 64,32,33792
MA 150,72,74
buffer_load_dwordx4 v19, s[44:47], s60 offen offset:1024 lds
MA 154,76,74
s_add_u32 s60, s60, 229376
WL 4
MA 94,64,54
RT 66,33,32768
MA 98,68,54
RT 68,33,33792
MA 158,72,54
buffer_load_dwordx4 a[152:155], v17, s[40:43], s86 offen offset:0 sc1
MA 162,76,54
WL 4
MA 102,64,58
RT 70,34,32768
MA 106,68,58
RT 72,34,33792
MA 166,72,58
buffer_load_dwordx4 a[156:159], v17, s[40:43], s86 offen offset:1024 sc1
MA 170,76,58
s_add_u32 s86, s86, 4096
WL 4
MA 110,64,62
RT 74,35,32768
MA 114,68,62
RT 76,35,33792
s_add_u32 m0, s52, 16384
MA 174,72,62
buffer_load_dwordx4 v18, s[44:47], s60 offen offset:0 lds
MA 178,76,62
WL 4
MA 118,64,66
RT 54,28,40960
MA 122,68,66
RT 56,28,41984
MA 182,72,66
buffer_load_dwordx4 v19, s[44:47], s60 offen offset:1024 lds
MA 186,76,66
s_add_u32 s60, s60, 229376
WL 4
MA 126,64,70
RT 58,29,40960
MA 130,68,70
RT 60,29,41984
MA 190,72,70
buffer_load_dwordx4 a[160:163], v17, s[40:43], s86 offen offset:0 sc1
MA 194,76,70
WL 4
MA 134,64,74
RT 62,30,40960
MA 138,68,74
RT 64,30,41984
MA 198,72,74
buffer_load_dwordx4 a[164:167], v17, s[40:43], s86 offen offset:1024 sc1
MA 202,76,74
s_add_u32 s86, s86, 4096
s_waitcnt vmcnt(34) lgkmcnt(4)
MA 78,80,54
RT 66,31,40960
MA 82,84,54
RT 68,31,41984
s_add_u32 m0, s52, 24576
MA 142,88,54
buffer_load_dwordx4 v18, s[44:47], s60 offen offset:0 lds
MA 146,92,54
WL 4
MA 86,80,58
RT 70,32,40960
MA 90,84,58
RT 72,32,41984
MA 150,88,58
buffer_load_dwordx4 v19, s[44:47], s60 offen offset:1024 lds
MA 154,92,58
s_add_u32 s60, s60, 229376
WL 4
MA 94,80,62
RT 74,33,40960
MA 98,84,62
RT 76,33,41984
MA 158,88,62
buffer_load_dwordx4 a[168:171], v17, s[40:43], s86 offen offset:0 sc1
MA 162,92,62
WL 4
MA 102,80,66
RT 54,34,40960
MA 106,84,66
RT 56,34,41984
MA 166,88,66
buffer_load_dwordx4 a[172:175], v17, s[40:43], s86 offen offset:1024 sc1
MA 170,92,66
s_add_u32 s86, s86, 4096
WL 4
MA 110,80,70
RT 58,35,40960
MA 114,84,70
RT 60,35,41984
s_add_u32 m0, s52, 32768
MA 174,88,70
buffer_load_dwordx4 v18, s[44:47], s60 offen offset:0 lds
MA 178,92,70
WL 4
MA 118,80,74
s_waitcnt vmcnt(37)
s_barrier
RT 62,28,49152
MA 122,84,74
RT 64,28,50176
MA 182,88,74
buffer_load_dwordx4 v19, s[44:47], s60 offen offset:1024 lds
MA 186,92,74
s_add_u32 s60, s60, 229376
WL 4
MA 126,80,54
RT 66,29,49152
MA 130,84,54
RT 68,29,50176
MA 190,88,54
buffer_load_dwordx4 a[176:179], v17, s[40:43], s86 offen offset:0 sc1
MA 194,92,54
WL 4
MA 134,80,58
RT 70,30,49152
MA 138,84,58
RT 72,30,50176
MA 198,88,58
buffer_load_dwordx4 a[180:183], v17, s[40:43], s86 offen offset:1024 sc1
MA 202,92,58
s_add_u32 s86, s86, 4096
s_waitcnt vmcnt(36) lgkmcnt(4)
MA 78,96,62
RT 74,31,49152
MA 82,100,62
RT 76,31,50176
s_add_u32 m0, s52, 40960
MA 142,104,62
buffer_load_dwordx4 v18, s[44:47], s60 offen offset:0 lds
MA 146,108,62
WL 4
MA 86,96,66
RT 54,32,49152
MA 90,100,66
RT 56,32,50176
MA 150,104,66
buffer_load_dwordx4 v19, s[44:47], s60 offen offset:1024 lds
MA 154,108,66
s_add_u32 s60, s60, 229376
WL 4
MA 94,96,70
RT 58,33,49152
MA 98,100,70
RT 60,33,50176
MA 158,104,70
buffer_load_dwordx4 a[184:187], v17, s[40:43], s86 offen offset:0 sc1
MA 162,108,70
WL 4
MA 102,96,74
RT 62,34,49152
MA 106,100,74
RT 64,34,50176
MA 166,104,74
buffer_load_dwordx4 a[188:191], v17, s[40:43], s86 offen offset:1024 sc1
MA 170,108,74
s_add_u32 s86, s86, 4096
WL 4
MA 110,96,54
RT 66,35,49152
MA 114,100,54
RT 68,35,50176
s_add_u32 m0, s52, 49152
MA 174,104,54
buffer_load_dwordx4 v18, s[44:47], s60 offen offset:0 lds
MA 178,108,54
WL 4
MA 118,96,58
RT 70,28,57344
MA 122,100,58
RT 72,28,58368
MA 182,104,58
buffer_load_dwordx4 v19, s[44:47], s60 offen offset:1024 lds
MA 186,108,58
s_add_u32 s60, s60, 229376
WL 4
MA 126,96,62
RT 74,29,57344
MA 130,100,62
RT 76,29,58368
MA 190,104,62
buffer_load_dwordx4 a[32:35], v17, s[40:43], s86 offen offset:0 sc1
MA 194,108,62
WL 4
MA 134,96,66
RT 54,30,57344
MA 138,100,66
RT 56,30,58368
MA 198,104,66
buffer_load_dwordx4 a[36:39], v17, s[40:43], s86 offen offset:1024 sc1
MA 202,108,66
s_add_u32 s86, s86, 4096
s_waitcnt vmcnt(38) lgkmcnt(4)
MA 78,112,70
RT 58,31,57344
MA 82,116,70
RT 60,31,58368
s_add_u32 m0, s52, 57344
MA 142,120,70
buffer_load_dwordx4 v18, s[44:47], s60 offen offset:0 lds
MA 146,124,70
WL 4
MA 86,112,74
RT 62,32,57344
MA 90,116,74
RT 64,32,58368
MA 150,120,74
buffer_load_dwordx4 v19, s[44:47], s60 offen offset:1024 lds
MA 154,124,74
s_add_u32 s60, s60, 229376
WL 4
MA 94,112,54
RT 66,33,57344
MA 98,116,54
RT 68,33,58368
MA 158,120,54
buffer_load_dwordx4 a[40:43], v17, s[40:43], s86 offen offset:0 sc1
MA 162,124,54
WL 4
MA 102,112,58
RT 70,34,57344
MA 106,116,58
RT 72,34,58368
MA 166,120,58
buffer_load_dwordx4 a[44:47], v17, s[40:43], s86 offen offset:1024 sc1
MA 170,124,58
s_add_u32 s86, s86, 4096
WL 4
MA 110,112,62
RT 74,35,57344
MA 114,116,62
RT 76,35,58368
s_add_u32 m0, s52, 65536
MA 174,120,62
buffer_load_dwordx4 v18, s[44:47], s60 offen offset:0 lds
MA 178,124,62
WL 4
MA 118,112,66
s_waitcnt vmcnt(35)
s_barrier
RT 54,36,0
MA 122,116,66
RT 56,36,1024
MA 182,120,66
buffer_load_dwordx4 v19, s[44:47], s60 offen offset:1024 lds
MA 186,124,66
)"
R"(s_add_u32 s60, s60, 229376
WL 4
MA 126,112,70
RT 58,37,0
MA 130,116,70
RT 60,37,1024
MA 190,120,70
buffer_load_dwordx4 a[48:51], v17, s[40:43], s86 offen offset:0 sc1
MA 194,124,70
WL 4
MA 134,112,74
RT 62,38,0
MA 138,116,74
RT 64,38,1024
MA 198,120,74
buffer_load_dwordx4 a[52:55], v17, s[40:43], s86 offen offset:1024 sc1
MA 202,124,74
s_add_u32 s86, s86, 4096
WL 4
MA 142,128,54
RT 66,39,0
MA 146,132,54
RT 68,39,1024
WL 4
MA 150,128,58
RT 70,40,0
s_add_u32 m0, s52, 73728
MA 154,132,58
RT 72,40,1024
buffer_load_dwordx4 v18, s[44:47], s60 offen offset:0 lds
WL 4
MA 158,128,62
RT 74,41,0
MA 162,132,62
RT 76,41,1024
WL 4
MA 166,128,66
RT 54,42,0
MA 170,132,66
RT 56,42,1024
buffer_load_dwordx4 v19, s[44:47], s60 offen offset:1024 lds
WL 4
MA 174,128,70
RT 58,43,0
s_add_u32 s60, s60, 229376
MA 178,132,70
RT 60,43,1024
WL 4
MA 182,128,74
RT 62,36,8192
MA 186,132,74
RT 64,36,9216
buffer_load_dwordx4 a[56:59], v17, s[40:43], s86 offen offset:0 sc1
WL 4
MA 190,128,54
RT 66,37,8192
MA 194,132,54
RT 68,37,9216
WL 4
MA 198,128,58
RT 70,38,8192
MA 202,132,58
RT 72,38,9216
buffer_load_dwordx4 a[60:63], v17, s[40:43], s86 offen offset:1024 sc1
s_waitcnt vmcnt(40) lgkmcnt(4)
MA 142,136,62
RT 74,39,8192
s_add_u32 s86, s86, 4096
MA 146,140,62
RT 76,39,9216
WL 4
MA 150,136,66
RT 54,40,8192
s_add_u32 m0, s52, 81920
MA 154,140,66
RT 56,40,9216
buffer_load_dwordx4 v18, s[44:47], s60 offen offset:0 lds
WL 4
MA 158,136,70
RT 58,41,8192
MA 162,140,70
RT 60,41,9216
WL 4
MA 166,136,74
RT 62,42,8192
MA 170,140,74
RT 64,42,9216
buffer_load_dwordx4 v19, s[44:47], s60 offen offset:1024 lds
WL 4
MA 174,136,54
RT 66,43,8192
s_add_u32 s60, s60, 229376
MA 178,140,54
RT 68,43,9216
WL 4
MA 182,136,58
RT 70,36,16384
MA 186,140,58
RT 72,36,17408
buffer_load_dwordx4 a[64:67], v17, s[40:43], s86 offen offset:0 sc1
WL 4
MA 190,136,62
RT 74,37,16384
MA 194,140,62
RT 76,37,17408
WL 4
MA 198,136,66
RT 54,38,16384
MA 202,140,66
RT 56,38,17408
buffer_load_dwordx4 a[68:71], v17, s[40:43], s86 offen offset:1024 sc1
s_waitcnt vmcnt(40) lgkmcnt(4)
MA 142,144,70
RT 58,39,16384
s_add_u32 s86, s86, 4096
MA 146,148,70
RT 60,39,17408
WL 4
MA 150,144,74
RT 62,40,16384
s_add_u32 m0, s52, 90112
MA 154,148,74
RT 64,40,17408
buffer_load_dwordx4 v18, s[44:47], s60 offen offset:0 lds
WL 4
MA 158,144,54
RT 66,41,16384
MA 162,148,54
RT 68,41,17408
WL 4
MA 166,144,58
RT 70,42,16384
MA 170,148,58
RT 72,42,17408
buffer_load_dwordx4 v19, s[44:47], s60 offen offset:1024 lds
WL 4
MA 174,144,62
RT 74,43,16384
s_add_u32 s60, s60, 229376
MA 178,148,62
RT 76,43,17408
WL 4
MA 182,144,66
RT 54,36,24576
MA 186,148,66
RT 56,36,25600
buffer_load_dwordx4 a[72:75], v17, s[40:43], s86 offen offset:0 sc1
WL 4
MA 190,144,70
RT 58,37,24576
MA 194,148,70
RT 60,37,25600
WL 4
MA 198,144,74
RT 62,38,24576
MA 202,148,74
RT 64,38,25600
buffer_load_dwordx4 a[76:79], v17, s[40:43], s86 offen offset:1024 sc1
s_waitcnt vmcnt(40) lgkmcnt(4)
MA 142,152,54
RT 66,39,24576
s_add_u32 s86, s86, 4096
MA 146,156,54
RT 68,39,25600
WL 4
MA 150,152,58
RT 70,40,24576
s_add_u32 m0, s52, 98304
MA 154,156,58
RT 72,40,25600
buffer_load_dwordx4 v18, s[44:47], s60 offen offset:0 lds
WL 4
MA 158,152,62
RT 74,41,24576
MA 162,156,62
RT 76,41,25600
WL 4
MA 166,152,66
RT 54,42,24576
MA 170,156,66
RT 56,42,25600
buffer_load_dwordx4 v19, s[44:47], s60 offen offset:1024 lds
WL 4
MA 174,152,70
RT 58,43,24576
s_add_u32 s60, s60, 229376
MA 178,156,70
RT 60,43,25600
WL 4
MA 182,152,74
s_waitcnt vmcnt(36)
s_barrier
RT 62,20,0
MA 186,156,74
RT 64,20,1024
buffer_load_dwordx4 a[80:83], v17, s[40:43], s86 offen offset:0 sc1
WL 4
MA 190,152,54
RT 66,21,0
MA 194,156,54
RT 68,21,1024
WL 4
MA 198,152,58
RT 70,22,0
MA 202,156,58
RT 72,22,1024
buffer_load_dwordx4 a[84:87], v17, s[40:43], s86 offen offset:1024 sc1
WL 4
MA 142,160,62
RT 74,23,0
s_add_u32 s86, s86, 4096
MA 146,164,62
RT 76,23,1024
WL 4
MA 150,160,66
RT 54,24,0
s_add_u32 m0, s52, 106496
MA 154,164,66
RT 56,24,1024
buffer_load_dwordx4 v18, s[44:47], s60 offen offset:0 lds
WL 4
MA 158,160,70
RT 58,25,0
MA 162,164,70
RT 60,25,1024
WL 4
MA 166,160,74
RT 62,26,0
MA 170,164,74
RT 64,26,1024
buffer_load_dwordx4 v19, s[44:47], s60 offen offset:1024 lds
WL 4
MA 174,160,54
RT 66,27,0
s_add_u32 s60, s60, 229376
MA 178,164,54
RT 68,27,1024
WL 4
MA 182,160,58
RT 70,20,8192
MA 186,164,58
RT 72,20,9216
buffer_load_dwordx4 a[88:91], v17, s[40:43], s86 offen offset:0 sc1
WL 4
MA 190,160,62
RT 74,21,8192
MA 194,164,62
RT 76,21,9216
WL 4
MA 198,160,66
RT 54,22,8192
MA 202,164,66
RT 56,22,9216
buffer_load_dwordx4 a[92:95], v17, s[40:43], s86 offen offset:1024 sc1
s_waitcnt vmcnt(40) lgkmcnt(4)
MA 142,168,70
RT 58,23,8192
s_add_u32 s86, s86, 4096
MA 146,172,70
RT 60,23,9216
WL 4
MA 150,168,74
RT 62,24,8192
s_add_u32 m0, s52, 114688
MA 154,172,74
RT 64,24,9216
buffer_load_dwordx4 v18, s[44:47], s60 offen offset:0 lds
WL 4
MA 158,168,54
RT 66,25,8192
MA 162,172,54
RT 68,25,9216
WL 4
MA 166,168,58
RT 70,26,8192
MA 170,172,58
RT 72,26,9216
buffer_load_dwordx4 v19, s[44:47], s60 offen offset:1024 lds
WL 4
MA 174,168,62
RT 74,27,8192
s_add_u32 s60, s60, 229376
MA 178,172,62
RT 76,27,9216
WL 4
MA 182,168,66
RT 54,20,16384
MA 186,172,66
RT 56,20,17408
buffer_load_dwordx4 a[96:99], v17, s[40:43], s86 offen offset:0 sc1
WL 4
MA 190,168,70
RT 58,21,16384
MA 194,172,70
RT 60,21,17408
WL 4
MA 198,168,74
RT 62,22,16384
MA 202,172,74
RT 64,22,17408
buffer_load_dwordx4 a[100:103], v17, s[40:43], s86 offen offset:1024 sc1
s_waitcnt vmcnt(40) lgkmcnt(4)
MA 142,176,54
RT 66,23,16384
s_add_u32 s86, s86, 4096
MA 146,180,54
RT 68,23,17408
WL 4
MA 150,176,58
RT 70,24,16384
s_add_u32 m0, s52, 122880
MA 154,180,58
RT 72,24,17408
buffer_load_dwordx4 v18, s[44:47], s60 offen offset:0 lds
WL 4
MA 158,176,62
RT 74,25,16384
MA 162,180,62
RT 76,25,17408
WL 4
MA 166,176,66
RT 54,26,16384
MA 170,180,66
RT 56,26,17408
buffer_load_dwordx4 v19, s[44:47], s60 offen offset:1024 lds
WL 4
MA 174,176,70
RT 58,27,16384
s_add_u32 s60, s60, 229376
MA 178,180,70
RT 60,27,17408
WL 4
MA 182,176,74
RT 62,20,24576
MA 186,180,74
RT 64,20,25600
buffer_load_dwordx4 a[104:107], v17, s[40:43], s86 offen offset:0 sc1
WL 4
MA 190,176,54
RT 66,21,24576
MA 194,180,54
RT 68,21,25600
WL 4
MA 198,176,58
RT 70,22,24576
MA 202,180,58
RT 72,22,25600
buffer_load_dwordx4 a[108:111], v17, s[40:43], s86 offen offset:1024 sc1
s_waitcnt vmcnt(40) lgkmcnt(4)
MA 142,184,62
RT 74,23,24576
s_add_u32 s86, s86, 4096
MA 146,188,62
RT 76,23,25600
WL 4
MA 150,184,66
RT 54,24,24576
MA 154,188,66
RT 56,24,25600
buffer_load_dwordx4 a[112:115], v17, s[40:43], s86 offen offset:0 sc1
WL 4
MA 158,184,70
RT 58,25,24576
MA 162,188,70
RT 60,25,25600
WL 4
MA 166,184,74
RT 62,26,24576
MA 170,188,74
RT 64,26,25600
buffer_load_dwordx4 a[116:119], v17, s[40:43], s86 offen offset:1024 sc1
WL 4
MA 174,184,54
RT 66,27,24576
s_add_u32 s86, s86, 4096
MA 178,188,54
RT 68,27,25600
WL 4
MA 182,184,58
s_waitcnt vmcnt(36)
s_barrier
RT 70,20,32768
MA 186,188,58
RT 72,20,33792
buffer_load_dwordx4 a[120:123], v17, s[40:43], s86 offen offset:0 sc1
WL 4
MA 190,184,62
RT 74,21,32768
MA 194,188,62
RT 76,21,33792
WL 4
MA 198,184,66
RT 54,22,32768
MA 202,188,66
RT 56,22,33792
buffer_load_dwordx4 a[124:127], v17, s[40:43], s86 offen offset:1024 sc1
WL 4
MA 142,32,70
RT 58,23,32768
s_add_u32 s86, s86, 4096
)"
R"(MA 146,36,70
RT 60,23,33792
WL 4
MA 150,32,74
RT 62,24,32768
MA 154,36,74
RT 64,24,33792
buffer_load_dwordx4 a[128:131], v16, s[40:43], s63 offen offset:0 sc1
WL 4
MA 158,32,54
RT 66,25,32768
MA 162,36,54
RT 68,25,33792
WL 4
MA 166,32,58
RT 70,26,32768
MA 170,36,58
RT 72,26,33792
buffer_load_dwordx4 a[132:135], v16, s[40:43], s63 offen offset:1024 sc1
WL 4
MA 174,32,62
RT 74,27,32768
s_add_u32 s63, s63, 4096
MA 178,36,62
RT 76,27,33792
WL 4
MA 182,32,66
RT 54,20,40960
MA 186,36,66
RT 56,20,41984
WL 4
MA 190,32,70
RT 58,21,40960
MA 194,36,70
RT 60,21,41984
WL 4
MA 198,32,74
RT 62,22,40960
MA 202,36,74
RT 64,22,41984
s_waitcnt vmcnt(38) lgkmcnt(4)
MA 142,40,54
RT 66,23,40960
MA 146,44,54
RT 68,23,41984
buffer_load_dwordx4 a[136:139], v16, s[40:43], s63 offen offset:0 sc1
WL 4
MA 150,40,58
RT 70,24,40960
MA 154,44,58
RT 72,24,41984
WL 4
MA 158,40,62
RT 74,25,40960
MA 162,44,62
RT 76,25,41984
buffer_load_dwordx4 a[140:143], v16, s[40:43], s63 offen offset:1024 sc1
WL 4
MA 166,40,66
RT 54,26,40960
s_add_u32 s63, s63, 4096
MA 170,44,66
RT 56,26,41984
WL 4
MA 174,40,70
RT 58,27,40960
MA 178,44,70
RT 60,27,41984
WL 4
MA 182,40,74
RT 62,20,49152
MA 186,44,74
RT 64,20,50176
WL 4
MA 190,40,54
RT 66,21,49152
MA 194,44,54
RT 68,21,50176
WL 4
MA 198,40,58
RT 70,22,49152
MA 202,44,58
RT 72,22,50176
s_waitcnt vmcnt(36) lgkmcnt(4)
MA 142,48,62
RT 74,23,49152
MA 146,52,62
RT 76,23,50176
buffer_load_dwordx4 a[144:147], v16, s[40:43], s63 offen offset:0 sc1
WL 4
MA 150,48,66
RT 54,24,49152
MA 154,52,66
RT 56,24,50176
WL 4
MA 158,48,70
RT 58,25,49152
MA 162,52,70
RT 60,25,50176
buffer_load_dwordx4 a[148:151], v16, s[40:43], s63 offen offset:1024 sc1
WL 4
MA 166,48,74
RT 62,26,49152
s_add_u32 s63, s63, 4096
MA 170,52,74
RT 64,26,50176
WL 4
MA 174,48,54
RT 66,27,49152
MA 178,52,54
RT 68,27,50176
WL 4
MA 182,48,58
RT 70,20,57344
MA 186,52,58
RT 72,20,58368
WL 4
MA 190,48,62
RT 74,21,57344
MA 194,52,62
RT 76,21,58368
WL 4
MA 198,48,66
RT 54,22,57344
MA 202,52,66
RT 56,22,58368
s_branch L_join_%=
L_cls3_%=:
s_waitcnt vmcnt(12) lgkmcnt(4)
v_mov_b32 v206, s98
v_mov_b32 v207, s99
s_mov_b64 exec, 1
buffer_store_dwordx2 v[206:207], off, s[92:95], s89 offset:256 sc1
s_mov_b64 exec, -1
v_mfma_f32_16x16x32_bf16 v[78:81], a[32:35], v[54:57], 0
RT 66,23,0
v_mfma_f32_16x16x32_bf16 v[82:85], a[36:39], v[54:57], 0
RT 68,23,1024
v_mfma_f32_16x16x32_bf16 v[142:145], a[40:43], v[54:57], 0
buffer_load_dwordx4 a[96:99], v16, s[40:43], s63 offen offset:0 sc1
v_mfma_f32_16x16x32_bf16 v[146:149], a[44:47], v[54:57], 0
WL 4
v_mfma_f32_16x16x32_bf16 v[86:89], a[32:35], v[58:61], 0
RT 70,24,0
v_mfma_f32_16x16x32_bf16 v[90:93], a[36:39], v[58:61], 0
RT 72,24,1024
v_mfma_f32_16x16x32_bf16 v[150:153], a[40:43], v[58:61], 0
buffer_load_dwordx4 a[100:103], v16, s[40:43], s63 offen offset:1024 sc1
v_mfma_f32_16x16x32_bf16 v[154:157], a[44:47], v[58:61], 0
s_add_u32 s63, s63, 4096
WL 4
v_mfma_f32_16x16x32_bf16 v[94:97], a[32:35], v[62:65], 0
RT 74,25,0
v_mfma_f32_16x16x32_bf16 v[98:101], a[36:39], v[62:65], 0
RT 76,25,1024
v_mfma_f32_16x16x32_bf16 v[158:161], a[40:43], v[62:65], 0
buffer_load_dwordx4 a[104:107], v17, s[40:43], s86 offen offset:0 sc1
v_mfma_f32_16x16x32_bf16 v[162:165], a[44:47], v[62:65], 0
WL 4
v_mfma_f32_16x16x32_bf16 v[102:105], a[32:35], v[66:69], 0
RT 54,26,0
v_mfma_f32_16x16x32_bf16 v[106:109], a[36:39], v[66:69], 0
RT 56,26,1024
v_mfma_f32_16x16x32_bf16 v[166:169], a[40:43], v[66:69], 0
buffer_load_dwordx4 a[108:111], v17, s[40:43], s86 offen offset:1024 sc1
v_mfma_f32_16x16x32_bf16 v[170:173], a[44:47], v[66:69], 0
s_add_u32 s86, s86, 4096
WL 4
v_mfma_f32_16x16x32_bf16 v[110:113], a[32:35], v[70:73], 0
RT 58,27,0
v_mfma_f32_16x16x32_bf16 v[114:117], a[36:39], v[70:73], 0
RT 60,27,1024
v_mfma_f32_16x16x32_bf16 v[174:177], a[40:43], v[70:73], 0
buffer_load_dwordx4 a[112:115], v16, s[40:43], s63 offen offset:0 sc1
v_mfma_f32_16x16x32_bf16 v[178:181], a[44:47], v[70:73], 0
WL 4
v_mfma_f32_16x16x32_bf16 v[118:121], a[32:35], v[74:77], 0
RT 62,20,8192
v_mfma_f32_16x16x32_bf16 v[122:125], a[36:39], v[74:77], 0
RT 64,20,9216
v_mfma_f32_16x16x32_bf16 v[182:185], a[40:43], v[74:77], 0
buffer_load_dwordx4 a[116:119], v16, s[40:43], s63 offen offset:1024 sc1
v_mfma_f32_16x16x32_bf16 v[186:189], a[44:47], v[74:77], 0
s_add_u32 s63, s63, 4096
WL 4
v_mfma_f32_16x16x32_bf16 v[126:129], a[32:35], v[54:57], 0
RT 66,21,8192
v_mfma_f32_16x16x32_bf16 v[130:133], a[36:39], v[54:57], 0
RT 68,21,9216
v_mfma_f32_16x16x32_bf16 v[190:193], a[40:43], v[54:57], 0
buffer_load_dwordx4 a[120:123], v17, s[40:43], s86 offen offset:0 sc1
v_mfma_f32_16x16x32_bf16 v[194:197], a[44:47], v[54:57], 0
WL 4
v_mfma_f32_16x16x32_bf16 v[134:137], a[32:35], v[58:61], 0
RT 70,22,8192
v_mfma_f32_16x16x32_bf16 v[138:141], a[36:39], v[58:61], 0
RT 72,22,9216
v_mfma_f32_16x16x32_bf16 v[198:201], a[40:43], v[58:61], 0
buffer_load_dwordx4 a[124:127], v17, s[40:43], s86 offen offset:1024 sc1
v_mfma_f32_16x16x32_bf16 v[202:205], a[44:47], v[58:61], 0
s_add_u32 s86, s86, 4096
s_waitcnt vmcnt(17) lgkmcnt(4)
MA 78,48,62
RT 74,23,8192
MA 82,52,62
RT 76,23,9216
MA 142,56,62
buffer_load_dwordx4 a[128:131], v16, s[40:43], s63 offen offset:0 sc1
MA 146,60,62
WL 4
MA 86,48,66
RT 54,24,8192
MA 90,52,66
RT 56,24,9216
MA 150,56,66
buffer_load_dwordx4 a[132:135], v16, s[40:43], s63 offen offset:1024 sc1
MA 154,60,66
s_add_u32 s63, s63, 4096
WL 4
MA 94,48,70
RT 58,25,8192
MA 98,52,70
RT 60,25,9216
MA 158,56,70
buffer_load_dwordx4 a[136:139], v17, s[40:43], s86 offen offset:0 sc1
MA 162,60,70
WL 4
MA 102,48,74
RT 62,26,8192
MA 106,52,74
RT 64,26,9216
MA 166,56,74
buffer_load_dwordx4 a[140:143], v17, s[40:43], s86 offen offset:1024 sc1
MA 170,60,74
s_add_u32 s86, s86, 4096
WL 4
MA 110,48,54
RT 66,27,8192
MA 114,52,54
RT 68,27,9216
s_add_u32 m0, s52, 65536
MA 174,56,54
buffer_load_dwordx4 v18, s[44:47], s60 offen offset:0 lds
MA 178,60,54
WL 4
MA 118,48,58
s_barrier
RT 70,20,16384
MA 122,52,58
RT 72,20,17408
MA 182,56,58
buffer_load_dwordx4 v19, s[44:47], s60 offen offset:1024 lds
MA 186,60,58
s_add_u32 s60, s60, 229376
WL 4
MA 126,48,62
RT 74,21,16384
MA 130,52,62
RT 76,21,17408
MA 190,56,62
buffer_load_dwordx4 a[144:147], v16, s[40:43], s63 offen offset:0 sc1
MA 194,60,62
WL 4
MA 134,48,66
RT 54,22,16384
MA 138,52,66
RT 56,22,17408
MA 198,56,66
buffer_load_dwordx4 a[148:151], v16, s[40:43], s63 offen offset:1024 sc1
MA 202,60,66
s_add_u32 s63, s63, 4096
s_waitcnt vmcnt(21) lgkmcnt(4)
MA 78,64,70
RT 58,23,16384
MA 82,68,70
RT 60,23,17408
MA 142,72,70
buffer_load_dwordx4 a[152:155], v17, s[40:43], s86 offen offset:0 sc1
MA 146,76,70
WL 4
MA 86,64,74
RT 62,24,16384
MA 90,68,74
RT 64,24,17408
MA 150,72,74
buffer_load_dwordx4 a[156:159], v17, s[40:43], s86 offen offset:1024 sc1
MA 154,76,74
s_add_u32 s86, s86, 4096
WL 4
MA 94,64,54
RT 66,25,16384
MA 98,68,54
RT 68,25,17408
s_add_u32 m0, s52, 73728
MA 158,72,54
buffer_load_dwordx4 v18, s[44:47], s60 offen offset:0 lds
MA 162,76,54
WL 4
MA 102,64,58
RT 70,26,16384
MA 106,68,58
RT 72,26,17408
MA 166,72,58
buffer_load_dwordx4 v19, s[44:47], s60 offen offset:1024 lds
MA 170,76,58
s_add_u32 s60, s60, 229376
WL 4
MA 110,64,62
RT 74,27,16384
MA 114,68,62
RT 76,27,17408
MA 174,72,62
buffer_load_dwordx4 a[160:163], v16, s[40:43], s63 offen offset:0 sc1
MA 178,76,62
WL 4
MA 118,64,66
RT 54,20,24576
MA 122,68,66
RT 56,20,25600
MA 182,72,66
buffer_load_dwordx4 a[164:167], v16, s[40:43], s63 offen offset:1024 sc1
MA 186,76,66
s_add_u32 s63, s63, 4096
WL 4
MA 126,64,70
RT 58,21,24576
MA 130,68,70
RT 60,21,25600
MA 190,72,70
buffer_load_dwordx4 a[168:171], v17, s[40:43], s86 offen offset:0 sc1
MA 194,76,70
WL 4
MA 134,64,74
RT 62,22,24576
MA 138,68,74
RT 64,22,25600
MA 198,72,74
buffer_load_dwordx4 a[172:175], v17, s[40:43], s86 offen offset:1024 sc1
MA 202,76,74
s_add_u32 s86, s86, 4096
buffer_load_dwordx2 v[44:45], v47, s[92:95], 0 offen offset:256 sc1
s_waitcnt vmcnt(26) lgkmcnt(4)
MA 78,80,54
RT 66,23,24576
MA 82,84,54
RT 68,23,25600
s_add_u32 m0, s52, 81920
MA 142,88,54
buffer_load_dwordx4 v18, s[44:47], s60 offen offset:0 lds
MA 146,92,54
WL 4
MA 86,80,58
RT 70,24,24576
MA 90,84,58
RT 72,24,25600
MA 150,88,58
buffer_load_dwordx4 v19, s[44:47], s60 offen offset:1024 lds
MA 154,92,58
s_add_u32 s60, s60, 229376
WL 4
MA 94,80,62
RT 74,25,24576
MA 98,84,62
RT 76,25,25600
MA 158,88,62
buffer_load_dwordx4 a[176:179], v16, s[40:43], s63 offen offset:0 sc1
MA 162,92,62
WL 4
MA 102,80,66
RT 54,26,24576
MA 106,84,66
RT 56,26,25600
MA 166,88,66
buffer_load_dwordx4 a[180:183], v16, s[40:43], s63 offen offset:1024 sc1
MA 170,92,66
s_add_u32 s63, s63, 4096
WL 4
MA 110,80,70
RT 58,27,24576
MA 114,84,70
RT 60,27,25600
MA 174,88,70
buffer_load_dwordx4 a[184:187], v17, s[40:43], s86 offen offset:0 sc1
MA 178,92,70
WL 4
MA 118,80,74
s_barrier
RT 62,20,32768
MA 122,84,74
RT 64,20,33792
MA 182,88,74
buffer_load_dwordx4 a[188:191], v17, s[40:43], s86 offen offset:1024 sc1
MA 186,92,74
s_add_u32 s86, s86, 4096
WL 4
MA 126,80,54
RT 66,21,32768
MA 130,84,54
RT 68,21,33792
s_add_u32 m0, s52, 90112
)"
R"(MA 190,88,54
buffer_load_dwordx4 v18, s[44:47], s60 offen offset:0 lds
MA 194,92,54
WL 4
MA 134,80,58
RT 70,22,32768
MA 138,84,58
RT 72,22,33792
MA 198,88,58
buffer_load_dwordx4 v19, s[44:47], s60 offen offset:1024 lds
MA 202,92,58
s_add_u32 s60, s60, 229376
s_waitcnt vmcnt(29) lgkmcnt(4)
MA 78,96,62
RT 74,23,32768
MA 82,100,62
RT 76,23,33792
MA 142,104,62
buffer_load_dwordx4 a[32:35], v16, s[40:43], s63 offen offset:0 sc1
MA 146,108,62
WL 4
MA 86,96,66
RT 54,24,32768
MA 90,100,66
RT 56,24,33792
MA 150,104,66
buffer_load_dwordx4 a[36:39], v16, s[40:43], s63 offen offset:1024 sc1
MA 154,108,66
s_add_u32 s63, s63, 4096
WL 4
MA 94,96,70
RT 58,25,32768
MA 98,100,70
RT 60,25,33792
MA 158,104,70
buffer_load_dwordx4 a[40:43], v17, s[40:43], s86 offen offset:0 sc1
MA 162,108,70
WL 4
MA 102,96,74
RT 62,26,32768
MA 106,100,74
RT 64,26,33792
MA 166,104,74
buffer_load_dwordx4 a[44:47], v17, s[40:43], s86 offen offset:1024 sc1
MA 170,108,74
s_add_u32 s86, s86, 4096
WL 4
MA 110,96,54
RT 66,27,32768
MA 114,100,54
RT 68,27,33792
MA 174,104,54
MA 178,108,54
WL 4
MA 118,96,58
RT 70,20,40960
MA 122,100,58
RT 72,20,41984
s_add_u32 m0, s52, 98304
MA 182,104,58
buffer_load_dwordx4 v18, s[44:47], s60 offen offset:0 lds
MA 186,108,58
WL 4
MA 126,96,62
RT 74,21,40960
MA 130,100,62
RT 76,21,41984
MA 190,104,62
buffer_load_dwordx4 v19, s[44:47], s60 offen offset:1024 lds
MA 194,108,62
s_add_u32 s60, s60, 229376
WL 4
MA 134,96,66
RT 54,22,40960
MA 138,100,66
RT 56,22,41984
MA 198,104,66
MA 202,108,66
s_waitcnt vmcnt(31) lgkmcnt(4)
MA 78,112,70
RT 58,23,40960
MA 82,116,70
RT 60,23,41984
MA 142,120,70
buffer_load_dword v50, v49, s[44:47], s58 offen
MA 146,124,70
s_add_u32 s58, s58, 3670016
s_cmp_ge_u32 s58, s61
s_cselect_b32 s58, 0x40000000, s58
WL 4
MA 86,112,74
RT 62,24,40960
MA 90,116,74
RT 64,24,41984
MA 150,120,74
MA 154,124,74
WL 4
MA 94,112,54
RT 66,25,40960
MA 98,116,54
RT 68,25,41984
MA 158,120,54
MA 162,124,54
WL 4
MA 102,112,58
RT 70,26,40960
MA 106,116,58
RT 72,26,41984
MA 166,120,58
MA 170,124,58
WL 4
MA 110,112,62
RT 74,27,40960
MA 114,116,62
RT 76,27,41984
MA 174,120,62
MA 178,124,62
WL 4
MA 118,112,66
s_barrier
RT 54,20,49152
MA 122,116,66
RT 56,20,50176
MA 182,120,66
MA 186,124,66
WL 4
MA 126,112,70
RT 58,21,49152
MA 130,116,70
RT 60,21,50176
MA 190,120,70
MA 194,124,70
WL 4
MA 134,112,74
RT 62,22,49152
MA 138,116,74
RT 64,22,50176
MA 198,120,74
MA 202,124,74
s_waitcnt vmcnt(28) lgkmcnt(4)
MA 78,128,54
RT 66,23,49152
MA 82,132,54
RT 68,23,50176
MA 142,136,54
MA 146,140,54
WL 4
MA 86,128,58
RT 70,24,49152
MA 90,132,58
RT 72,24,50176
MA 150,136,58
MA 154,140,58
WL 4
MA 94,128,62
RT 74,25,49152
MA 98,132,62
RT 76,25,50176
MA 158,136,62
MA 162,140,62
WL 4
MA 102,128,66
RT 54,26,49152
MA 106,132,66
RT 56,26,50176
MA 166,136,66
MA 170,140,66
WL 4
MA 110,128,70
RT 58,27,49152
MA 114,132,70
RT 60,27,50176
MA 174,136,70
MA 178,140,70
WL 4
MA 118,128,74
RT 62,20,57344
MA 122,132,74
RT 64,20,58368
MA 182,136,74
MA 186,140,74
WL 4
MA 126,128,54
RT 66,21,57344
MA 130,132,54
RT 68,21,58368
MA 190,136,54
MA 194,140,54
WL 4
MA 134,128,58
RT 70,22,57344
MA 138,132,58
RT 72,22,58368
MA 198,136,58
MA 202,140,58
s_waitcnt vmcnt(22) lgkmcnt(4)
MA 78,144,62
RT 74,23,57344
MA 82,148,62
RT 76,23,58368
MA 142,152,62
buffer_load_dwordx4 a[48:51], v16, s[40:43], s63 offen offset:0 sc1
MA 146,156,62
WL 4
MA 86,144,66
RT 54,24,57344
MA 90,148,66
RT 56,24,58368
MA 150,152,66
buffer_load_dwordx4 a[52:55], v16, s[40:43], s63 offen offset:1024 sc1
MA 154,156,66
s_add_u32 s63, s63, 4096
WL 4
MA 94,144,70
RT 58,25,57344
MA 98,148,70
RT 60,25,58368
MA 158,152,70
buffer_load_dwordx4 a[56:59], v17, s[40:43], s86 offen offset:0 sc1
MA 162,156,70
WL 4
MA 102,144,74
RT 62,26,57344
MA 106,148,74
RT 64,26,58368
MA 166,152,74
buffer_load_dwordx4 a[60:63], v17, s[40:43], s86 offen offset:1024 sc1
MA 170,156,74
s_add_u32 s86, s86, 4096
WL 4
MA 110,144,54
RT 66,27,57344
MA 114,148,54
RT 68,27,58368
MA 174,152,54
buffer_load_dwordx4 a[64:67], v16, s[40:43], s63 offen offset:0 sc1
MA 178,156,54
WL 4
MA 118,144,58
s_waitcnt vmcnt(25)
s_barrier
RT 70,28,0
MA 122,148,58
RT 72,28,1024
MA 182,152,58
buffer_load_dwordx4 a[68:71], v16, s[40:43], s63 offen offset:1024 sc1
MA 186,156,58
s_add_u32 s63, s63, 4096
WL 4
MA 126,144,62
RT 74,29,0
MA 130,148,62
RT 76,29,1024
MA 190,152,62
buffer_load_dwordx4 a[72:75], v17, s[40:43], s86 offen offset:0 sc1
MA 194,156,62
WL 4
MA 134,144,66
RT 54,30,0
MA 138,148,66
RT 56,30,1024
MA 198,152,66
buffer_load_dwordx4 a[76:79], v17, s[40:43], s86 offen offset:1024 sc1
MA 202,156,66
s_add_u32 s86, s86, 4096
s_waitcnt vmcnt(23)
v_cmp_ne_u64 vcc, s[98:99], v[44:45]
s_cmp_eq_u64 vcc, 0
s_cbranch_scc0 L_slowgB3_%=
L_backgB3_%=:
WL 4
MA 78,160,70
RT 58,31,0
MA 82,164,70
RT 60,31,1024
s_add_u32 m0, s52, 106496
MA 142,168,70
buffer_load_dwordx4 v18, s[44:47], s60 offen offset:0 lds
MA 146,172,70
WL 4
MA 86,160,74
RT 62,32,0
MA 90,164,74
RT 64,32,1024
MA 150,168,74
buffer_load_dwordx4 v19, s[44:47], s60 offen offset:1024 lds
MA 154,172,74
s_add_u32 s60, s60, 229376
WL 4
MA 94,160,54
RT 66,33,0
MA 98,164,54
RT 68,33,1024
MA 158,168,54
buffer_load_dwordx4 a[80:83], v16, s[40:43], s63 offen offset:0 sc1
MA 162,172,54
WL 4
MA 102,160,58
RT 70,34,0
MA 106,164,58
RT 72,34,1024
MA 166,168,58
buffer_load_dwordx4 a[84:87], v16, s[40:43], s63 offen offset:1024 sc1
MA 170,172,58
s_add_u32 s63, s63, 4096
WL 4
MA 110,160,62
RT 74,35,0
MA 114,164,62
RT 76,35,1024
MA 174,168,62
buffer_load_dwordx4 a[88:91], v17, s[40:43], s86 offen offset:0 sc1
MA 178,172,62
WL 4
MA 118,160,66
RT 54,28,8192
MA 122,164,66
RT 56,28,9216
MA 182,168,66
buffer_load_dwordx4 a[92:95], v17, s[40:43], s86 offen offset:1024 sc1
MA 186,172,66
s_add_u32 s86, s86, 4096
WL 4
MA 126,160,70
RT 58,29,8192
MA 130,164,70
RT 60,29,9216
s_add_u32 m0, s52, 114688
MA 190,168,70
buffer_load_dwordx4 v18, s[44:47], s60 offen offset:0 lds
MA 194,172,70
WL 4
MA 134,160,74
RT 62,30,8192
MA 138,164,74
RT 64,30,9216
MA 198,168,74
buffer_load_dwordx4 v19, s[44:47], s60 offen offset:1024 lds
MA 202,172,74
s_add_u32 s60, s60, 229376
s_waitcnt vmcnt(25) lgkmcnt(4)
MA 78,176,54
RT 66,31,8192
MA 82,180,54
RT 68,31,9216
s_add_u32 m0, s52, 122880
MA 142,184,54
buffer_load_dwordx4 v18, s[44:47], s60 offen offset:0 lds
MA 146,188,54
WL 4
MA 86,176,58
RT 70,32,8192
MA 90,180,58
RT 72,32,9216
MA 150,184,58
buffer_load_dwordx4 v19, s[44:47], s60 offen offset:1024 lds
MA 154,188,58
s_add_u32 s60, s60, 229376
WL 4
MA 94,176,62
RT 74,33,8192
MA 98,180,62
RT 76,33,9216
MA 158,184,62
buffer_load_dwordx4 a[96:99], v16, s[40:43], s63 offen offset:0 sc1
MA 162,188,62
WL 4
MA 102,176,66
RT 54,34,8192
MA 106,180,66
RT 56,34,9216
MA 166,184,66
buffer_load_dwordx4 a[100:103], v16, s[40:43], s63 offen offset:1024 sc1
MA 170,188,66
s_add_u32 s63, s63, 4096
WL 4
MA 110,176,70
RT 58,35,8192
MA 114,180,70
RT 60,35,9216
MA 174,184,70
buffer_load_dwordx4 a[104:107], v17, s[40:43], s86 offen offset:0 sc1
MA 178,188,70
WL 4
MA 118,176,74
s_waitcnt vmcnt(28)
s_barrier
RT 62,28,16384
MA 122,180,74
RT 64,28,17408
MA 182,184,74
buffer_load_dwordx4 a[108:111], v17, s[40:43], s86 offen offset:1024 sc1
MA 186,188,74
s_add_u32 s86, s86, 4096
WL 4
MA 126,176,54
RT 66,29,16384
MA 130,180,54
RT 68,29,17408
s_add_u32 m0, s52, 131072
)"
R"(MA 190,184,54
buffer_load_dwordx4 v18, s[44:47], s60 offen offset:0 lds
MA 194,188,54
WL 4
MA 134,176,58
RT 70,30,16384
MA 138,180,58
RT 72,30,17408
MA 198,184,58
buffer_load_dwordx4 v19, s[44:47], s60 offen offset:1024 lds
MA 202,188,58
s_add_u32 s60, s60, 229376
s_waitcnt vmcnt(27) lgkmcnt(4)
MA 78,32,62
RT 74,31,16384
MA 82,36,62
RT 76,31,17408
MA 142,40,62
buffer_load_dwordx4 a[112:115], v16, s[40:43], s63 offen offset:0 sc1
MA 146,44,62
WL 4
MA 86,32,66
RT 54,32,16384
MA 90,36,66
RT 56,32,17408
MA 150,40,66
buffer_load_dwordx4 a[116:119], v16, s[40:43], s63 offen offset:1024 sc1
MA 154,44,66
s_add_u32 s63, s63, 4096
WL 4
MA 94,32,70
RT 58,33,16384
MA 98,36,70
RT 60,33,17408
MA 158,40,70
buffer_load_dwordx4 a[120:123], v17, s[40:43], s86 offen offset:0 sc1
MA 162,44,70
WL 4
MA 102,32,74
RT 62,34,16384
MA 106,36,74
RT 64,34,17408
MA 166,40,74
buffer_load_dwordx4 a[124:127], v17, s[40:43], s86 offen offset:1024 sc1
MA 170,44,74
s_add_u32 s86, s86, 4096
WL 4
MA 110,32,54
RT 66,35,16384
MA 114,36,54
RT 68,35,17408
s_add_u32 m0, s52, 139264
MA 174,40,54
buffer_load_dwordx4 v18, s[44:47], s60 offen offset:0 lds
MA 178,44,54
WL 4
MA 118,32,58
RT 70,28,24576
MA 122,36,58
RT 72,28,25600
MA 182,40,58
buffer_load_dwordx4 v19, s[44:47], s60 offen offset:1024 lds
MA 186,44,58
s_add_u32 s60, s60, 229376
WL 4
MA 126,32,62
RT 74,29,24576
MA 130,36,62
RT 76,29,25600
MA 190,40,62
buffer_load_dwordx4 a[128:131], v16, s[40:43], s63 offen offset:0 sc1
MA 194,44,62
WL 4
MA 134,32,66
RT 54,30,24576
MA 138,36,66
RT 56,30,25600
MA 198,40,66
buffer_load_dwordx4 a[132:135], v16, s[40:43], s63 offen offset:1024 sc1
MA 202,44,66
s_add_u32 s63, s63, 4096
s_waitcnt vmcnt(28) lgkmcnt(4)
MA 78,48,70
RT 58,31,24576
MA 82,52,70
RT 60,31,25600
MA 142,56,70
buffer_load_dwordx4 a[136:139], v17, s[40:43], s86 offen offset:0 sc1
MA 146,60,70
WL 4
MA 86,48,74
RT 62,32,24576
MA 90,52,74
RT 64,32,25600
MA 150,56,74
buffer_load_dwordx4 a[140:143], v17, s[40:43], s86 offen offset:1024 sc1
MA 154,60,74
s_add_u32 s86, s86, 4096
WL 4
MA 94,48,54
RT 66,33,24576
MA 98,52,54
RT 68,33,25600
s_add_u32 m0, s52, 147456
MA 158,56,54
buffer_load_dwordx4 v18, s[44:47], s60 offen offset:0 lds
MA 162,60,54
WL 4
MA 102,48,58
RT 70,34,24576
MA 106,52,58
RT 72,34,25600
MA 166,56,58
buffer_load_dwordx4 v19, s[44:47], s60 offen offset:1024 lds
MA 170,60,58
s_add_u32 s60, s60, 229376
WL 4
MA 110,48,62
RT 74,35,24576
MA 114,52,62
RT 76,35,25600
MA 174,56,62
buffer_load_dwordx4 a[144:147], v16, s[40:43], s63 offen offset:0 sc1
MA 178,60,62
WL 4
MA 118,48,66
s_waitcnt vmcnt(27)
s_barrier
RT 54,28,32768
MA 122,52,66
RT 56,28,33792
MA 182,56,66
buffer_load_dwordx4 a[148:151], v16, s[40:43], s63 offen offset:1024 sc1
MA 186,60,66
s_add_u32 s63, s63, 4096
WL 4
MA 126,48,70
RT 58,29,32768
MA 130,52,70
RT 60,29,33792
MA 190,56,70
buffer_load_dwordx4 a[152:155], v17, s[40:43], s86 offen offset:0 sc1
MA 194,60,70
WL 4
MA 134,48,74
RT 62,30,32768
MA 138,52,74
RT 64,30,33792
MA 198,56,74
buffer_load_dwordx4 a[156:159], v17, s[40:43], s86 offen offset:1024 sc1
MA 202,60,74
s_add_u32 s86, s86, 4096
WL 4
MA 78,64,54
RT 66,31,32768
MA 82,68,54
RT 68,31,33792
s_add_u32 m0, s52, 155648
MA 142,72,54
buffer_load_dwordx4 v18, s[44:47], s60 offen offset:0 lds
MA 146,76,54
WL 4
MA 86,64,58
RT 70,32,32768
MA 90,68,58
RT 72,32,33792
MA 150,72,58
buffer_load_dwordx4 v19, s[44:47], s60 offen offset:1024 lds
MA 154,76,58
s_add_u32 s60, s60, 229376
WL 4
MA 94,64,62
RT 74,33,32768
MA 98,68,62
RT 76,33,33792
MA 158,72,62
buffer_load_dwordx4 a[160:163], v16, s[40:43], s63 offen offset:0 sc1
MA 162,76,62
WL 4
MA 102,64,66
RT 54,34,32768
MA 106,68,66
RT 56,34,33792
MA 166,72,66
buffer_load_dwordx4 a[164:167], v16, s[40:43], s63 offen offset:1024 sc1
MA 170,76,66
s_add_u32 s63, s63, 4096
WL 4
MA 110,64,70
RT 58,35,32768
MA 114,68,70
RT 60,35,33792
MA 174,72,70
buffer_load_dwordx4 a[168:171], v17, s[40:43], s86 offen offset:0 sc1
MA 178,76,70
WL 4
MA 118,64,74
RT 62,28,40960
MA 122,68,74
RT 64,28,41984
MA 182,72,74
buffer_load_dwordx4 a[172:175], v17, s[40:43], s86 offen offset:1024 sc1
MA 186,76,74
s_add_u32 s86, s86, 4096
WL 4
MA 126,64,54
RT 66,29,40960
MA 130,68,54
RT 68,29,41984
s_add_u32 m0, s52, 0
MA 190,72,54
buffer_load_dwordx4 v18, s[44:47], s60 offen offset:0 lds
MA 194,76,54
WL 4
MA 134,64,58
RT 70,30,40960
MA 138,68,58
RT 72,30,41984
MA 198,72,58
buffer_load_dwordx4 v19, s[44:47], s60 offen offset:1024 lds
MA 202,76,58
s_add_u32 s60, s60, 229376
s_waitcnt vmcnt(34) lgkmcnt(4)
MA 78,80,62
RT 74,31,40960
MA 82,84,62
RT 76,31,41984
MA 142,88,62
buffer_load_dwordx4 a[176:179], v16, s[40:43], s63 offen offset:0 sc1
MA 146,92,62
WL 4
MA 86,80,66
RT 54,32,40960
MA 90,84,66
RT 56,32,41984
MA 150,88,66
buffer_load_dwordx4 a[180:183], v16, s[40:43], s63 offen offset:1024 sc1
MA 154,92,66
s_add_u32 s63, s63, 4096
WL 4
MA 94,80,70
RT 58,33,40960
MA 98,84,70
RT 60,33,41984
MA 158,88,70
buffer_load_dwordx4 a[184:187], v17, s[40:43], s86 offen offset:0 sc1
MA 162,92,70
WL 4
MA 102,80,74
RT 62,34,40960
MA 106,84,74
RT 64,34,41984
MA 166,88,74
buffer_load_dwordx4 a[188:191], v17, s[40:43], s86 offen offset:1024 sc1
MA 170,92,74
s_add_u32 s86, s86, 4096
WL 4
MA 110,80,54
RT 66,35,40960
MA 114,84,54
RT 68,35,41984
MA 174,88,54
MA 178,92,54
WL 4
MA 118,80,58
s_waitcnt vmcnt(34)
s_barrier
RT 70,28,49152
MA 122,84,58
RT 72,28,50176
s_add_u32 m0, s52, 8192
MA 182,88,58
buffer_load_dwordx4 v18, s[44:47], s60 offen offset:0 lds
MA 186,92,58
WL 4
MA 126,80,62
RT 74,29,49152
MA 130,84,62
RT 76,29,50176
MA 190,88,62
buffer_load_dwordx4 v19, s[44:47], s60 offen offset:1024 lds
MA 194,92,62
s_add_u32 s60, s60, 229376
WL 4
MA 134,80,66
RT 54,30,49152
MA 138,84,66
RT 56,30,50176
MA 198,88,66
MA 202,92,66
s_waitcnt vmcnt(32) lgkmcnt(4)
MA 78,96,70
RT 58,31,49152
MA 82,100,70
RT 60,31,50176
MA 142,104,70
buffer_load_dwordx4 a[32:35], v16, s[40:43], s63 offen offset:0 sc1
MA 146,108,70
WL 4
MA 86,96,74
RT 62,32,49152
MA 90,100,74
RT 64,32,50176
MA 150,104,74
buffer_load_dwordx4 a[36:39], v16, s[40:43], s63 offen offset:1024 sc1
MA 154,108,74
s_add_u32 s63, s63, 4096
WL 4
MA 94,96,54
RT 66,33,49152
MA 98,100,54
RT 68,33,50176
MA 158,104,54
buffer_load_dwordx4 a[40:43], v17, s[40:43], s86 offen offset:0 sc1
MA 162,108,54
WL 4
MA 102,96,58
RT 70,34,49152
MA 106,100,58
RT 72,34,50176
MA 166,104,58
buffer_load_dwordx4 a[44:47], v17, s[40:43], s86 offen offset:1024 sc1
MA 170,108,58
s_add_u32 s86, s86, 4096
WL 4
MA 110,96,62
RT 74,35,49152
MA 114,100,62
RT 76,35,50176
MA 174,104,62
MA 178,108,62
WL 4
MA 118,96,66
RT 54,28,57344
MA 122,100,66
RT 56,28,58368
s_add_u32 m0, s52, 16384
MA 182,104,66
buffer_load_dwordx4 v18, s[44:47], s60 offen offset:0 lds
MA 186,108,66
WL 4
MA 126,96,70
RT 58,29,57344
MA 130,100,70
RT 60,29,58368
MA 190,104,70
buffer_load_dwordx4 v19, s[44:47], s60 offen offset:1024 lds
MA 194,108,70
s_add_u32 s60, s60, 229376
WL 4
MA 134,96,74
RT 62,30,57344
MA 138,100,74
RT 64,30,58368
MA 198,104,74
MA 202,108,74
s_waitcnt vmcnt(32) lgkmcnt(4)
MA 78,112,54
RT 66,31,57344
MA 82,116,54
RT 68,31,58368
MA 142,120,54
buffer_load_dwordx4 a[48:51], v16, s[40:43], s63 offen offset:0 sc1
MA 146,124,54
WL 4
MA 86,112,58
RT 70,32,57344
MA 90,116,58
RT 72,32,58368
MA 150,120,58
buffer_load_dwordx4 a[52:55], v16, s[40:43], s63 offen offset:1024 sc1
MA 154,124,58
s_add_u32 s63, s63, 4096
WL 4
MA 94,112,62
RT 74,33,57344
MA 98,116,62
RT 76,33,58368
MA 158,120,62
buffer_load_dwordx4 a[56:59], v17, s[40:43], s86 offen offset:0 sc1
MA 162,124,62
WL 4
MA 102,112,66
RT 54,34,57344
MA 106,116,66
RT 56,34,58368
MA 166,120,66
buffer_load_dwordx4 a[60:63], v17, s[40:43], s86 offen offset:1024 sc1
MA 170,124,66
s_add_u32 s86, s86, 4096
WL 4
MA 110,112,70
RT 58,35,57344
MA 114,116,70
RT 60,35,58368
MA 174,120,70
MA 178,124,70
WL 4
MA 118,112,74
s_waitcnt vmcnt(34)
)"
R"(s_barrier
RT 62,36,0
MA 122,116,74
RT 64,36,1024
s_add_u32 m0, s52, 24576
MA 182,120,74
buffer_load_dwordx4 v18, s[44:47], s60 offen offset:0 lds
MA 186,124,74
WL 4
MA 126,112,54
RT 66,37,0
MA 130,116,54
RT 68,37,1024
MA 190,120,54
buffer_load_dwordx4 v19, s[44:47], s60 offen offset:1024 lds
MA 194,124,54
s_add_u32 s60, s60, 229376
WL 4
MA 134,112,58
RT 70,38,0
MA 138,116,58
RT 72,38,1024
MA 198,120,58
MA 202,124,58
s_waitcnt vmcnt(32) lgkmcnt(4)
MA 78,128,62
RT 74,39,0
MA 82,132,62
RT 76,39,1024
MA 142,136,62
buffer_load_dwordx4 a[64:67], v16, s[40:43], s63 offen offset:0 sc1
MA 146,140,62
WL 4
MA 86,128,66
RT 54,40,0
MA 90,132,66
RT 56,40,1024
MA 150,136,66
buffer_load_dwordx4 a[68:71], v16, s[40:43], s63 offen offset:1024 sc1
MA 154,140,66
s_add_u32 s63, s63, 4096
WL 4
MA 94,128,70
RT 58,41,0
MA 98,132,70
RT 60,41,1024
MA 158,136,70
buffer_load_dwordx4 a[72:75], v17, s[40:43], s86 offen offset:0 sc1
MA 162,140,70
WL 4
MA 102,128,74
RT 62,42,0
MA 106,132,74
RT 64,42,1024
MA 166,136,74
buffer_load_dwordx4 a[76:79], v17, s[40:43], s86 offen offset:1024 sc1
MA 170,140,74
s_add_u32 s86, s86, 4096
WL 4
MA 110,128,54
RT 66,43,0
MA 114,132,54
RT 68,43,1024
MA 174,136,54
MA 178,140,54
WL 4
MA 118,128,58
RT 70,36,8192
MA 122,132,58
RT 72,36,9216
s_add_u32 m0, s52, 32768
MA 182,136,58
buffer_load_dwordx4 v18, s[44:47], s60 offen offset:0 lds
MA 186,140,58
WL 4
MA 126,128,62
RT 74,37,8192
MA 130,132,62
RT 76,37,9216
MA 190,136,62
buffer_load_dwordx4 v19, s[44:47], s60 offen offset:1024 lds
MA 194,140,62
s_add_u32 s60, s60, 229376
WL 4
MA 134,128,66
RT 54,38,8192
MA 138,132,66
RT 56,38,9216
MA 198,136,66
MA 202,140,66
s_waitcnt vmcnt(32) lgkmcnt(4)
MA 78,144,70
RT 58,39,8192
MA 82,148,70
RT 60,39,9216
MA 142,152,70
buffer_load_dword v50, v49, s[44:47], s58 offen
MA 146,156,70
s_add_u32 s58, s58, 3670016
s_cmp_ge_u32 s58, s61
s_cselect_b32 s58, 0x40000000, s58
WL 4
MA 86,144,74
RT 62,40,8192
MA 90,148,74
RT 64,40,9216
MA 150,152,74
MA 154,156,74
WL 4
MA 94,144,54
RT 66,41,8192
MA 98,148,54
RT 68,41,9216
MA 158,152,54
MA 162,156,54
WL 4
MA 102,144,58
RT 70,42,8192
MA 106,148,58
RT 72,42,9216
MA 166,152,58
MA 170,156,58
WL 4
MA 110,144,62
RT 74,43,8192
MA 114,148,62
RT 76,43,9216
MA 174,152,62
MA 178,156,62
WL 4
MA 118,144,66
s_waitcnt vmcnt(31)
s_barrier
RT 54,36,16384
MA 122,148,66
RT 56,36,17408
MA 182,152,66
MA 186,156,66
WL 4
MA 126,144,70
RT 58,37,16384
MA 130,148,70
RT 60,37,17408
MA 190,152,70
MA 194,156,70
WL 4
MA 134,144,74
RT 62,38,16384
MA 138,148,74
RT 64,38,17408
MA 198,152,74
MA 202,156,74
s_waitcnt vmcnt(27) lgkmcnt(4)
MA 78,160,54
RT 66,39,16384
MA 82,164,54
RT 68,39,17408
MA 142,168,54
MA 146,172,54
WL 4
MA 86,160,58
RT 70,40,16384
MA 90,164,58
RT 72,40,17408
MA 150,168,58
MA 154,172,58
WL 4
MA 94,160,62
RT 74,41,16384
MA 98,164,62
RT 76,41,17408
MA 158,168,62
MA 162,172,62
WL 4
MA 102,160,66
RT 54,42,16384
MA 106,164,66
RT 56,42,17408
MA 166,168,66
MA 170,172,66
WL 4
MA 110,160,70
RT 58,43,16384
MA 114,164,70
RT 60,43,17408
MA 174,168,70
MA 178,172,70
WL 4
MA 118,160,74
RT 62,36,24576
MA 122,164,74
RT 64,36,25600
MA 182,168,74
MA 186,172,74
WL 4
MA 126,160,54
RT 66,37,24576
MA 130,164,54
RT 68,37,25600
MA 190,168,54
MA 194,172,54
WL 4
MA 134,160,58
RT 70,38,24576
MA 138,164,58
RT 72,38,25600
MA 198,168,58
MA 202,172,58
s_waitcnt vmcnt(21) lgkmcnt(4)
MA 78,176,62
RT 74,39,24576
MA 82,180,62
RT 76,39,25600
MA 142,184,62
buffer_load_dwordx4 a[80:83], v16, s[40:43], s63 offen offset:0 sc1
MA 146,188,62
WL 4
MA 86,176,66
RT 54,40,24576
MA 90,180,66
RT 56,40,25600
MA 150,184,66
buffer_load_dwordx4 a[84:87], v16, s[40:43], s63 offen offset:1024 sc1
MA 154,188,66
s_add_u32 s63, s63, 4096
WL 4
MA 94,176,70
RT 58,41,24576
MA 98,180,70
RT 60,41,25600
MA 158,184,70
buffer_load_dwordx4 a[88:91], v17, s[40:43], s86 offen offset:0 sc1
MA 162,188,70
WL 4
MA 102,176,74
RT 62,42,24576
MA 106,180,74
RT 64,42,25600
MA 166,184,74
buffer_load_dwordx4 a[92:95], v17, s[40:43], s86 offen offset:1024 sc1
MA 170,188,74
s_add_u32 s86, s86, 4096
WL 4
MA 110,176,54
RT 66,43,24576
MA 114,180,54
RT 68,43,25600
MA 174,184,54
buffer_load_dwordx4 a[96:99], v16, s[40:43], s63 offen offset:0 sc1
MA 178,188,54
WL 4
MA 118,176,58
s_waitcnt vmcnt(24)
s_barrier
RT 70,20,0
MA 122,180,58
RT 72,20,1024
MA 182,184,58
buffer_load_dwordx4 a[100:103], v16, s[40:43], s63 offen offset:1024 sc1
MA 186,188,58
s_add_u32 s63, s63, 4096
WL 4
MA 126,176,62
RT 74,21,0
MA 130,180,62
RT 76,21,1024
MA 190,184,62
buffer_load_dwordx4 a[104:107], v17, s[40:43], s86 offen offset:0 sc1
MA 194,188,62
WL 4
MA 134,176,66
RT 54,22,0
MA 138,180,66
RT 56,22,1024
MA 198,184,66
buffer_load_dwordx4 a[108:111], v17, s[40:43], s86 offen offset:1024 sc1
MA 202,188,66
s_add_u32 s86, s86, 4096
s_waitcnt vmcnt(23) lgkmcnt(4)
MA 78,32,70
RT 58,23,0
MA 82,36,70
RT 60,23,1024
s_add_u32 m0, s52, 40960
MA 142,40,70
buffer_load_dwordx4 v18, s[44:47], s60 offen offset:0 lds
MA 146,44,70
WL 4
MA 86,32,74
RT 62,24,0
MA 90,36,74
RT 64,24,1024
MA 150,40,74
buffer_load_dwordx4 v19, s[44:47], s60 offen offset:1024 lds
MA 154,44,74
s_add_u32 s60, s60, 229376
WL 4
MA 94,32,54
RT 66,25,0
MA 98,36,54
RT 68,25,1024
MA 158,40,54
buffer_load_dwordx4 a[112:115], v16, s[40:43], s63 offen offset:0 sc1
MA 162,44,54
WL 4
MA 102,32,58
RT 70,26,0
MA 106,36,58
RT 72,26,1024
MA 166,40,58
buffer_load_dwordx4 a[116:119], v16, s[40:43], s63 offen offset:1024 sc1
MA 170,44,58
s_add_u32 s63, s63, 4096
WL 4
MA 110,32,62
RT 74,27,0
MA 114,36,62
RT 76,27,1024
MA 174,40,62
buffer_load_dwordx4 a[120:123], v17, s[40:43], s86 offen offset:0 sc1
MA 178,44,62
WL 4
MA 118,32,66
RT 54,20,8192
MA 122,36,66
RT 56,20,9216
MA 182,40,66
buffer_load_dwordx4 a[124:127], v17, s[40:43], s86 offen offset:1024 sc1
MA 186,44,66
s_add_u32 s86, s86, 4096
WL 4
MA 126,32,70
RT 58,21,8192
MA 130,36,70
RT 60,21,9216
s_add_u32 m0, s52, 49152
MA 190,40,70
buffer_load_dwordx4 v18, s[44:47], s60 offen offset:0 lds
MA 194,44,70
WL 4
MA 134,32,74
RT 62,22,8192
MA 138,36,74
RT 64,22,9216
MA 198,40,74
buffer_load_dwordx4 v19, s[44:47], s60 offen offset:1024 lds
MA 202,44,74
s_add_u32 s60, s60, 229376
s_waitcnt vmcnt(25) lgkmcnt(4)
MA 78,48,54
RT 66,23,8192
MA 82,52,54
RT 68,23,9216
s_add_u32 m0, s52, 57344
MA 142,56,54
buffer_load_dwordx4 v18, s[44:47], s60 offen offset:0 lds
MA 146,60,54
WL 4
MA 86,48,58
RT 70,24,8192
MA 90,52,58
RT 72,24,9216
MA 150,56,58
buffer_load_dwordx4 v19, s[44:47], s60 offen offset:1024 lds
MA 154,60,58
s_add_u32 s60, s60, 229376
WL 4
MA 94,48,62
RT 74,25,8192
MA 98,52,62
RT 76,25,9216
MA 158,56,62
buffer_load_dwordx4 a[128:131], v16, s[40:43], s63 offen offset:0 sc1
MA 162,60,62
WL 4
MA 102,48,66
RT 54,26,8192
MA 106,52,66
RT 56,26,9216
MA 166,56,66
buffer_load_dwordx4 a[132:135], v16, s[40:43], s63 offen offset:1024 sc1
MA 170,60,66
s_add_u32 s63, s63, 4096
WL 4
MA 110,48,70
RT 58,27,8192
MA 114,52,70
RT 60,27,9216
MA 174,56,70
buffer_load_dwordx4 a[136:139], v17, s[40:43], s86 offen offset:0 sc1
MA 178,60,70
WL 4
MA 118,48,74
s_waitcnt vmcnt(28)
s_barrier
RT 62,20,16384
MA 122,52,74
RT 64,20,17408
MA 182,56,74
buffer_load_dwordx4 a[140:143], v17, s[40:43], s86 offen offset:1024 sc1
)"
R"(MA 186,60,74
s_add_u32 s86, s86, 4096
WL 4
MA 126,48,54
RT 66,21,16384
MA 130,52,54
RT 68,21,17408
s_add_u32 m0, s52, 65536
MA 190,56,54
buffer_load_dwordx4 v18, s[44:47], s60 offen offset:0 lds
MA 194,60,54
WL 4
MA 134,48,58
RT 70,22,16384
MA 138,52,58
RT 72,22,17408
MA 198,56,58
buffer_load_dwordx4 v19, s[44:47], s60 offen offset:1024 lds
MA 202,60,58
s_add_u32 s60, s60, 229376
s_waitcnt vmcnt(27) lgkmcnt(4)
MA 78,64,62
RT 74,23,16384
MA 82,68,62
RT 76,23,17408
MA 142,72,62
buffer_load_dwordx4 a[144:147], v16, s[40:43], s63 offen offset:0 sc1
MA 146,76,62
WL 4
MA 86,64,66
RT 54,24,16384
MA 90,68,66
RT 56,24,17408
MA 150,72,66
buffer_load_dwordx4 a[148:151], v16, s[40:43], s63 offen offset:1024 sc1
MA 154,76,66
s_add_u32 s63, s63, 4096
WL 4
MA 94,64,70
RT 58,25,16384
MA 98,68,70
RT 60,25,17408
MA 158,72,70
buffer_load_dwordx4 a[152:155], v17, s[40:43], s86 offen offset:0 sc1
MA 162,76,70
WL 4
MA 102,64,74
RT 62,26,16384
MA 106,68,74
RT 64,26,17408
MA 166,72,74
buffer_load_dwordx4 a[156:159], v17, s[40:43], s86 offen offset:1024 sc1
MA 170,76,74
s_add_u32 s86, s86, 4096
WL 4
MA 110,64,54
RT 66,27,16384
MA 114,68,54
RT 68,27,17408
s_add_u32 m0, s52, 73728
MA 174,72,54
buffer_load_dwordx4 v18, s[44:47], s60 offen offset:0 lds
MA 178,76,54
WL 4
MA 118,64,58
RT 70,20,24576
MA 122,68,58
RT 72,20,25600
MA 182,72,58
buffer_load_dwordx4 v19, s[44:47], s60 offen offset:1024 lds
MA 186,76,58
s_add_u32 s60, s60, 229376
WL 4
MA 126,64,62
RT 74,21,24576
MA 130,68,62
RT 76,21,25600
MA 190,72,62
buffer_load_dwordx4 a[160:163], v16, s[40:43], s63 offen offset:0 sc1
MA 194,76,62
WL 4
MA 134,64,66
RT 54,22,24576
MA 138,68,66
RT 56,22,25600
MA 198,72,66
buffer_load_dwordx4 a[164:167], v16, s[40:43], s63 offen offset:1024 sc1
MA 202,76,66
s_add_u32 s63, s63, 4096
s_waitcnt vmcnt(28) lgkmcnt(4)
MA 78,80,70
RT 58,23,24576
MA 82,84,70
RT 60,23,25600
MA 142,88,70
buffer_load_dwordx4 a[168:171], v17, s[40:43], s86 offen offset:0 sc1
MA 146,92,70
WL 4
MA 86,80,74
RT 62,24,24576
MA 90,84,74
RT 64,24,25600
MA 150,88,74
buffer_load_dwordx4 a[172:175], v17, s[40:43], s86 offen offset:1024 sc1
MA 154,92,74
s_add_u32 s86, s86, 4096
WL 4
MA 94,80,54
RT 66,25,24576
MA 98,84,54
RT 68,25,25600
s_add_u32 m0, s52, 81920
MA 158,88,54
buffer_load_dwordx4 v18, s[44:47], s60 offen offset:0 lds
MA 162,92,54
WL 4
MA 102,80,58
RT 70,26,24576
MA 106,84,58
RT 72,26,25600
MA 166,88,58
buffer_load_dwordx4 v19, s[44:47], s60 offen offset:1024 lds
MA 170,92,58
s_add_u32 s60, s60, 229376
WL 4
MA 110,80,62
RT 74,27,24576
MA 114,84,62
RT 76,27,25600
MA 174,88,62
buffer_load_dwordx4 a[176:179], v16, s[40:43], s63 offen offset:0 sc1
MA 178,92,62
WL 4
MA 118,80,66
s_waitcnt vmcnt(27)
s_barrier
RT 54,20,32768
MA 122,84,66
RT 56,20,33792
MA 182,88,66
buffer_load_dwordx4 a[180:183], v16, s[40:43], s63 offen offset:1024 sc1
MA 186,92,66
s_add_u32 s63, s63, 4096
WL 4
MA 126,80,70
RT 58,21,32768
MA 130,84,70
RT 60,21,33792
MA 190,88,70
buffer_load_dwordx4 a[184:187], v17, s[40:43], s86 offen offset:0 sc1
MA 194,92,70
WL 4
MA 134,80,74
RT 62,22,32768
MA 138,84,74
RT 64,22,33792
MA 198,88,74
buffer_load_dwordx4 a[188:191], v17, s[40:43], s86 offen offset:1024 sc1
MA 202,92,74
s_add_u32 s86, s86, 4096
WL 4
MA 78,96,54
RT 66,23,32768
MA 82,100,54
RT 68,23,33792
s_add_u32 m0, s52, 90112
MA 142,104,54
buffer_load_dwordx4 v18, s[44:47], s60 offen offset:0 lds
MA 146,108,54
WL 4
MA 86,96,58
RT 70,24,32768
MA 90,100,58
RT 72,24,33792
MA 150,104,58
buffer_load_dwordx4 v19, s[44:47], s60 offen offset:1024 lds
MA 154,108,58
s_add_u32 s60, s60, 229376
WL 4
MA 94,96,62
RT 74,25,32768
MA 98,100,62
RT 76,25,33792
MA 158,104,62
buffer_load_dwordx4 a[32:35], v16, s[40:43], s63 offen offset:0 sc1
MA 162,108,62
WL 4
MA 102,96,66
RT 54,26,32768
MA 106,100,66
RT 56,26,33792
MA 166,104,66
buffer_load_dwordx4 a[36:39], v16, s[40:43], s63 offen offset:1024 sc1
MA 170,108,66
s_add_u32 s63, s63, 4096
WL 4
MA 110,96,70
RT 58,27,32768
MA 114,100,70
RT 60,27,33792
MA 174,104,70
buffer_load_dwordx4 a[40:43], v17, s[40:43], s86 offen offset:0 sc1
MA 178,108,70
WL 4
MA 118,96,74
RT 62,20,40960
MA 122,100,74
RT 64,20,41984
MA 182,104,74
buffer_load_dwordx4 a[44:47], v17, s[40:43], s86 offen offset:1024 sc1
MA 186,108,74
s_add_u32 s86, s86, 4096
WL 4
MA 126,96,54
RT 66,21,40960
MA 130,100,54
RT 68,21,41984
s_add_u32 m0, s52, 98304
MA 190,104,54
buffer_load_dwordx4 v18, s[44:47], s60 offen offset:0 lds
MA 194,108,54
WL 4
MA 134,96,58
RT 70,22,40960
MA 138,100,58
RT 72,22,41984
MA 198,104,58
buffer_load_dwordx4 v19, s[44:47], s60 offen offset:1024 lds
MA 202,108,58
s_add_u32 s60, s60, 229376
s_waitcnt vmcnt(34) lgkmcnt(4)
MA 78,112,62
RT 74,23,40960
MA 82,116,62
RT 76,23,41984
MA 142,120,62
buffer_load_dwordx4 a[48:51], v16, s[40:43], s63 offen offset:0 sc1
MA 146,124,62
WL 4
MA 86,112,66
RT 54,24,40960
MA 90,116,66
RT 56,24,41984
MA 150,120,66
buffer_load_dwordx4 a[52:55], v16, s[40:43], s63 offen offset:1024 sc1
MA 154,124,66
s_add_u32 s63, s63, 4096
WL 4
MA 94,112,70
RT 58,25,40960
MA 98,116,70
RT 60,25,41984
MA 158,120,70
buffer_load_dwordx4 a[56:59], v17, s[40:43], s86 offen offset:0 sc1
MA 162,124,70
WL 4
MA 102,112,74
RT 62,26,40960
MA 106,116,74
RT 64,26,41984
MA 166,120,74
buffer_load_dwordx4 a[60:63], v17, s[40:43], s86 offen offset:1024 sc1
MA 170,124,74
s_add_u32 s86, s86, 4096
WL 4
MA 110,112,54
RT 66,27,40960
MA 114,116,54
RT 68,27,41984
MA 174,120,54
MA 178,124,54
WL 4
MA 118,112,58
s_waitcnt vmcnt(34)
s_barrier
RT 70,20,49152
MA 122,116,58
RT 72,20,50176
s_add_u32 m0, s52, 106496
MA 182,120,58
buffer_load_dwordx4 v18, s[44:47], s60 offen offset:0 lds
MA 186,124,58
WL 4
MA 126,112,62
RT 74,21,49152
MA 130,116,62
RT 76,21,50176
MA 190,120,62
buffer_load_dwordx4 v19, s[44:47], s60 offen offset:1024 lds
MA 194,124,62
s_add_u32 s60, s60, 229376
WL 4
MA 134,112,66
RT 54,22,49152
MA 138,116,66
RT 56,22,50176
MA 198,120,66
MA 202,124,66
s_waitcnt vmcnt(32) lgkmcnt(4)
MA 78,128,70
RT 58,23,49152
MA 82,132,70
RT 60,23,50176
MA 142,136,70
buffer_load_dword v50, v49, s[44:47], s58 offen
MA 146,140,70
s_add_u32 s58, s58, 3670016
s_cmp_ge_u32 s58, s61
s_cselect_b32 s58, 0x40000000, s58
WL 4
MA 86,128,74
RT 62,24,49152
MA 90,132,74
RT 64,24,50176
MA 150,136,74
MA 154,140,74
WL 4
MA 94,128,54
RT 66,25,49152
MA 98,132,54
RT 68,25,50176
MA 158,136,54
MA 162,140,54
WL 4
MA 102,128,58
RT 70,26,49152
MA 106,132,58
RT 72,26,50176
MA 166,136,58
MA 170,140,58
WL 4
MA 110,128,62
RT 74,27,49152
MA 114,132,62
RT 76,27,50176
MA 174,136,62
MA 178,140,62
WL 4
MA 118,128,66
RT 54,20,57344
MA 122,132,66
RT 56,20,58368
MA 182,136,66
MA 186,140,66
WL 4
MA 126,128,70
RT 58,21,57344
MA 130,132,70
RT 60,21,58368
MA 190,136,70
MA 194,140,70
WL 4
MA 134,128,74
RT 62,22,57344
MA 138,132,74
RT 64,22,58368
MA 198,136,74
MA 202,140,74
s_waitcnt vmcnt(27) lgkmcnt(4)
MA 78,144,54
RT 66,23,57344
MA 82,148,54
RT 68,23,58368
MA 142,152,54
MA 146,156,54
WL 4
MA 86,144,58
RT 70,24,57344
MA 90,148,58
RT 72,24,58368
MA 150,152,58
MA 154,156,58
WL 4
MA 94,144,62
RT 74,25,57344
MA 98,148,62
RT 76,25,58368
MA 158,152,62
MA 162,156,62
WL 4
MA 102,144,66
RT 54,26,57344
MA 106,148,66
RT 56,26,58368
MA 166,152,66
MA 170,156,66
WL 4
MA 110,144,70
RT 58,27,57344
MA 114,148,70
RT 60,27,58368
MA 174,152,70
MA 178,156,70
WL 4
MA 118,144,74
s_waitcnt vmcnt(25)
s_barrier
RT 62,28,0
MA 122,148,74
RT 64,28,1024
)"
R"(MA 182,152,74
MA 186,156,74
WL 4
MA 126,144,54
RT 66,29,0
MA 130,148,54
RT 68,29,1024
MA 190,152,54
MA 194,156,54
WL 4
MA 134,144,58
RT 70,30,0
MA 138,148,58
RT 72,30,1024
MA 198,152,58
MA 202,156,58
s_waitcnt vmcnt(21) lgkmcnt(4)
MA 78,160,62
RT 74,31,0
MA 82,164,62
RT 76,31,1024
MA 142,168,62
buffer_load_dwordx4 a[64:67], v16, s[40:43], s63 offen offset:0 sc1
MA 146,172,62
WL 4
MA 86,160,66
RT 54,32,0
MA 90,164,66
RT 56,32,1024
MA 150,168,66
buffer_load_dwordx4 a[68:71], v16, s[40:43], s63 offen offset:1024 sc1
MA 154,172,66
s_add_u32 s63, s63, 4096
WL 4
MA 94,160,70
RT 58,33,0
MA 98,164,70
RT 60,33,1024
MA 158,168,70
buffer_load_dwordx4 a[72:75], v17, s[40:43], s86 offen offset:0 sc1
MA 162,172,70
WL 4
MA 102,160,74
RT 62,34,0
MA 106,164,74
RT 64,34,1024
MA 166,168,74
buffer_load_dwordx4 a[76:79], v17, s[40:43], s86 offen offset:1024 sc1
MA 170,172,74
s_add_u32 s86, s86, 4096
WL 4
MA 110,160,54
RT 66,35,0
MA 114,164,54
RT 68,35,1024
MA 174,168,54
buffer_load_dwordx4 a[80:83], v16, s[40:43], s63 offen offset:0 sc1
MA 178,172,54
WL 4
MA 118,160,58
RT 70,28,8192
MA 122,164,58
RT 72,28,9216
MA 182,168,58
buffer_load_dwordx4 a[84:87], v16, s[40:43], s63 offen offset:1024 sc1
MA 186,172,58
s_add_u32 s63, s63, 4096
WL 4
MA 126,160,62
RT 74,29,8192
MA 130,164,62
RT 76,29,9216
MA 190,168,62
buffer_load_dwordx4 a[88:91], v17, s[40:43], s86 offen offset:0 sc1
MA 194,172,62
WL 4
MA 134,160,66
RT 54,30,8192
MA 138,164,66
RT 56,30,9216
MA 198,168,66
buffer_load_dwordx4 a[92:95], v17, s[40:43], s86 offen offset:1024 sc1
MA 202,172,66
s_add_u32 s86, s86, 4096
s_waitcnt vmcnt(23) lgkmcnt(4)
MA 78,176,70
RT 58,31,8192
MA 82,180,70
RT 60,31,9216
s_add_u32 m0, s52, 114688
MA 142,184,70
buffer_load_dwordx4 v18, s[44:47], s60 offen offset:0 lds
MA 146,188,70
WL 4
MA 86,176,74
RT 62,32,8192
MA 90,180,74
RT 64,32,9216
MA 150,184,74
buffer_load_dwordx4 v19, s[44:47], s60 offen offset:1024 lds
MA 154,188,74
s_add_u32 s60, s60, 229376
WL 4
MA 94,176,54
RT 66,33,8192
MA 98,180,54
RT 68,33,9216
MA 158,184,54
buffer_load_dwordx4 a[96:99], v16, s[40:43], s63 offen offset:0 sc1
MA 162,188,54
WL 4
MA 102,176,58
RT 70,34,8192
MA 106,180,58
RT 72,34,9216
MA 166,184,58
buffer_load_dwordx4 a[100:103], v16, s[40:43], s63 offen offset:1024 sc1
MA 170,188,58
s_add_u32 s63, s63, 4096
WL 4
MA 110,176,62
RT 74,35,8192
MA 114,180,62
RT 76,35,9216
MA 174,184,62
buffer_load_dwordx4 a[104:107], v17, s[40:43], s86 offen offset:0 sc1
MA 178,188,62
WL 4
MA 118,176,66
s_waitcnt vmcnt(26)
s_barrier
RT 54,28,16384
MA 122,180,66
RT 56,28,17408
MA 182,184,66
buffer_load_dwordx4 a[108:111], v17, s[40:43], s86 offen offset:1024 sc1
MA 186,188,66
s_add_u32 s86, s86, 4096
WL 4
MA 126,176,70
RT 58,29,16384
MA 130,180,70
RT 60,29,17408
s_add_u32 m0, s52, 122880
MA 190,184,70
buffer_load_dwordx4 v18, s[44:47], s60 offen offset:0 lds
MA 194,188,70
WL 4
MA 134,176,74
RT 62,30,16384
MA 138,180,74
RT 64,30,17408
MA 198,184,74
buffer_load_dwordx4 v19, s[44:47], s60 offen offset:1024 lds
MA 202,188,74
s_add_u32 s60, s60, 229376
s_waitcnt vmcnt(25) lgkmcnt(4)
MA 78,32,54
RT 66,31,16384
MA 82,36,54
RT 68,31,17408
s_add_u32 m0, s52, 131072
MA 142,40,54
buffer_load_dwordx4 v18, s[44:47], s60 offen offset:0 lds
MA 146,44,54
WL 4
MA 86,32,58
RT 70,32,16384
MA 90,36,58
RT 72,32,17408
MA 150,40,58
buffer_load_dwordx4 v19, s[44:47], s60 offen offset:1024 lds
MA 154,44,58
s_add_u32 s60, s60, 229376
WL 4
MA 94,32,62
RT 74,33,16384
MA 98,36,62
RT 76,33,17408
MA 158,40,62
buffer_load_dwordx4 a[112:115], v16, s[40:43], s63 offen offset:0 sc1
MA 162,44,62
WL 4
MA 102,32,66
RT 54,34,16384
MA 106,36,66
RT 56,34,17408
MA 166,40,66
buffer_load_dwordx4 a[116:119], v16, s[40:43], s63 offen offset:1024 sc1
MA 170,44,66
s_add_u32 s63, s63, 4096
WL 4
MA 110,32,70
RT 58,35,16384
MA 114,36,70
RT 60,35,17408
MA 174,40,70
buffer_load_dwordx4 a[120:123], v17, s[40:43], s86 offen offset:0 sc1
MA 178,44,70
WL 4
MA 118,32,74
RT 62,28,24576
MA 122,36,74
RT 64,28,25600
MA 182,40,74
buffer_load_dwordx4 a[124:127], v17, s[40:43], s86 offen offset:1024 sc1
MA 186,44,74
s_add_u32 s86, s86, 4096
WL 4
MA 126,32,54
RT 66,29,24576
MA 130,36,54
RT 68,29,25600
s_add_u32 m0, s52, 139264
MA 190,40,54
buffer_load_dwordx4 v18, s[44:47], s60 offen offset:0 lds
MA 194,44,54
WL 4
MA 134,32,58
RT 70,30,24576
MA 138,36,58
RT 72,30,25600
MA 198,40,58
buffer_load_dwordx4 v19, s[44:47], s60 offen offset:1024 lds
MA 202,44,58
s_add_u32 s60, s60, 229376
s_waitcnt vmcnt(27) lgkmcnt(4)
MA 78,48,62
RT 74,31,24576
MA 82,52,62
RT 76,31,25600
s_add_u32 m0, s52, 147456
MA 142,56,62
buffer_load_dwordx4 v18, s[44:47], s60 offen offset:0 lds
MA 146,60,62
WL 4
MA 86,48,66
RT 54,32,24576
MA 90,52,66
RT 56,32,25600
MA 150,56,66
buffer_load_dwordx4 v19, s[44:47], s60 offen offset:1024 lds
MA 154,60,66
s_add_u32 s60, s60, 229376
WL 4
MA 94,48,70
RT 58,33,24576
MA 98,52,70
RT 60,33,25600
MA 158,56,70
buffer_load_dwordx4 a[128:131], v17, s[40:43], s86 offen offset:0 sc1
MA 162,60,70
WL 4
MA 102,48,74
RT 62,34,24576
MA 106,52,74
RT 64,34,25600
MA 166,56,74
buffer_load_dwordx4 a[132:135], v17, s[40:43], s86 offen offset:1024 sc1
MA 170,60,74
s_add_u32 s86, s86, 4096
WL 4
MA 110,48,54
RT 66,35,24576
MA 114,52,54
RT 68,35,25600
s_add_u32 m0, s52, 155648
MA 174,56,54
buffer_load_dwordx4 v18, s[44:47], s60 offen offset:0 lds
MA 178,60,54
WL 4
MA 118,48,58
s_waitcnt vmcnt(30)
s_barrier
RT 70,28,32768
MA 122,52,58
RT 72,28,33792
MA 182,56,58
buffer_load_dwordx4 v19, s[44:47], s60 offen offset:1024 lds
MA 186,60,58
s_add_u32 s60, s60, 229376
WL 4
MA 126,48,62
RT 74,29,32768
MA 130,52,62
RT 76,29,33792
MA 190,56,62
buffer_load_dwordx4 a[136:139], v17, s[40:43], s86 offen offset:0 sc1
MA 194,60,62
WL 4
MA 134,48,66
RT 54,30,32768
MA 138,52,66
RT 56,30,33792
MA 198,56,66
buffer_load_dwordx4 a[140:143], v17, s[40:43], s86 offen offset:1024 sc1
MA 202,60,66
s_add_u32 s86, s86, 4096
s_waitcnt vmcnt(28) lgkmcnt(4)
MA 78,64,70
RT 58,31,32768
MA 82,68,70
RT 60,31,33792
s_add_u32 m0, s52, 0
MA 142,72,70
buffer_load_dwordx4 v18, s[44:47], s60 offen offset:0 lds
MA 146,76,70
WL 4
MA 86,64,74
RT 62,32,32768
MA 90,68,74
RT 64,32,33792
MA 150,72,74
buffer_load_dwordx4 v19, s[44:47], s60 offen offset:1024 lds
MA 154,76,74
s_add_u32 s60, s60, 229376
WL 4
MA 94,64,54
RT 66,33,32768
MA 98,68,54
RT 68,33,33792
MA 158,72,54
buffer_load_dwordx4 a[144:147], v17, s[40:43], s86 offen offset:0 sc1
MA 162,76,54
WL 4
MA 102,64,58
RT 70,34,32768
MA 106,68,58
RT 72,34,33792
MA 166,72,58
buffer_load_dwordx4 a[148:151], v17, s[40:43], s86 offen offset:1024 sc1
MA 170,76,58
s_add_u32 s86, s86, 4096
WL 4
MA 110,64,62
RT 74,35,32768
MA 114,68,62
RT 76,35,33792
s_add_u32 m0, s52, 8192
MA 174,72,62
buffer_load_dwordx4 v18, s[44:47], s60 offen offset:0 lds
MA 178,76,62
WL 4
MA 118,64,66
RT 54,28,40960
MA 122,68,66
RT 56,28,41984
MA 182,72,66
buffer_load_dwordx4 v19, s[44:47], s60 offen offset:1024 lds
MA 186,76,66
s_add_u32 s60, s60, 229376
WL 4
MA 126,64,70
RT 58,29,40960
MA 130,68,70
RT 60,29,41984
MA 190,72,70
buffer_load_dwordx4 a[152:155], v17, s[40:43], s86 offen offset:0 sc1
MA 194,76,70
WL 4
MA 134,64,74
RT 62,30,40960
MA 138,68,74
RT 64,30,41984
MA 198,72,74
buffer_load_dwordx4 a[156:159], v17, s[40:43], s86 offen offset:1024 sc1
MA 202,76,74
s_add_u32 s86, s86, 4096
s_waitcnt vmcnt(32) lgkmcnt(4)
MA 78,80,54
RT 66,31,40960
MA 82,84,54
RT 68,31,41984
s_add_u32 m0, s52, 16384
MA 142,88,54
buffer_load_dwordx4 v18, s[44:47], s60 offen offset:0 lds
MA 146,92,54
WL 4
MA 86,80,58
RT 70,32,40960
MA 90,84,58
RT 72,32,41984
MA 150,88,58
buffer_load_dwordx4 v19, s[44:47], s60 offen offset:1024 lds
MA 154,92,58
s_add_u32 s60, s60, 229376
WL 4
MA 94,80,62
RT 74,33,40960
MA 98,84,62
RT 76,33,41984
MA 158,88,62
buffer_load_dwordx4 a[160:163], v17, s[40:43], s86 offen offset:0 sc1
MA 162,92,62
WL 4
MA 102,80,66
RT 54,34,40960
MA 106,84,66
RT 56,34,41984
MA 166,88,66
)"
R"(buffer_load_dwordx4 a[164:167], v17, s[40:43], s86 offen offset:1024 sc1
MA 170,92,66
s_add_u32 s86, s86, 4096
WL 4
MA 110,80,70
RT 58,35,40960
MA 114,84,70
RT 60,35,41984
s_add_u32 m0, s52, 24576
MA 174,88,70
buffer_load_dwordx4 v18, s[44:47], s60 offen offset:0 lds
MA 178,92,70
WL 4
MA 118,80,74
s_waitcnt vmcnt(29)
s_barrier
RT 62,28,49152
MA 122,84,74
RT 64,28,50176
MA 182,88,74
buffer_load_dwordx4 v19, s[44:47], s60 offen offset:1024 lds
MA 186,92,74
s_add_u32 s60, s60, 229376
WL 4
MA 126,80,54
RT 66,29,49152
MA 130,84,54
RT 68,29,50176
MA 190,88,54
buffer_load_dwordx4 a[168:171], v17, s[40:43], s86 offen offset:0 sc1
MA 194,92,54
WL 4
MA 134,80,58
RT 70,30,49152
MA 138,84,58
RT 72,30,50176
MA 198,88,58
buffer_load_dwordx4 a[172:175], v17, s[40:43], s86 offen offset:1024 sc1
MA 202,92,58
s_add_u32 s86, s86, 4096
WL 4
MA 78,96,62
RT 74,31,49152
MA 82,100,62
RT 76,31,50176
s_add_u32 m0, s52, 32768
MA 142,104,62
buffer_load_dwordx4 v18, s[44:47], s60 offen offset:0 lds
MA 146,108,62
WL 4
MA 86,96,66
RT 54,32,49152
MA 90,100,66
RT 56,32,50176
MA 150,104,66
buffer_load_dwordx4 v19, s[44:47], s60 offen offset:1024 lds
MA 154,108,66
s_add_u32 s60, s60, 229376
WL 4
MA 94,96,70
RT 58,33,49152
MA 98,100,70
RT 60,33,50176
MA 158,104,70
buffer_load_dwordx4 a[176:179], v17, s[40:43], s86 offen offset:0 sc1
MA 162,108,70
WL 4
MA 102,96,74
RT 62,34,49152
MA 106,100,74
RT 64,34,50176
MA 166,104,74
buffer_load_dwordx4 a[180:183], v17, s[40:43], s86 offen offset:1024 sc1
MA 170,108,74
s_add_u32 s86, s86, 4096
WL 4
MA 110,96,54
RT 66,35,49152
MA 114,100,54
RT 68,35,50176
s_add_u32 m0, s52, 40960
MA 174,104,54
buffer_load_dwordx4 v18, s[44:47], s60 offen offset:0 lds
MA 178,108,54
WL 4
MA 118,96,58
RT 70,28,57344
MA 122,100,58
RT 72,28,58368
MA 182,104,58
buffer_load_dwordx4 v19, s[44:47], s60 offen offset:1024 lds
MA 186,108,58
s_add_u32 s60, s60, 229376
WL 4
MA 126,96,62
RT 74,29,57344
MA 130,100,62
RT 76,29,58368
MA 190,104,62
buffer_load_dwordx4 a[184:187], v17, s[40:43], s86 offen offset:0 sc1
MA 194,108,62
WL 4
MA 134,96,66
RT 54,30,57344
MA 138,100,66
RT 56,30,58368
MA 198,104,66
buffer_load_dwordx4 a[188:191], v17, s[40:43], s86 offen offset:1024 sc1
MA 202,108,66
s_add_u32 s86, s86, 4096
s_waitcnt vmcnt(34) lgkmcnt(4)
MA 78,112,70
RT 58,31,57344
MA 82,116,70
RT 60,31,58368
s_add_u32 m0, s52, 49152
MA 142,120,70
buffer_load_dwordx4 v18, s[44:47], s60 offen offset:0 lds
MA 146,124,70
WL 4
MA 86,112,74
RT 62,32,57344
MA 90,116,74
RT 64,32,58368
MA 150,120,74
buffer_load_dwordx4 v19, s[44:47], s60 offen offset:1024 lds
MA 154,124,74
s_add_u32 s60, s60, 229376
WL 4
MA 94,112,54
RT 66,33,57344
MA 98,116,54
RT 68,33,58368
MA 158,120,54
buffer_load_dwordx4 a[32:35], v17, s[40:43], s86 offen offset:0 sc1
MA 162,124,54
WL 4
MA 102,112,58
RT 70,34,57344
MA 106,116,58
RT 72,34,58368
MA 166,120,58
buffer_load_dwordx4 a[36:39], v17, s[40:43], s86 offen offset:1024 sc1
MA 170,124,58
s_add_u32 s86, s86, 4096
WL 4
MA 110,112,62
RT 74,35,57344
MA 114,116,62
RT 76,35,58368
s_add_u32 m0, s52, 57344
MA 174,120,62
buffer_load_dwordx4 v18, s[44:47], s60 offen offset:0 lds
MA 178,124,62
WL 4
MA 118,112,66
s_waitcnt vmcnt(31)
s_barrier
RT 54,36,0
MA 122,116,66
RT 56,36,1024
MA 182,120,66
buffer_load_dwordx4 v19, s[44:47], s60 offen offset:1024 lds
MA 186,124,66
s_add_u32 s60, s60, 229376
WL 4
MA 126,112,70
RT 58,37,0
MA 130,116,70
RT 60,37,1024
MA 190,120,70
buffer_load_dwordx4 a[40:43], v17, s[40:43], s86 offen offset:0 sc1
MA 194,124,70
WL 4
MA 134,112,74
RT 62,38,0
MA 138,116,74
RT 64,38,1024
MA 198,120,74
buffer_load_dwordx4 a[44:47], v17, s[40:43], s86 offen offset:1024 sc1
MA 202,124,74
s_add_u32 s86, s86, 4096
WL 4
MA 142,128,54
RT 66,39,0
MA 146,132,54
RT 68,39,1024
WL 4
MA 150,128,58
RT 70,40,0
s_add_u32 m0, s52, 65536
MA 154,132,58
RT 72,40,1024
buffer_load_dwordx4 v18, s[44:47], s60 offen offset:0 lds
WL 4
MA 158,128,62
RT 74,41,0
MA 162,132,62
RT 76,41,1024
WL 4
MA 166,128,66
RT 54,42,0
MA 170,132,66
RT 56,42,1024
buffer_load_dwordx4 v19, s[44:47], s60 offen offset:1024 lds
WL 4
MA 174,128,70
RT 58,43,0
s_add_u32 s60, s60, 229376
MA 178,132,70
RT 60,43,1024
WL 4
MA 182,128,74
RT 62,36,8192
MA 186,132,74
RT 64,36,9216
buffer_load_dwordx4 a[48:51], v17, s[40:43], s86 offen offset:0 sc1
WL 4
MA 190,128,54
RT 66,37,8192
MA 194,132,54
RT 68,37,9216
WL 4
MA 198,128,58
RT 70,38,8192
MA 202,132,58
RT 72,38,9216
buffer_load_dwordx4 a[52:55], v17, s[40:43], s86 offen offset:1024 sc1
s_waitcnt vmcnt(36) lgkmcnt(4)
MA 142,136,62
RT 74,39,8192
s_add_u32 s86, s86, 4096
MA 146,140,62
RT 76,39,9216
WL 4
MA 150,136,66
RT 54,40,8192
s_add_u32 m0, s52, 73728
MA 154,140,66
RT 56,40,9216
buffer_load_dwordx4 v18, s[44:47], s60 offen offset:0 lds
WL 4
MA 158,136,70
RT 58,41,8192
MA 162,140,70
RT 60,41,9216
WL 4
MA 166,136,74
RT 62,42,8192
MA 170,140,74
RT 64,42,9216
buffer_load_dwordx4 v19, s[44:47], s60 offen offset:1024 lds
WL 4
MA 174,136,54
RT 66,43,8192
s_add_u32 s60, s60, 229376
MA 178,140,54
RT 68,43,9216
WL 4
MA 182,136,58
RT 70,36,16384
MA 186,140,58
RT 72,36,17408
buffer_load_dwordx4 a[56:59], v17, s[40:43], s86 offen offset:0 sc1
WL 4
MA 190,136,62
RT 74,37,16384
MA 194,140,62
RT 76,37,17408
WL 4
MA 198,136,66
RT 54,38,16384
MA 202,140,66
RT 56,38,17408
buffer_load_dwordx4 a[60:63], v17, s[40:43], s86 offen offset:1024 sc1
s_waitcnt vmcnt(36) lgkmcnt(4)
MA 142,144,70
RT 58,39,16384
s_add_u32 s86, s86, 4096
MA 146,148,70
RT 60,39,17408
WL 4
MA 150,144,74
RT 62,40,16384
s_add_u32 m0, s52, 81920
MA 154,148,74
RT 64,40,17408
buffer_load_dwordx4 v18, s[44:47], s60 offen offset:0 lds
WL 4
MA 158,144,54
RT 66,41,16384
MA 162,148,54
RT 68,41,17408
WL 4
MA 166,144,58
RT 70,42,16384
MA 170,148,58
RT 72,42,17408
buffer_load_dwordx4 v19, s[44:47], s60 offen offset:1024 lds
WL 4
MA 174,144,62
RT 74,43,16384
s_add_u32 s60, s60, 229376
MA 178,148,62
RT 76,43,17408
WL 4
MA 182,144,66
RT 54,36,24576
MA 186,148,66
RT 56,36,25600
buffer_load_dwordx4 a[64:67], v17, s[40:43], s86 offen offset:0 sc1
WL 4
MA 190,144,70
RT 58,37,24576
MA 194,148,70
RT 60,37,25600
WL 4
MA 198,144,74
RT 62,38,24576
MA 202,148,74
RT 64,38,25600
buffer_load_dwordx4 a[68:71], v17, s[40:43], s86 offen offset:1024 sc1
s_waitcnt vmcnt(36) lgkmcnt(4)
MA 142,152,54
RT 66,39,24576
s_add_u32 s86, s86, 4096
MA 146,156,54
RT 68,39,25600
WL 4
MA 150,152,58
RT 70,40,24576
s_add_u32 m0, s52, 90112
MA 154,156,58
RT 72,40,25600
buffer_load_dwordx4 v18, s[44:47], s60 offen offset:0 lds
WL 4
MA 158,152,62
RT 74,41,24576
MA 162,156,62
RT 76,41,25600
WL 4
MA 166,152,66
RT 54,42,24576
MA 170,156,66
RT 56,42,25600
buffer_load_dwordx4 v19, s[44:47], s60 offen offset:1024 lds
WL 4
MA 174,152,70
RT 58,43,24576
s_add_u32 s60, s60, 229376
MA 178,156,70
RT 60,43,25600
WL 4
MA 182,152,74
s_waitcnt vmcnt(32)
s_barrier
RT 62,20,0
MA 186,156,74
RT 64,20,1024
buffer_load_dwordx4 a[72:75], v17, s[40:43], s86 offen offset:0 sc1
WL 4
MA 190,152,54
RT 66,21,0
MA 194,156,54
RT 68,21,1024
WL 4
MA 198,152,58
RT 70,22,0
MA 202,156,58
RT 72,22,1024
buffer_load_dwordx4 a[76:79], v17, s[40:43], s86 offen offset:1024 sc1
WL 4
MA 142,160,62
RT 74,23,0
s_add_u32 s86, s86, 4096
MA 146,164,62
RT 76,23,1024
WL 4
MA 150,160,66
RT 54,24,0
s_add_u32 m0, s52, 98304
MA 154,164,66
RT 56,24,1024
buffer_load_dwordx4 v18, s[44:47], s60 offen offset:0 lds
WL 4
MA 158,160,70
RT 58,25,0
MA 162,164,70
RT 60,25,1024
WL 4
MA 166,160,74
RT 62,26,0
MA 170,164,74
RT 64,26,1024
buffer_load_dwordx4 v19, s[44:47], s60 offen offset:1024 lds
WL 4
MA 174,160,54
RT 66,27,0
s_add_u32 s60, s60, 229376
MA 178,164,54
)"
R"(RT 68,27,1024
WL 4
MA 182,160,58
RT 70,20,8192
MA 186,164,58
RT 72,20,9216
buffer_load_dwordx4 a[80:83], v17, s[40:43], s86 offen offset:0 sc1
WL 4
MA 190,160,62
RT 74,21,8192
MA 194,164,62
RT 76,21,9216
WL 4
MA 198,160,66
RT 54,22,8192
MA 202,164,66
RT 56,22,9216
buffer_load_dwordx4 a[84:87], v17, s[40:43], s86 offen offset:1024 sc1
s_waitcnt vmcnt(36) lgkmcnt(4)
MA 142,168,70
RT 58,23,8192
s_add_u32 s86, s86, 4096
MA 146,172,70
RT 60,23,9216
WL 4
MA 150,168,74
RT 62,24,8192
s_add_u32 m0, s52, 106496
MA 154,172,74
RT 64,24,9216
buffer_load_dwordx4 v18, s[44:47], s60 offen offset:0 lds
WL 4
MA 158,168,54
RT 66,25,8192
MA 162,172,54
RT 68,25,9216
WL 4
MA 166,168,58
RT 70,26,8192
MA 170,172,58
RT 72,26,9216
buffer_load_dwordx4 v19, s[44:47], s60 offen offset:1024 lds
WL 4
MA 174,168,62
RT 74,27,8192
s_add_u32 s60, s60, 229376
MA 178,172,62
RT 76,27,9216
WL 4
MA 182,168,66
RT 54,20,16384
MA 186,172,66
RT 56,20,17408
buffer_load_dwordx4 a[88:91], v17, s[40:43], s86 offen offset:0 sc1
WL 4
MA 190,168,70
RT 58,21,16384
MA 194,172,70
RT 60,21,17408
WL 4
MA 198,168,74
RT 62,22,16384
MA 202,172,74
RT 64,22,17408
buffer_load_dwordx4 a[92:95], v17, s[40:43], s86 offen offset:1024 sc1
s_waitcnt vmcnt(36) lgkmcnt(4)
MA 142,176,54
RT 66,23,16384
s_add_u32 s86, s86, 4096
MA 146,180,54
RT 68,23,17408
WL 4
MA 150,176,58
RT 70,24,16384
s_add_u32 m0, s52, 114688
MA 154,180,58
RT 72,24,17408
buffer_load_dwordx4 v18, s[44:47], s60 offen offset:0 lds
WL 4
MA 158,176,62
RT 74,25,16384
MA 162,180,62
RT 76,25,17408
WL 4
MA 166,176,66
RT 54,26,16384
MA 170,180,66
RT 56,26,17408
buffer_load_dwordx4 v19, s[44:47], s60 offen offset:1024 lds
WL 4
MA 174,176,70
RT 58,27,16384
s_add_u32 s60, s60, 229376
MA 178,180,70
RT 60,27,17408
WL 4
MA 182,176,74
RT 62,20,24576
MA 186,180,74
RT 64,20,25600
buffer_load_dwordx4 a[96:99], v17, s[40:43], s86 offen offset:0 sc1
WL 4
MA 190,176,54
RT 66,21,24576
MA 194,180,54
RT 68,21,25600
WL 4
MA 198,176,58
RT 70,22,24576
MA 202,180,58
RT 72,22,25600
buffer_load_dwordx4 a[100:103], v17, s[40:43], s86 offen offset:1024 sc1
s_waitcnt vmcnt(36) lgkmcnt(4)
MA 142,184,62
RT 74,23,24576
s_add_u32 s86, s86, 4096
MA 146,188,62
RT 76,23,25600
WL 4
MA 150,184,66
RT 54,24,24576
s_add_u32 m0, s52, 122880
MA 154,188,66
RT 56,24,25600
buffer_load_dwordx4 v18, s[44:47], s60 offen offset:0 lds
WL 4
MA 158,184,70
RT 58,25,24576
MA 162,188,70
RT 60,25,25600
WL 4
MA 166,184,74
RT 62,26,24576
MA 170,188,74
RT 64,26,25600
buffer_load_dwordx4 v19, s[44:47], s60 offen offset:1024 lds
WL 4
MA 174,184,54
RT 66,27,24576
s_add_u32 s60, s60, 229376
MA 178,188,54
RT 68,27,25600
WL 4
MA 182,184,58
s_waitcnt vmcnt(32)
s_barrier
RT 70,20,32768
MA 186,188,58
RT 72,20,33792
buffer_load_dwordx4 a[104:107], v17, s[40:43], s86 offen offset:0 sc1
WL 4
MA 190,184,62
RT 74,21,32768
MA 194,188,62
RT 76,21,33792
WL 4
MA 198,184,66
RT 54,22,32768
MA 202,188,66
RT 56,22,33792
buffer_load_dwordx4 a[108:111], v17, s[40:43], s86 offen offset:1024 sc1
WL 4
MA 142,32,70
RT 58,23,32768
s_add_u32 s86, s86, 4096
MA 146,36,70
RT 60,23,33792
WL 4
MA 150,32,74
RT 62,24,32768
MA 154,36,74
RT 64,24,33792
buffer_load_dwordx4 a[112:115], v17, s[40:43], s86 offen offset:0 sc1
WL 4
MA 158,32,54
RT 66,25,32768
MA 162,36,54
RT 68,25,33792
WL 4
MA 166,32,58
RT 70,26,32768
MA 170,36,58
RT 72,26,33792
buffer_load_dwordx4 a[116:119], v17, s[40:43], s86 offen offset:1024 sc1
WL 4
MA 174,32,62
RT 74,27,32768
s_add_u32 s86, s86, 4096
MA 178,36,62
RT 76,27,33792
WL 4
MA 182,32,66
RT 54,20,40960
MA 186,36,66
RT 56,20,41984
buffer_load_dwordx4 a[120:123], v17, s[40:43], s86 offen offset:0 sc1
WL 4
MA 190,32,70
RT 58,21,40960
MA 194,36,70
RT 60,21,41984
WL 4
MA 198,32,74
RT 62,22,40960
MA 202,36,74
RT 64,22,41984
buffer_load_dwordx4 a[124:127], v17, s[40:43], s86 offen offset:1024 sc1
s_waitcnt vmcnt(36) lgkmcnt(4)
MA 142,40,54
RT 66,23,40960
s_add_u32 s86, s86, 4096
MA 146,44,54
RT 68,23,41984
WL 4
MA 150,40,58
RT 70,24,40960
MA 154,44,58
RT 72,24,41984
buffer_load_dwordx4 a[128:131], v16, s[40:43], s63 offen offset:0 sc1
WL 4
MA 158,40,62
RT 74,25,40960
MA 162,44,62
RT 76,25,41984
WL 4
MA 166,40,66
RT 54,26,40960
MA 170,44,66
RT 56,26,41984
buffer_load_dwordx4 a[132:135], v16, s[40:43], s63 offen offset:1024 sc1
WL 4
MA 174,40,70
RT 58,27,40960
s_add_u32 s63, s63, 4096
MA 178,44,70
RT 60,27,41984
WL 4
MA 182,40,74
RT 62,20,49152
MA 186,44,74
RT 64,20,50176
buffer_load_dwordx4 a[136:139], v16, s[40:43], s63 offen offset:0 sc1
WL 4
MA 190,40,54
RT 66,21,49152
MA 194,44,54
RT 68,21,50176
WL 4
MA 198,40,58
RT 70,22,49152
MA 202,44,58
RT 72,22,50176
buffer_load_dwordx4 a[140:143], v16, s[40:43], s63 offen offset:1024 sc1
s_waitcnt vmcnt(36) lgkmcnt(4)
MA 142,48,62
RT 74,23,49152
s_add_u32 s63, s63, 4096
MA 146,52,62
RT 76,23,50176
WL 4
MA 150,48,66
RT 54,24,49152
MA 154,52,66
RT 56,24,50176
buffer_load_dwordx4 a[144:147], v16, s[40:43], s63 offen offset:0 sc1
WL 4
MA 158,48,70
RT 58,25,49152
MA 162,52,70
RT 60,25,50176
WL 4
MA 166,48,74
RT 62,26,49152
MA 170,52,74
RT 64,26,50176
buffer_load_dwordx4 a[148:151], v16, s[40:43], s63 offen offset:1024 sc1
WL 4
MA 174,48,54
RT 66,27,49152
s_add_u32 s63, s63, 4096
MA 178,52,54
RT 68,27,50176
WL 4
MA 182,48,58
RT 70,20,57344
MA 186,52,58
RT 72,20,58368
WL 4
MA 190,48,62
RT 74,21,57344
MA 194,52,62
RT 76,21,58368
WL 4
MA 198,48,66
RT 54,22,57344
MA 202,52,66
RT 56,22,58368
s_branch L_join_%=
L_cls0_%=:
s_waitcnt vmcnt(12) lgkmcnt(4)
v_mov_b32 v206, s98
v_mov_b32 v207, s99
s_mov_b64 exec, 1
buffer_store_dwordx2 v[206:207], off, s[92:95], s89 offset:256 sc1
s_mov_b64 exec, -1
v_mfma_f32_16x16x32_bf16 v[78:81], a[32:35], v[54:57], 0
RT 66,23,0
v_mfma_f32_16x16x32_bf16 v[82:85], a[36:39], v[54:57], 0
RT 68,23,1024
v_mfma_f32_16x16x32_bf16 v[142:145], a[40:43], v[54:57], 0
buffer_load_dwordx4 a[96:99], v16, s[40:43], s63 offen offset:0 sc1
v_mfma_f32_16x16x32_bf16 v[146:149], a[44:47], v[54:57], 0
WL 4
v_mfma_f32_16x16x32_bf16 v[86:89], a[32:35], v[58:61], 0
RT 70,24,0
v_mfma_f32_16x16x32_bf16 v[90:93], a[36:39], v[58:61], 0
RT 72,24,1024
v_mfma_f32_16x16x32_bf16 v[150:153], a[40:43], v[58:61], 0
buffer_load_dwordx4 a[100:103], v16, s[40:43], s63 offen offset:1024 sc1
v_mfma_f32_16x16x32_bf16 v[154:157], a[44:47], v[58:61], 0
s_add_u32 s63, s63, 4096
WL 4
v_mfma_f32_16x16x32_bf16 v[94:97], a[32:35], v[62:65], 0
RT 74,25,0
v_mfma_f32_16x16x32_bf16 v[98:101], a[36:39], v[62:65], 0
RT 76,25,1024
v_mfma_f32_16x16x32_bf16 v[158:161], a[40:43], v[62:65], 0
buffer_load_dwordx4 a[104:107], v17, s[40:43], s86 offen offset:0 sc1
v_mfma_f32_16x16x32_bf16 v[162:165], a[44:47], v[62:65], 0
WL 4
v_mfma_f32_16x16x32_bf16 v[102:105], a[32:35], v[66:69], 0
RT 54,26,0
v_mfma_f32_16x16x32_bf16 v[106:109], a[36:39], v[66:69], 0
RT 56,26,1024
v_mfma_f32_16x16x32_bf16 v[166:169], a[40:43], v[66:69], 0
buffer_load_dwordx4 a[108:111], v17, s[40:43], s86 offen offset:1024 sc1
v_mfma_f32_16x16x32_bf16 v[170:173], a[44:47], v[66:69], 0
s_add_u32 s86, s86, 4096
WL 4
v_mfma_f32_16x16x32_bf16 v[110:113], a[32:35], v[70:73], 0
RT 58,27,0
v_mfma_f32_16x16x32_bf16 v[114:117], a[36:39], v[70:73], 0
RT 60,27,1024
v_mfma_f32_16x16x32_bf16 v[174:177], a[40:43], v[70:73], 0
buffer_load_dwordx4 a[112:115], v16, s[40:43], s63 offen offset:0 sc1
v_mfma_f32_16x16x32_bf16 v[178:181], a[44:47], v[70:73], 0
WL 4
v_mfma_f32_16x16x32_bf16 v[118:121], a[32:35], v[74:77], 0
RT 62,20,8192
v_mfma_f32_16x16x32_bf16 v[122:125], a[36:39], v[74:77], 0
RT 64,20,9216
v_mfma_f32_16x16x32_bf16 v[182:185], a[40:43], v[74:77], 0
buffer_load_dwordx4 a[116:119], v16, s[40:43], s63 offen offset:1024 sc1
v_mfma_f32_16x16x32_bf16 v[186:189], a[44:47], v[74:77], 0
s_add_u32 s63, s63, 4096
WL 4
v_mfma_f32_16x16x32_bf16 v[126:129], a[32:35], v[54:57], 0
RT 66,21,8192
v_mfma_f32_16x16x32_bf16 v[130:133], a[36:39], v[54:57], 0
RT 68,21,9216
v_mfma_f32_16x16x32_bf16 v[190:193], a[40:43], v[54:57], 0
buffer_load_dwordx4 a[120:123], v17, s[40:43], s86 offen offset:0 sc1
v_mfma_f32_16x16x32_bf16 v[194:197], a[44:47], v[54:57], 0
WL 4
v_mfma_f32_16x16x32_bf16 v[134:137], a[32:35], v[58:61], 0
RT 70,22,8192
v_mfma_f32_16x16x32_bf16 v[138:141], a[36:39], v[58:61], 0
RT 72,22,9216
v_mfma_f32_16x16x32_bf16 v[198:201], a[40:43], v[58:61], 0
buffer_load_dwordx4 a[124:127], v17, s[40:43], s86 offen offset:1024 sc1
v_mfma_f32_16x16x32_bf16 v[202:205], a[44:47], v[58:61], 0
s_add_u32 s86, s86, 4096
s_waitcnt vmcnt(17) lgkmcnt(4)
MA 78,48,62
RT 74,23,8192
MA 82,52,62
RT 76,23,9216
MA 142,56,62
buffer_load_dword v50, v49, s[44:47], s58 offen
MA 146,60,62
s_add_u32 s58, s58, 3670016
s_cmp_ge_u32 s58, s61
s_cselect_b32 s58, 0x40000000, s58
WL 4
MA 86,48,66
RT 54,24,8192
MA 90,52,66
RT 56,24,9216
MA 150,56,66
MA 154,60,66
WL 4
MA 94,48,70
RT 58,25,8192
MA 98,52,70
RT 60,25,9216
MA 158,56,70
MA 162,60,70
WL 4
MA 102,48,74
RT 62,26,8192
)"
R"(MA 106,52,74
RT 64,26,9216
MA 166,56,74
MA 170,60,74
WL 4
MA 110,48,54
RT 66,27,8192
MA 114,52,54
RT 68,27,9216
MA 174,56,54
MA 178,60,54
WL 4
MA 118,48,58
s_barrier
RT 70,20,16384
MA 122,52,58
RT 72,20,17408
MA 182,56,58
MA 186,60,58
WL 4
MA 126,48,62
RT 74,21,16384
MA 130,52,62
RT 76,21,17408
MA 190,56,62
MA 194,60,62
WL 4
MA 134,48,66
RT 54,22,16384
MA 138,52,66
RT 56,22,17408
MA 198,56,66
MA 202,60,66
s_waitcnt vmcnt(14) lgkmcnt(4)
MA 78,64,70
RT 58,23,16384
MA 82,68,70
RT 60,23,17408
MA 142,72,70
buffer_load_dwordx4 a[128:131], v16, s[40:43], s63 offen offset:0 sc1
MA 146,76,70
WL 4
MA 86,64,74
RT 62,24,16384
MA 90,68,74
RT 64,24,17408
MA 150,72,74
buffer_load_dwordx4 a[132:135], v16, s[40:43], s63 offen offset:1024 sc1
MA 154,76,74
s_add_u32 s63, s63, 4096
WL 4
MA 94,64,54
RT 66,25,16384
MA 98,68,54
RT 68,25,17408
MA 158,72,54
buffer_load_dwordx4 a[136:139], v17, s[40:43], s86 offen offset:0 sc1
MA 162,76,54
WL 4
MA 102,64,58
RT 70,26,16384
MA 106,68,58
RT 72,26,17408
MA 166,72,58
buffer_load_dwordx4 a[140:143], v17, s[40:43], s86 offen offset:1024 sc1
MA 170,76,58
s_add_u32 s86, s86, 4096
WL 4
MA 110,64,62
RT 74,27,16384
MA 114,68,62
RT 76,27,17408
MA 174,72,62
buffer_load_dwordx4 a[144:147], v16, s[40:43], s63 offen offset:0 sc1
MA 178,76,62
WL 4
MA 118,64,66
RT 54,20,24576
MA 122,68,66
RT 56,20,25600
MA 182,72,66
buffer_load_dwordx4 a[148:151], v16, s[40:43], s63 offen offset:1024 sc1
MA 186,76,66
s_add_u32 s63, s63, 4096
WL 4
MA 126,64,70
RT 58,21,24576
MA 130,68,70
RT 60,21,25600
MA 190,72,70
buffer_load_dwordx4 a[152:155], v17, s[40:43], s86 offen offset:0 sc1
MA 194,76,70
WL 4
MA 134,64,74
RT 62,22,24576
MA 138,68,74
RT 64,22,25600
MA 198,72,74
buffer_load_dwordx4 a[156:159], v17, s[40:43], s86 offen offset:1024 sc1
MA 202,76,74
s_add_u32 s86, s86, 4096
s_waitcnt vmcnt(18) lgkmcnt(4)
MA 78,80,54
RT 66,23,24576
MA 82,84,54
RT 68,23,25600
s_add_u32 m0, s52, 65536
MA 142,88,54
buffer_load_dwordx4 v18, s[44:47], s60 offen offset:0 lds
MA 146,92,54
WL 4
MA 86,80,58
RT 70,24,24576
MA 90,84,58
RT 72,24,25600
MA 150,88,58
buffer_load_dwordx4 v19, s[44:47], s60 offen offset:1024 lds
MA 154,92,58
s_add_u32 s60, s60, 229376
WL 4
MA 94,80,62
RT 74,25,24576
MA 98,84,62
RT 76,25,25600
MA 158,88,62
buffer_load_dwordx4 a[160:163], v16, s[40:43], s63 offen offset:0 sc1
MA 162,92,62
WL 4
MA 102,80,66
RT 54,26,24576
MA 106,84,66
RT 56,26,25600
MA 166,88,66
buffer_load_dwordx4 a[164:167], v16, s[40:43], s63 offen offset:1024 sc1
MA 170,92,66
s_add_u32 s63, s63, 4096
WL 4
MA 110,80,70
RT 58,27,24576
MA 114,84,70
RT 60,27,25600
MA 174,88,70
buffer_load_dwordx4 a[168:171], v17, s[40:43], s86 offen offset:0 sc1
MA 178,92,70
WL 4
MA 118,80,74
s_barrier
RT 62,20,32768
MA 122,84,74
RT 64,20,33792
MA 182,88,74
buffer_load_dwordx4 a[172:175], v17, s[40:43], s86 offen offset:1024 sc1
MA 186,92,74
s_add_u32 s86, s86, 4096
WL 4
MA 126,80,54
RT 66,21,32768
MA 130,84,54
RT 68,21,33792
s_add_u32 m0, s52, 73728
MA 190,88,54
buffer_load_dwordx4 v18, s[44:47], s60 offen offset:0 lds
MA 194,92,54
WL 4
MA 134,80,58
RT 70,22,32768
MA 138,84,58
RT 72,22,33792
MA 198,88,58
buffer_load_dwordx4 v19, s[44:47], s60 offen offset:1024 lds
MA 202,92,58
s_add_u32 s60, s60, 229376
s_waitcnt vmcnt(21) lgkmcnt(4)
MA 78,96,62
RT 74,23,32768
MA 82,100,62
RT 76,23,33792
s_add_u32 m0, s52, 81920
MA 142,104,62
buffer_load_dwordx4 v18, s[44:47], s60 offen offset:0 lds
MA 146,108,62
WL 4
MA 86,96,66
RT 54,24,32768
MA 90,100,66
RT 56,24,33792
MA 150,104,66
buffer_load_dwordx4 v19, s[44:47], s60 offen offset:1024 lds
MA 154,108,66
s_add_u32 s60, s60, 229376
WL 4
MA 94,96,70
RT 58,25,32768
MA 98,100,70
RT 60,25,33792
MA 158,104,70
buffer_load_dwordx4 a[176:179], v16, s[40:43], s63 offen offset:0 sc1
MA 162,108,70
WL 4
MA 102,96,74
RT 62,26,32768
MA 106,100,74
RT 64,26,33792
MA 166,104,74
buffer_load_dwordx4 a[180:183], v16, s[40:43], s63 offen offset:1024 sc1
MA 170,108,74
s_add_u32 s63, s63, 4096
WL 4
MA 110,96,54
RT 66,27,32768
MA 114,100,54
RT 68,27,33792
MA 174,104,54
buffer_load_dwordx4 a[184:187], v17, s[40:43], s86 offen offset:0 sc1
MA 178,108,54
WL 4
MA 118,96,58
RT 70,20,40960
MA 122,100,58
RT 72,20,41984
MA 182,104,58
buffer_load_dwordx4 a[188:191], v17, s[40:43], s86 offen offset:1024 sc1
MA 186,108,58
s_add_u32 s86, s86, 4096
WL 4
MA 126,96,62
RT 74,21,40960
MA 130,100,62
RT 76,21,41984
s_add_u32 m0, s52, 90112
MA 190,104,62
buffer_load_dwordx4 v18, s[44:47], s60 offen offset:0 lds
MA 194,108,62
WL 4
MA 134,96,66
RT 54,22,40960
MA 138,100,66
RT 56,22,41984
MA 198,104,66
buffer_load_dwordx4 v19, s[44:47], s60 offen offset:1024 lds
MA 202,108,66
s_add_u32 s60, s60, 229376
buffer_load_dwordx2 v[44:45], v47, s[92:95], 0 offen offset:256 sc1
s_waitcnt vmcnt(26) lgkmcnt(4)
MA 78,112,70
RT 58,23,40960
MA 82,116,70
RT 60,23,41984
MA 142,120,70
buffer_load_dwordx4 a[32:35], v16, s[40:43], s63 offen offset:0 sc1
MA 146,124,70
WL 4
MA 86,112,74
RT 62,24,40960
MA 90,116,74
RT 64,24,41984
MA 150,120,74
buffer_load_dwordx4 a[36:39], v16, s[40:43], s63 offen offset:1024 sc1
MA 154,124,74
s_add_u32 s63, s63, 4096
WL 4
MA 94,112,54
RT 66,25,40960
MA 98,116,54
RT 68,25,41984
MA 158,120,54
buffer_load_dwordx4 a[40:43], v17, s[40:43], s86 offen offset:0 sc1
MA 162,124,54
WL 4
MA 102,112,58
RT 70,26,40960
MA 106,116,58
RT 72,26,41984
MA 166,120,58
buffer_load_dwordx4 a[44:47], v17, s[40:43], s86 offen offset:1024 sc1
MA 170,124,58
s_add_u32 s86, s86, 4096
WL 4
MA 110,112,62
RT 74,27,40960
MA 114,116,62
RT 76,27,41984
s_add_u32 m0, s52, 98304
MA 174,120,62
buffer_load_dwordx4 v18, s[44:47], s60 offen offset:0 lds
MA 178,124,62
WL 4
MA 118,112,66
s_barrier
RT 54,20,49152
MA 122,116,66
RT 56,20,50176
MA 182,120,66
buffer_load_dwordx4 v19, s[44:47], s60 offen offset:1024 lds
MA 186,124,66
s_add_u32 s60, s60, 229376
WL 4
MA 126,112,70
RT 58,21,49152
MA 130,116,70
RT 60,21,50176
MA 190,120,70
buffer_load_dwordx4 a[48:51], v16, s[40:43], s63 offen offset:0 sc1
MA 194,124,70
WL 4
MA 134,112,74
RT 62,22,49152
MA 138,116,74
RT 64,22,50176
MA 198,120,74
buffer_load_dwordx4 a[52:55], v16, s[40:43], s63 offen offset:1024 sc1
MA 202,124,74
s_add_u32 s63, s63, 4096
s_waitcnt vmcnt(29) lgkmcnt(4)
MA 78,128,54
RT 66,23,49152
MA 82,132,54
RT 68,23,50176
MA 142,136,54
buffer_load_dwordx4 a[56:59], v17, s[40:43], s86 offen offset:0 sc1
MA 146,140,54
WL 4
MA 86,128,58
RT 70,24,49152
MA 90,132,58
RT 72,24,50176
MA 150,136,58
buffer_load_dwordx4 a[60:63], v17, s[40:43], s86 offen offset:1024 sc1
MA 154,140,58
s_add_u32 s86, s86, 4096
WL 4
MA 94,128,62
RT 74,25,49152
MA 98,132,62
RT 76,25,50176
s_add_u32 m0, s52, 106496
MA 158,136,62
buffer_load_dwordx4 v18, s[44:47], s60 offen offset:0 lds
MA 162,140,62
WL 4
MA 102,128,66
RT 54,26,49152
MA 106,132,66
RT 56,26,50176
MA 166,136,66
buffer_load_dwordx4 v19, s[44:47], s60 offen offset:1024 lds
MA 170,140,66
s_add_u32 s60, s60, 229376
WL 4
MA 110,128,70
RT 58,27,49152
MA 114,132,70
RT 60,27,50176
MA 174,136,70
buffer_load_dwordx4 a[64:67], v16, s[40:43], s63 offen offset:0 sc1
MA 178,140,70
WL 4
MA 118,128,74
RT 62,20,57344
MA 122,132,74
RT 64,20,58368
MA 182,136,74
buffer_load_dwordx4 a[68:71], v16, s[40:43], s63 offen offset:1024 sc1
MA 186,140,74
s_add_u32 s63, s63, 4096
WL 4
MA 126,128,54
RT 66,21,57344
MA 130,132,54
RT 68,21,58368
MA 190,136,54
buffer_load_dwordx4 a[72:75], v17, s[40:43], s86 offen offset:0 sc1
MA 194,140,54
WL 4
MA 134,128,58
RT 70,22,57344
MA 138,132,58
RT 72,22,58368
MA 198,136,58
buffer_load_dwordx4 a[76:79], v17, s[40:43], s86 offen offset:1024 sc1
MA 202,140,58
s_add_u32 s86, s86, 4096
s_waitcnt vmcnt(33) lgkmcnt(4)
MA 78,144,62
RT 74,23,57344
MA 82,148,62
RT 76,23,58368
s_add_u32 m0, s52, 114688
MA 142,152,62
buffer_load_dwordx4 v18, s[44:47], s60 offen offset:0 lds
MA 146,156,62
WL 4
MA 86,144,66
RT 54,24,57344
MA 90,148,66
RT 56,24,58368
MA 150,152,66
buffer_load_dwordx4 v19, s[44:47], s60 offen offset:1024 lds
MA 154,156,66
s_add_u32 s60, s60, 229376
)"
R"(WL 4
MA 94,144,70
RT 58,25,57344
MA 98,148,70
RT 60,25,58368
MA 158,152,70
buffer_load_dwordx4 a[80:83], v16, s[40:43], s63 offen offset:0 sc1
MA 162,156,70
WL 4
MA 102,144,74
RT 62,26,57344
MA 106,148,74
RT 64,26,58368
MA 166,152,74
buffer_load_dwordx4 a[84:87], v16, s[40:43], s63 offen offset:1024 sc1
MA 170,156,74
s_add_u32 s63, s63, 4096
WL 4
MA 110,144,54
RT 66,27,57344
MA 114,148,54
RT 68,27,58368
MA 174,152,54
buffer_load_dwordx4 a[88:91], v17, s[40:43], s86 offen offset:0 sc1
MA 178,156,54
WL 4
MA 118,144,58
s_waitcnt vmcnt(30)
s_barrier
RT 70,28,0
MA 122,148,58
RT 72,28,1024
MA 182,152,58
buffer_load_dwordx4 a[92:95], v17, s[40:43], s86 offen offset:1024 sc1
MA 186,156,58
s_add_u32 s86, s86, 4096
WL 4
MA 126,144,62
RT 74,29,0
MA 130,148,62
RT 76,29,1024
s_add_u32 m0, s52, 122880
MA 190,152,62
buffer_load_dwordx4 v18, s[44:47], s60 offen offset:0 lds
MA 194,156,62
WL 4
MA 134,144,66
RT 54,30,0
MA 138,148,66
RT 56,30,1024
MA 198,152,66
buffer_load_dwordx4 v19, s[44:47], s60 offen offset:1024 lds
MA 202,156,66
s_add_u32 s60, s60, 229376
WL 4
MA 78,160,70
RT 58,31,0
MA 82,164,70
RT 60,31,1024
MA 142,168,70
buffer_load_dword v50, v49, s[44:47], s58 offen
MA 146,172,70
s_add_u32 s58, s58, 3670016
s_cmp_ge_u32 s58, s61
s_cselect_b32 s58, 0x40000000, s58
WL 4
MA 86,160,74
RT 62,32,0
MA 90,164,74
RT 64,32,1024
MA 150,168,74
MA 154,172,74
WL 4
MA 94,160,54
RT 66,33,0
MA 98,164,54
RT 68,33,1024
MA 158,168,54
MA 162,172,54
WL 4
MA 102,160,58
RT 70,34,0
MA 106,164,58
RT 72,34,1024
MA 166,168,58
MA 170,172,58
WL 4
MA 110,160,62
RT 74,35,0
MA 114,164,62
RT 76,35,1024
MA 174,168,62
MA 178,172,62
WL 4
MA 118,160,66
RT 54,28,8192
MA 122,164,66
RT 56,28,9216
MA 182,168,66
MA 186,172,66
WL 4
MA 126,160,70
RT 58,29,8192
MA 130,164,70
RT 60,29,9216
MA 190,168,70
MA 194,172,70
WL 4
MA 134,160,74
RT 62,30,8192
MA 138,164,74
RT 64,30,9216
MA 198,168,74
MA 202,172,74
s_waitcnt vmcnt(28) lgkmcnt(4)
MA 78,176,54
RT 66,31,8192
MA 82,180,54
RT 68,31,9216
MA 142,184,54
MA 146,188,54
WL 4
MA 86,176,58
RT 70,32,8192
MA 90,180,58
RT 72,32,9216
MA 150,184,58
MA 154,188,58
WL 4
MA 94,176,62
RT 74,33,8192
MA 98,180,62
RT 76,33,9216
MA 158,184,62
MA 162,188,62
WL 4
MA 102,176,66
RT 54,34,8192
MA 106,180,66
RT 56,34,9216
MA 166,184,66
MA 170,188,66
WL 4
MA 110,176,70
RT 58,35,8192
MA 114,180,70
RT 60,35,9216
MA 174,184,70
MA 178,188,70
WL 4
MA 118,176,74
s_waitcnt vmcnt(26)
s_barrier
RT 62,28,16384
MA 122,180,74
RT 64,28,17408
MA 182,184,74
MA 186,188,74
WL 4
MA 126,176,54
RT 66,29,16384
MA 130,180,54
RT 68,29,17408
MA 190,184,54
MA 194,188,54
WL 4
MA 134,176,58
RT 70,30,16384
MA 138,180,58
RT 72,30,17408
MA 198,184,58
MA 202,188,58
s_waitcnt vmcnt(25)
v_cmp_ne_u64 vcc, s[98:99], v[44:45]
s_cmp_eq_u64 vcc, 0
s_cbranch_scc0 L_slowgB0_%=
L_backgB0_%=:
s_waitcnt vmcnt(21) lgkmcnt(4)
MA 78,32,62
RT 74,31,16384
MA 82,36,62
RT 76,31,17408
MA 142,40,62
buffer_load_dwordx4 a[96:99], v16, s[40:43], s63 offen offset:0 sc1
MA 146,44,62
WL 4
MA 86,32,66
RT 54,32,16384
MA 90,36,66
RT 56,32,17408
MA 150,40,66
buffer_load_dwordx4 a[100:103], v16, s[40:43], s63 offen offset:1024 sc1
MA 154,44,66
s_add_u32 s63, s63, 4096
WL 4
MA 94,32,70
RT 58,33,16384
MA 98,36,70
RT 60,33,17408
MA 158,40,70
buffer_load_dwordx4 a[104:107], v17, s[40:43], s86 offen offset:0 sc1
MA 162,44,70
WL 4
MA 102,32,74
RT 62,34,16384
MA 106,36,74
RT 64,34,17408
MA 166,40,74
buffer_load_dwordx4 a[108:111], v17, s[40:43], s86 offen offset:1024 sc1
MA 170,44,74
s_add_u32 s86, s86, 4096
WL 4
MA 110,32,54
RT 66,35,16384
MA 114,36,54
RT 68,35,17408
MA 174,40,54
buffer_load_dwordx4 a[112:115], v16, s[40:43], s63 offen offset:0 sc1
MA 178,44,54
WL 4
MA 118,32,58
RT 70,28,24576
MA 122,36,58
RT 72,28,25600
MA 182,40,58
buffer_load_dwordx4 a[116:119], v16, s[40:43], s63 offen offset:1024 sc1
MA 186,44,58
s_add_u32 s63, s63, 4096
WL 4
MA 126,32,62
RT 74,29,24576
MA 130,36,62
RT 76,29,25600
MA 190,40,62
buffer_load_dwordx4 a[120:123], v17, s[40:43], s86 offen offset:0 sc1
MA 194,44,62
WL 4
MA 134,32,66
RT 54,30,24576
MA 138,36,66
RT 56,30,25600
MA 198,40,66
buffer_load_dwordx4 a[124:127], v17, s[40:43], s86 offen offset:1024 sc1
MA 202,44,66
s_add_u32 s86, s86, 4096
s_waitcnt vmcnt(23) lgkmcnt(4)
MA 78,48,70
RT 58,31,24576
MA 82,52,70
RT 60,31,25600
s_add_u32 m0, s52, 131072
MA 142,56,70
buffer_load_dwordx4 v18, s[44:47], s60 offen offset:0 lds
MA 146,60,70
WL 4
MA 86,48,74
RT 62,32,24576
MA 90,52,74
RT 64,32,25600
MA 150,56,74
buffer_load_dwordx4 v19, s[44:47], s60 offen offset:1024 lds
MA 154,60,74
s_add_u32 s60, s60, 229376
WL 4
MA 94,48,54
RT 66,33,24576
MA 98,52,54
RT 68,33,25600
MA 158,56,54
buffer_load_dwordx4 a[128:131], v16, s[40:43], s63 offen offset:0 sc1
MA 162,60,54
WL 4
MA 102,48,58
RT 70,34,24576
MA 106,52,58
RT 72,34,25600
MA 166,56,58
buffer_load_dwordx4 a[132:135], v16, s[40:43], s63 offen offset:1024 sc1
MA 170,60,58
s_add_u32 s63, s63, 4096
WL 4
MA 110,48,62
RT 74,35,24576
MA 114,52,62
RT 76,35,25600
MA 174,56,62
buffer_load_dwordx4 a[136:139], v17, s[40:43], s86 offen offset:0 sc1
MA 178,60,62
WL 4
MA 118,48,66
s_waitcnt vmcnt(26)
s_barrier
RT 54,28,32768
MA 122,52,66
RT 56,28,33792
MA 182,56,66
buffer_load_dwordx4 a[140:143], v17, s[40:43], s86 offen offset:1024 sc1
MA 186,60,66
s_add_u32 s86, s86, 4096
WL 4
MA 126,48,70
RT 58,29,32768
MA 130,52,70
RT 60,29,33792
s_add_u32 m0, s52, 139264
MA 190,56,70
buffer_load_dwordx4 v18, s[44:47], s60 offen offset:0 lds
MA 194,60,70
WL 4
MA 134,48,74
RT 62,30,32768
MA 138,52,74
RT 64,30,33792
MA 198,56,74
buffer_load_dwordx4 v19, s[44:47], s60 offen offset:1024 lds
MA 202,60,74
s_add_u32 s60, s60, 229376
s_waitcnt vmcnt(25) lgkmcnt(4)
MA 78,64,54
RT 66,31,32768
MA 82,68,54
RT 68,31,33792
s_add_u32 m0, s52, 147456
MA 142,72,54
buffer_load_dwordx4 v18, s[44:47], s60 offen offset:0 lds
MA 146,76,54
WL 4
MA 86,64,58
RT 70,32,32768
MA 90,68,58
RT 72,32,33792
MA 150,72,58
buffer_load_dwordx4 v19, s[44:47], s60 offen offset:1024 lds
MA 154,76,58
s_add_u32 s60, s60, 229376
WL 4
MA 94,64,62
RT 74,33,32768
MA 98,68,62
RT 76,33,33792
MA 158,72,62
buffer_load_dwordx4 a[144:147], v16, s[40:43], s63 offen offset:0 sc1
MA 162,76,62
WL 4
MA 102,64,66
RT 54,34,32768
MA 106,68,66
RT 56,34,33792
MA 166,72,66
buffer_load_dwordx4 a[148:151], v16, s[40:43], s63 offen offset:1024 sc1
MA 170,76,66
s_add_u32 s63, s63, 4096
WL 4
MA 110,64,70
RT 58,35,32768
MA 114,68,70
RT 60,35,33792
MA 174,72,70
buffer_load_dwordx4 a[152:155], v17, s[40:43], s86 offen offset:0 sc1
MA 178,76,70
WL 4
MA 118,64,74
RT 62,28,40960
MA 122,68,74
RT 64,28,41984
MA 182,72,74
buffer_load_dwordx4 a[156:159], v17, s[40:43], s86 offen offset:1024 sc1
MA 186,76,74
s_add_u32 s86, s86, 4096
WL 4
MA 126,64,54
RT 66,29,40960
MA 130,68,54
RT 68,29,41984
s_add_u32 m0, s52, 155648
MA 190,72,54
buffer_load_dwordx4 v18, s[44:47], s60 offen offset:0 lds
MA 194,76,54
WL 4
MA 134,64,58
RT 70,30,40960
MA 138,68,58
RT 72,30,41984
MA 198,72,58
buffer_load_dwordx4 v19, s[44:47], s60 offen offset:1024 lds
MA 202,76,58
s_add_u32 s60, s60, 229376
s_waitcnt vmcnt(27) lgkmcnt(4)
MA 78,80,62
RT 74,31,40960
MA 82,84,62
RT 76,31,41984
MA 142,88,62
buffer_load_dwordx4 a[160:163], v16, s[40:43], s63 offen offset:0 sc1
MA 146,92,62
WL 4
MA 86,80,66
RT 54,32,40960
MA 90,84,66
RT 56,32,41984
)"
R"(MA 150,88,66
buffer_load_dwordx4 a[164:167], v16, s[40:43], s63 offen offset:1024 sc1
MA 154,92,66
s_add_u32 s63, s63, 4096
WL 4
MA 94,80,70
RT 58,33,40960
MA 98,84,70
RT 60,33,41984
MA 158,88,70
buffer_load_dwordx4 a[168:171], v17, s[40:43], s86 offen offset:0 sc1
MA 162,92,70
WL 4
MA 102,80,74
RT 62,34,40960
MA 106,84,74
RT 64,34,41984
MA 166,88,74
buffer_load_dwordx4 a[172:175], v17, s[40:43], s86 offen offset:1024 sc1
MA 170,92,74
s_add_u32 s86, s86, 4096
WL 4
MA 110,80,54
RT 66,35,40960
MA 114,84,54
RT 68,35,41984
s_add_u32 m0, s52, 0
MA 174,88,54
buffer_load_dwordx4 v18, s[44:47], s60 offen offset:0 lds
MA 178,92,54
WL 4
MA 118,80,58
s_waitcnt vmcnt(30)
s_barrier
RT 70,28,49152
MA 122,84,58
RT 72,28,50176
MA 182,88,58
buffer_load_dwordx4 v19, s[44:47], s60 offen offset:1024 lds
MA 186,92,58
s_add_u32 s60, s60, 229376
WL 4
MA 126,80,62
RT 74,29,49152
MA 130,84,62
RT 76,29,50176
MA 190,88,62
buffer_load_dwordx4 a[176:179], v16, s[40:43], s63 offen offset:0 sc1
MA 194,92,62
WL 4
MA 134,80,66
RT 54,30,49152
MA 138,84,66
RT 56,30,50176
MA 198,88,66
buffer_load_dwordx4 a[180:183], v16, s[40:43], s63 offen offset:1024 sc1
MA 202,92,66
s_add_u32 s63, s63, 4096
s_waitcnt vmcnt(28) lgkmcnt(4)
MA 78,96,70
RT 58,31,49152
MA 82,100,70
RT 60,31,50176
MA 142,104,70
buffer_load_dwordx4 a[184:187], v17, s[40:43], s86 offen offset:0 sc1
MA 146,108,70
WL 4
MA 86,96,74
RT 62,32,49152
MA 90,100,74
RT 64,32,50176
MA 150,104,74
buffer_load_dwordx4 a[188:191], v17, s[40:43], s86 offen offset:1024 sc1
MA 154,108,74
s_add_u32 s86, s86, 4096
WL 4
MA 94,96,54
RT 66,33,49152
MA 98,100,54
RT 68,33,50176
s_add_u32 m0, s52, 8192
MA 158,104,54
buffer_load_dwordx4 v18, s[44:47], s60 offen offset:0 lds
MA 162,108,54
WL 4
MA 102,96,58
RT 70,34,49152
MA 106,100,58
RT 72,34,50176
MA 166,104,58
buffer_load_dwordx4 v19, s[44:47], s60 offen offset:1024 lds
MA 170,108,58
s_add_u32 s60, s60, 229376
WL 4
MA 110,96,62
RT 74,35,49152
MA 114,100,62
RT 76,35,50176
MA 174,104,62
buffer_load_dwordx4 a[32:35], v16, s[40:43], s63 offen offset:0 sc1
MA 178,108,62
WL 4
MA 118,96,66
RT 54,28,57344
MA 122,100,66
RT 56,28,58368
MA 182,104,66
buffer_load_dwordx4 a[36:39], v16, s[40:43], s63 offen offset:1024 sc1
MA 186,108,66
s_add_u32 s63, s63, 4096
WL 4
MA 126,96,70
RT 58,29,57344
MA 130,100,70
RT 60,29,58368
MA 190,104,70
buffer_load_dwordx4 a[40:43], v17, s[40:43], s86 offen offset:0 sc1
MA 194,108,70
WL 4
MA 134,96,74
RT 62,30,57344
MA 138,100,74
RT 64,30,58368
MA 198,104,74
buffer_load_dwordx4 a[44:47], v17, s[40:43], s86 offen offset:1024 sc1
MA 202,108,74
s_add_u32 s86, s86, 4096
s_waitcnt vmcnt(32) lgkmcnt(4)
MA 78,112,54
RT 66,31,57344
MA 82,116,54
RT 68,31,58368
s_add_u32 m0, s52, 16384
MA 142,120,54
buffer_load_dwordx4 v18, s[44:47], s60 offen offset:0 lds
MA 146,124,54
WL 4
MA 86,112,58
RT 70,32,57344
MA 90,116,58
RT 72,32,58368
MA 150,120,58
buffer_load_dwordx4 v19, s[44:47], s60 offen offset:1024 lds
MA 154,124,58
s_add_u32 s60, s60, 229376
WL 4
MA 94,112,62
RT 74,33,57344
MA 98,116,62
RT 76,33,58368
MA 158,120,62
buffer_load_dwordx4 a[48:51], v16, s[40:43], s63 offen offset:0 sc1
MA 162,124,62
WL 4
MA 102,112,66
RT 54,34,57344
MA 106,116,66
RT 56,34,58368
MA 166,120,66
buffer_load_dwordx4 a[52:55], v16, s[40:43], s63 offen offset:1024 sc1
MA 170,124,66
s_add_u32 s63, s63, 4096
WL 4
MA 110,112,70
RT 58,35,57344
MA 114,116,70
RT 60,35,58368
MA 174,120,70
buffer_load_dwordx4 a[56:59], v17, s[40:43], s86 offen offset:0 sc1
MA 178,124,70
WL 4
MA 118,112,74
s_waitcnt vmcnt(29)
s_barrier
RT 62,36,0
MA 122,116,74
RT 64,36,1024
MA 182,120,74
buffer_load_dwordx4 a[60:63], v17, s[40:43], s86 offen offset:1024 sc1
MA 186,124,74
s_add_u32 s86, s86, 4096
WL 4
MA 126,112,54
RT 66,37,0
MA 130,116,54
RT 68,37,1024
s_add_u32 m0, s52, 24576
MA 190,120,54
buffer_load_dwordx4 v18, s[44:47], s60 offen offset:0 lds
MA 194,124,54
WL 4
MA 134,112,58
RT 70,38,0
MA 138,116,58
RT 72,38,1024
MA 198,120,58
buffer_load_dwordx4 v19, s[44:47], s60 offen offset:1024 lds
MA 202,124,58
s_add_u32 s60, s60, 229376
WL 4
MA 78,128,62
RT 74,39,0
MA 82,132,62
RT 76,39,1024
MA 142,136,62
buffer_load_dwordx4 a[64:67], v16, s[40:43], s63 offen offset:0 sc1
MA 146,140,62
WL 4
MA 86,128,66
RT 54,40,0
MA 90,132,66
RT 56,40,1024
MA 150,136,66
buffer_load_dwordx4 a[68:71], v16, s[40:43], s63 offen offset:1024 sc1
MA 154,140,66
s_add_u32 s63, s63, 4096
WL 4
MA 94,128,70
RT 58,41,0
MA 98,132,70
RT 60,41,1024
MA 158,136,70
buffer_load_dwordx4 a[72:75], v17, s[40:43], s86 offen offset:0 sc1
MA 162,140,70
WL 4
MA 102,128,74
RT 62,42,0
MA 106,132,74
RT 64,42,1024
MA 166,136,74
buffer_load_dwordx4 a[76:79], v17, s[40:43], s86 offen offset:1024 sc1
MA 170,140,74
s_add_u32 s86, s86, 4096
WL 4
MA 110,128,54
RT 66,43,0
MA 114,132,54
RT 68,43,1024
MA 174,136,54
MA 178,140,54
WL 4
MA 118,128,58
RT 70,36,8192
MA 122,132,58
RT 72,36,9216
s_add_u32 m0, s52, 32768
MA 182,136,58
buffer_load_dwordx4 v18, s[44:47], s60 offen offset:0 lds
MA 186,140,58
WL 4
MA 126,128,62
RT 74,37,8192
MA 130,132,62
RT 76,37,9216
MA 190,136,62
buffer_load_dwordx4 v19, s[44:47], s60 offen offset:1024 lds
MA 194,140,62
s_add_u32 s60, s60, 229376
WL 4
MA 134,128,66
RT 54,38,8192
MA 138,132,66
RT 56,38,9216
MA 198,136,66
MA 202,140,66
s_waitcnt vmcnt(32) lgkmcnt(4)
MA 78,144,70
RT 58,39,8192
MA 82,148,70
RT 60,39,9216
MA 142,152,70
buffer_load_dwordx4 a[80:83], v16, s[40:43], s63 offen offset:0 sc1
MA 146,156,70
WL 4
MA 86,144,74
RT 62,40,8192
MA 90,148,74
RT 64,40,9216
MA 150,152,74
buffer_load_dwordx4 a[84:87], v16, s[40:43], s63 offen offset:1024 sc1
MA 154,156,74
s_add_u32 s63, s63, 4096
WL 4
MA 94,144,54
RT 66,41,8192
MA 98,148,54
RT 68,41,9216
MA 158,152,54
buffer_load_dwordx4 a[88:91], v17, s[40:43], s86 offen offset:0 sc1
MA 162,156,54
WL 4
MA 102,144,58
RT 70,42,8192
MA 106,148,58
RT 72,42,9216
MA 166,152,58
buffer_load_dwordx4 a[92:95], v17, s[40:43], s86 offen offset:1024 sc1
MA 170,156,58
s_add_u32 s86, s86, 4096
WL 4
MA 110,144,62
RT 74,43,8192
MA 114,148,62
RT 76,43,9216
MA 174,152,62
MA 178,156,62
WL 4
MA 118,144,66
s_waitcnt vmcnt(34)
s_barrier
RT 54,36,16384
MA 122,148,66
RT 56,36,17408
s_add_u32 m0, s52, 40960
MA 182,152,66
buffer_load_dwordx4 v18, s[44:47], s60 offen offset:0 lds
MA 186,156,66
WL 4
MA 126,144,70
RT 58,37,16384
MA 130,148,70
RT 60,37,17408
MA 190,152,70
buffer_load_dwordx4 v19, s[44:47], s60 offen offset:1024 lds
MA 194,156,70
s_add_u32 s60, s60, 229376
WL 4
MA 134,144,74
RT 62,38,16384
MA 138,148,74
RT 64,38,17408
MA 198,152,74
MA 202,156,74
s_waitcnt vmcnt(32) lgkmcnt(4)
MA 78,160,54
RT 66,39,16384
MA 82,164,54
RT 68,39,17408
MA 142,168,54
buffer_load_dwordx4 a[96:99], v16, s[40:43], s63 offen offset:0 sc1
MA 146,172,54
WL 4
MA 86,160,58
RT 70,40,16384
MA 90,164,58
RT 72,40,17408
MA 150,168,58
buffer_load_dwordx4 a[100:103], v16, s[40:43], s63 offen offset:1024 sc1
MA 154,172,58
s_add_u32 s63, s63, 4096
WL 4
MA 94,160,62
RT 74,41,16384
MA 98,164,62
RT 76,41,17408
MA 158,168,62
buffer_load_dwordx4 a[104:107], v17, s[40:43], s86 offen offset:0 sc1
MA 162,172,62
WL 4
MA 102,160,66
RT 54,42,16384
MA 106,164,66
RT 56,42,17408
MA 166,168,66
buffer_load_dwordx4 a[108:111], v17, s[40:43], s86 offen offset:1024 sc1
MA 170,172,66
s_add_u32 s86, s86, 4096
WL 4
MA 110,160,70
RT 58,43,16384
MA 114,164,70
RT 60,43,17408
MA 174,168,70
MA 178,172,70
WL 4
MA 118,160,74
RT 62,36,24576
MA 122,164,74
RT 64,36,25600
s_add_u32 m0, s52, 49152
MA 182,168,74
buffer_load_dwordx4 v18, s[44:47], s60 offen offset:0 lds
MA 186,172,74
WL 4
MA 126,160,54
RT 66,37,24576
MA 130,164,54
RT 68,37,25600
MA 190,168,54
buffer_load_dwordx4 v19, s[44:47], s60 offen offset:1024 lds
MA 194,172,54
s_add_u32 s60, s60, 229376
WL 4
MA 134,160,58
RT 70,38,24576
MA 138,164,58
RT 72,38,25600
MA 198,168,58
MA 202,172,58
s_waitcnt vmcnt(32) lgkmcnt(4)
)"
R"(MA 78,176,62
RT 74,39,24576
MA 82,180,62
RT 76,39,25600
MA 142,184,62
buffer_load_dwordx4 a[112:115], v16, s[40:43], s63 offen offset:0 sc1
MA 146,188,62
WL 4
MA 86,176,66
RT 54,40,24576
MA 90,180,66
RT 56,40,25600
MA 150,184,66
buffer_load_dwordx4 a[116:119], v16, s[40:43], s63 offen offset:1024 sc1
MA 154,188,66
s_add_u32 s63, s63, 4096
WL 4
MA 94,176,70
RT 58,41,24576
MA 98,180,70
RT 60,41,25600
MA 158,184,70
buffer_load_dwordx4 a[120:123], v17, s[40:43], s86 offen offset:0 sc1
MA 162,188,70
WL 4
MA 102,176,74
RT 62,42,24576
MA 106,180,74
RT 64,42,25600
MA 166,184,74
buffer_load_dwordx4 a[124:127], v17, s[40:43], s86 offen offset:1024 sc1
MA 170,188,74
s_add_u32 s86, s86, 4096
WL 4
MA 110,176,54
RT 66,43,24576
MA 114,180,54
RT 68,43,25600
MA 174,184,54
MA 178,188,54
WL 4
MA 118,176,58
s_waitcnt vmcnt(34)
s_barrier
RT 70,20,0
MA 122,180,58
RT 72,20,1024
s_add_u32 m0, s52, 57344
MA 182,184,58
buffer_load_dwordx4 v18, s[44:47], s60 offen offset:0 lds
MA 186,188,58
WL 4
MA 126,176,62
RT 74,21,0
MA 130,180,62
RT 76,21,1024
MA 190,184,62
buffer_load_dwordx4 v19, s[44:47], s60 offen offset:1024 lds
MA 194,188,62
s_add_u32 s60, s60, 229376
WL 4
MA 134,176,66
RT 54,22,0
MA 138,180,66
RT 56,22,1024
MA 198,184,66
MA 202,188,66
s_waitcnt vmcnt(32) lgkmcnt(4)
MA 78,32,70
RT 58,23,0
MA 82,36,70
RT 60,23,1024
MA 142,40,70
buffer_load_dword v50, v49, s[44:47], s58 offen
MA 146,44,70
s_add_u32 s58, s58, 3670016
s_cmp_ge_u32 s58, s61
s_cselect_b32 s58, 0x40000000, s58
WL 4
MA 86,32,74
RT 62,24,0
MA 90,36,74
RT 64,24,1024
MA 150,40,74
MA 154,44,74
WL 4
MA 94,32,54
RT 66,25,0
MA 98,36,54
RT 68,25,1024
MA 158,40,54
MA 162,44,54
WL 4
MA 102,32,58
RT 70,26,0
MA 106,36,58
RT 72,26,1024
MA 166,40,58
MA 170,44,58
WL 4
MA 110,32,62
RT 74,27,0
MA 114,36,62
RT 76,27,1024
MA 174,40,62
MA 178,44,62
WL 4
MA 118,32,66
RT 54,20,8192
MA 122,36,66
RT 56,20,9216
MA 182,40,66
MA 186,44,66
WL 4
MA 126,32,70
RT 58,21,8192
MA 130,36,70
RT 60,21,9216
MA 190,40,70
MA 194,44,70
WL 4
MA 134,32,74
RT 62,22,8192
MA 138,36,74
RT 64,22,9216
MA 198,40,74
MA 202,44,74
s_waitcnt vmcnt(27) lgkmcnt(4)
MA 78,48,54
RT 66,23,8192
MA 82,52,54
RT 68,23,9216
MA 142,56,54
MA 146,60,54
WL 4
MA 86,48,58
RT 70,24,8192
MA 90,52,58
RT 72,24,9216
MA 150,56,58
MA 154,60,58
WL 4
MA 94,48,62
RT 74,25,8192
MA 98,52,62
RT 76,25,9216
MA 158,56,62
MA 162,60,62
WL 4
MA 102,48,66
RT 54,26,8192
MA 106,52,66
RT 56,26,9216
MA 166,56,66
MA 170,60,66
WL 4
MA 110,48,70
RT 58,27,8192
MA 114,52,70
RT 60,27,9216
MA 174,56,70
MA 178,60,70
WL 4
MA 118,48,74
s_waitcnt vmcnt(25)
s_barrier
RT 62,20,16384
MA 122,52,74
RT 64,20,17408
MA 182,56,74
MA 186,60,74
WL 4
MA 126,48,54
RT 66,21,16384
MA 130,52,54
RT 68,21,17408
MA 190,56,54
MA 194,60,54
WL 4
MA 134,48,58
RT 70,22,16384
MA 138,52,58
RT 72,22,17408
MA 198,56,58
MA 202,60,58
s_waitcnt vmcnt(21) lgkmcnt(4)
MA 78,64,62
RT 74,23,16384
MA 82,68,62
RT 76,23,17408
MA 142,72,62
buffer_load_dwordx4 a[128:131], v16, s[40:43], s63 offen offset:0 sc1
MA 146,76,62
WL 4
MA 86,64,66
RT 54,24,16384
MA 90,68,66
RT 56,24,17408
MA 150,72,66
buffer_load_dwordx4 a[132:135], v16, s[40:43], s63 offen offset:1024 sc1
MA 154,76,66
s_add_u32 s63, s63, 4096
WL 4
MA 94,64,70
RT 58,25,16384
MA 98,68,70
RT 60,25,17408
MA 158,72,70
buffer_load_dwordx4 a[136:139], v17, s[40:43], s86 offen offset:0 sc1
MA 162,76,70
WL 4
MA 102,64,74
RT 62,26,16384
MA 106,68,74
RT 64,26,17408
MA 166,72,74
buffer_load_dwordx4 a[140:143], v17, s[40:43], s86 offen offset:1024 sc1
MA 170,76,74
s_add_u32 s86, s86, 4096
WL 4
MA 110,64,54
RT 66,27,16384
MA 114,68,54
RT 68,27,17408
MA 174,72,54
buffer_load_dwordx4 a[144:147], v16, s[40:43], s63 offen offset:0 sc1
MA 178,76,54
WL 4
MA 118,64,58
RT 70,20,24576
MA 122,68,58
RT 72,20,25600
MA 182,72,58
buffer_load_dwordx4 a[148:151], v16, s[40:43], s63 offen offset:1024 sc1
MA 186,76,58
s_add_u32 s63, s63, 4096
WL 4
MA 126,64,62
RT 74,21,24576
MA 130,68,62
RT 76,21,25600
MA 190,72,62
buffer_load_dwordx4 a[152:155], v17, s[40:43], s86 offen offset:0 sc1
MA 194,76,62
WL 4
MA 134,64,66
RT 54,22,24576
MA 138,68,66
RT 56,22,25600
MA 198,72,66
buffer_load_dwordx4 a[156:159], v17, s[40:43], s86 offen offset:1024 sc1
MA 202,76,66
s_add_u32 s86, s86, 4096
s_waitcnt vmcnt(23) lgkmcnt(4)
MA 78,80,70
RT 58,23,24576
MA 82,84,70
RT 60,23,25600
s_add_u32 m0, s52, 65536
MA 142,88,70
buffer_load_dwordx4 v18, s[44:47], s60 offen offset:0 lds
MA 146,92,70
WL 4
MA 86,80,74
RT 62,24,24576
MA 90,84,74
RT 64,24,25600
MA 150,88,74
buffer_load_dwordx4 v19, s[44:47], s60 offen offset:1024 lds
MA 154,92,74
s_add_u32 s60, s60, 229376
WL 4
MA 94,80,54
RT 66,25,24576
MA 98,84,54
RT 68,25,25600
MA 158,88,54
buffer_load_dwordx4 a[160:163], v16, s[40:43], s63 offen offset:0 sc1
MA 162,92,54
WL 4
MA 102,80,58
RT 70,26,24576
MA 106,84,58
RT 72,26,25600
MA 166,88,58
buffer_load_dwordx4 a[164:167], v16, s[40:43], s63 offen offset:1024 sc1
MA 170,92,58
s_add_u32 s63, s63, 4096
WL 4
MA 110,80,62
RT 74,27,24576
MA 114,84,62
RT 76,27,25600
MA 174,88,62
buffer_load_dwordx4 a[168:171], v17, s[40:43], s86 offen offset:0 sc1
MA 178,92,62
WL 4
MA 118,80,66
s_waitcnt vmcnt(26)
s_barrier
RT 54,20,32768
MA 122,84,66
RT 56,20,33792
MA 182,88,66
buffer_load_dwordx4 a[172:175], v17, s[40:43], s86 offen offset:1024 sc1
MA 186,92,66
s_add_u32 s86, s86, 4096
WL 4
MA 126,80,70
RT 58,21,32768
MA 130,84,70
RT 60,21,33792
s_add_u32 m0, s52, 73728
MA 190,88,70
buffer_load_dwordx4 v18, s[44:47], s60 offen offset:0 lds
MA 194,92,70
WL 4
MA 134,80,74
RT 62,22,32768
MA 138,84,74
RT 64,22,33792
MA 198,88,74
buffer_load_dwordx4 v19, s[44:47], s60 offen offset:1024 lds
MA 202,92,74
s_add_u32 s60, s60, 229376
s_waitcnt vmcnt(25) lgkmcnt(4)
MA 78,96,54
RT 66,23,32768
MA 82,100,54
RT 68,23,33792
s_add_u32 m0, s52, 81920
MA 142,104,54
buffer_load_dwordx4 v18, s[44:47], s60 offen offset:0 lds
MA 146,108,54
WL 4
MA 86,96,58
RT 70,24,32768
MA 90,100,58
RT 72,24,33792
MA 150,104,58
buffer_load_dwordx4 v19, s[44:47], s60 offen offset:1024 lds
MA 154,108,58
s_add_u32 s60, s60, 229376
WL 4
MA 94,96,62
RT 74,25,32768
MA 98,100,62
RT 76,25,33792
MA 158,104,62
buffer_load_dwordx4 a[176:179], v16, s[40:43], s63 offen offset:0 sc1
MA 162,108,62
WL 4
MA 102,96,66
RT 54,26,32768
MA 106,100,66
RT 56,26,33792
MA 166,104,66
buffer_load_dwordx4 a[180:183], v16, s[40:43], s63 offen offset:1024 sc1
MA 170,108,66
s_add_u32 s63, s63, 4096
WL 4
MA 110,96,70
RT 58,27,32768
MA 114,100,70
RT 60,27,33792
MA 174,104,70
buffer_load_dwordx4 a[184:187], v17, s[40:43], s86 offen offset:0 sc1
MA 178,108,70
WL 4
MA 118,96,74
RT 62,20,40960
MA 122,100,74
RT 64,20,41984
MA 182,104,74
buffer_load_dwordx4 a[188:191], v17, s[40:43], s86 offen offset:1024 sc1
MA 186,108,74
s_add_u32 s86, s86, 4096
WL 4
MA 126,96,54
RT 66,21,40960
MA 130,100,54
RT 68,21,41984
s_add_u32 m0, s52, 90112
MA 190,104,54
buffer_load_dwordx4 v18, s[44:47], s60 offen offset:0 lds
MA 194,108,54
WL 4
MA 134,96,58
RT 70,22,40960
MA 138,100,58
RT 72,22,41984
MA 198,104,58
buffer_load_dwordx4 v19, s[44:47], s60 offen offset:1024 lds
MA 202,108,58
s_add_u32 s60, s60, 229376
s_waitcnt vmcnt(27) lgkmcnt(4)
MA 78,112,62
RT 74,23,40960
MA 82,116,62
RT 76,23,41984
)"
R"(MA 142,120,62
buffer_load_dwordx4 a[32:35], v16, s[40:43], s63 offen offset:0 sc1
MA 146,124,62
WL 4
MA 86,112,66
RT 54,24,40960
MA 90,116,66
RT 56,24,41984
MA 150,120,66
buffer_load_dwordx4 a[36:39], v16, s[40:43], s63 offen offset:1024 sc1
MA 154,124,66
s_add_u32 s63, s63, 4096
WL 4
MA 94,112,70
RT 58,25,40960
MA 98,116,70
RT 60,25,41984
MA 158,120,70
buffer_load_dwordx4 a[40:43], v17, s[40:43], s86 offen offset:0 sc1
MA 162,124,70
WL 4
MA 102,112,74
RT 62,26,40960
MA 106,116,74
RT 64,26,41984
MA 166,120,74
buffer_load_dwordx4 a[44:47], v17, s[40:43], s86 offen offset:1024 sc1
MA 170,124,74
s_add_u32 s86, s86, 4096
WL 4
MA 110,112,54
RT 66,27,40960
MA 114,116,54
RT 68,27,41984
s_add_u32 m0, s52, 98304
MA 174,120,54
buffer_load_dwordx4 v18, s[44:47], s60 offen offset:0 lds
MA 178,124,54
WL 4
MA 118,112,58
s_waitcnt vmcnt(30)
s_barrier
RT 70,20,49152
MA 122,116,58
RT 72,20,50176
MA 182,120,58
buffer_load_dwordx4 v19, s[44:47], s60 offen offset:1024 lds
MA 186,124,58
s_add_u32 s60, s60, 229376
WL 4
MA 126,112,62
RT 74,21,49152
MA 130,116,62
RT 76,21,50176
MA 190,120,62
buffer_load_dwordx4 a[48:51], v16, s[40:43], s63 offen offset:0 sc1
MA 194,124,62
WL 4
MA 134,112,66
RT 54,22,49152
MA 138,116,66
RT 56,22,50176
MA 198,120,66
buffer_load_dwordx4 a[52:55], v16, s[40:43], s63 offen offset:1024 sc1
MA 202,124,66
s_add_u32 s63, s63, 4096
s_waitcnt vmcnt(28) lgkmcnt(4)
MA 78,128,70
RT 58,23,49152
MA 82,132,70
RT 60,23,50176
MA 142,136,70
buffer_load_dwordx4 a[56:59], v17, s[40:43], s86 offen offset:0 sc1
MA 146,140,70
WL 4
MA 86,128,74
RT 62,24,49152
MA 90,132,74
RT 64,24,50176
MA 150,136,74
buffer_load_dwordx4 a[60:63], v17, s[40:43], s86 offen offset:1024 sc1
MA 154,140,74
s_add_u32 s86, s86, 4096
WL 4
MA 94,128,54
RT 66,25,49152
MA 98,132,54
RT 68,25,50176
s_add_u32 m0, s52, 106496
MA 158,136,54
buffer_load_dwordx4 v18, s[44:47], s60 offen offset:0 lds
MA 162,140,54
WL 4
MA 102,128,58
RT 70,26,49152
MA 106,132,58
RT 72,26,50176
MA 166,136,58
buffer_load_dwordx4 v19, s[44:47], s60 offen offset:1024 lds
MA 170,140,58
s_add_u32 s60, s60, 229376
WL 4
MA 110,128,62
RT 74,27,49152
MA 114,132,62
RT 76,27,50176
MA 174,136,62
buffer_load_dwordx4 a[64:67], v16, s[40:43], s63 offen offset:0 sc1
MA 178,140,62
WL 4
MA 118,128,66
RT 54,20,57344
MA 122,132,66
RT 56,20,58368
MA 182,136,66
buffer_load_dwordx4 a[68:71], v16, s[40:43], s63 offen offset:1024 sc1
MA 186,140,66
s_add_u32 s63, s63, 4096
WL 4
MA 126,128,70
RT 58,21,57344
MA 130,132,70
RT 60,21,58368
MA 190,136,70
buffer_load_dwordx4 a[72:75], v17, s[40:43], s86 offen offset:0 sc1
MA 194,140,70
WL 4
MA 134,128,74
RT 62,22,57344
MA 138,132,74
RT 64,22,58368
MA 198,136,74
buffer_load_dwordx4 a[76:79], v17, s[40:43], s86 offen offset:1024 sc1
MA 202,140,74
s_add_u32 s86, s86, 4096
s_waitcnt vmcnt(32) lgkmcnt(4)
MA 78,144,54
RT 66,23,57344
MA 82,148,54
RT 68,23,58368
s_add_u32 m0, s52, 114688
MA 142,152,54
buffer_load_dwordx4 v18, s[44:47], s60 offen offset:0 lds
MA 146,156,54
WL 4
MA 86,144,58
RT 70,24,57344
MA 90,148,58
RT 72,24,58368
MA 150,152,58
buffer_load_dwordx4 v19, s[44:47], s60 offen offset:1024 lds
MA 154,156,58
s_add_u32 s60, s60, 229376
WL 4
MA 94,144,62
RT 74,25,57344
MA 98,148,62
RT 76,25,58368
MA 158,152,62
buffer_load_dwordx4 a[80:83], v16, s[40:43], s63 offen offset:0 sc1
MA 162,156,62
WL 4
MA 102,144,66
RT 54,26,57344
MA 106,148,66
RT 56,26,58368
MA 166,152,66
buffer_load_dwordx4 a[84:87], v16, s[40:43], s63 offen offset:1024 sc1
MA 170,156,66
s_add_u32 s63, s63, 4096
WL 4
MA 110,144,70
RT 58,27,57344
MA 114,148,70
RT 60,27,58368
MA 174,152,70
buffer_load_dwordx4 a[88:91], v17, s[40:43], s86 offen offset:0 sc1
MA 178,156,70
WL 4
MA 118,144,74
s_waitcnt vmcnt(29)
s_barrier
RT 62,28,0
MA 122,148,74
RT 64,28,1024
MA 182,152,74
buffer_load_dwordx4 a[92:95], v17, s[40:43], s86 offen offset:1024 sc1
MA 186,156,74
s_add_u32 s86, s86, 4096
WL 4
MA 126,144,54
RT 66,29,0
MA 130,148,54
RT 68,29,1024
s_add_u32 m0, s52, 122880
MA 190,152,54
buffer_load_dwordx4 v18, s[44:47], s60 offen offset:0 lds
MA 194,156,54
WL 4
MA 134,144,58
RT 70,30,0
MA 138,148,58
RT 72,30,1024
MA 198,152,58
buffer_load_dwordx4 v19, s[44:47], s60 offen offset:1024 lds
MA 202,156,58
s_add_u32 s60, s60, 229376
WL 4
MA 78,160,62
RT 74,31,0
MA 82,164,62
RT 76,31,1024
MA 142,168,62
buffer_load_dwordx4 a[96:99], v16, s[40:43], s63 offen offset:0 sc1
MA 146,172,62
WL 4
MA 86,160,66
RT 54,32,0
MA 90,164,66
RT 56,32,1024
MA 150,168,66
buffer_load_dwordx4 a[100:103], v16, s[40:43], s63 offen offset:1024 sc1
MA 154,172,66
s_add_u32 s63, s63, 4096
WL 4
MA 94,160,70
RT 58,33,0
MA 98,164,70
RT 60,33,1024
MA 158,168,70
buffer_load_dwordx4 a[104:107], v17, s[40:43], s86 offen offset:0 sc1
MA 162,172,70
WL 4
MA 102,160,74
RT 62,34,0
MA 106,164,74
RT 64,34,1024
MA 166,168,74
buffer_load_dwordx4 a[108:111], v17, s[40:43], s86 offen offset:1024 sc1
MA 170,172,74
s_add_u32 s86, s86, 4096
WL 4
MA 110,160,54
RT 66,35,0
MA 114,164,54
RT 68,35,1024
MA 174,168,54
MA 178,172,54
WL 4
MA 118,160,58
RT 70,28,8192
MA 122,164,58
RT 72,28,9216
s_add_u32 m0, s52, 131072
MA 182,168,58
buffer_load_dwordx4 v18, s[44:47], s60 offen offset:0 lds
MA 186,172,58
WL 4
MA 126,160,62
RT 74,29,8192
MA 130,164,62
RT 76,29,9216
MA 190,168,62
buffer_load_dwordx4 v19, s[44:47], s60 offen offset:1024 lds
MA 194,172,62
s_add_u32 s60, s60, 229376
WL 4
MA 134,160,66
RT 54,30,8192
MA 138,164,66
RT 56,30,9216
MA 198,168,66
MA 202,172,66
s_waitcnt vmcnt(32) lgkmcnt(4)
MA 78,176,70
RT 58,31,8192
MA 82,180,70
RT 60,31,9216
MA 142,184,70
buffer_load_dwordx4 a[112:115], v16, s[40:43], s63 offen offset:0 sc1
MA 146,188,70
WL 4
MA 86,176,74
RT 62,32,8192
MA 90,180,74
RT 64,32,9216
MA 150,184,74
buffer_load_dwordx4 a[116:119], v16, s[40:43], s63 offen offset:1024 sc1
MA 154,188,74
s_add_u32 s63, s63, 4096
WL 4
MA 94,176,54
RT 66,33,8192
MA 98,180,54
RT 68,33,9216
MA 158,184,54
buffer_load_dwordx4 a[120:123], v17, s[40:43], s86 offen offset:0 sc1
MA 162,188,54
WL 4
MA 102,176,58
RT 70,34,8192
MA 106,180,58
RT 72,34,9216
MA 166,184,58
buffer_load_dwordx4 a[124:127], v17, s[40:43], s86 offen offset:1024 sc1
MA 170,188,58
s_add_u32 s86, s86, 4096
WL 4
MA 110,176,62
RT 74,35,8192
MA 114,180,62
RT 76,35,9216
s_add_u32 m0, s52, 139264
MA 174,184,62
buffer_load_dwordx4 v18, s[44:47], s60 offen offset:0 lds
MA 178,188,62
WL 4
MA 118,176,66
s_waitcnt vmcnt(35)
s_barrier
RT 54,28,16384
MA 122,180,66
RT 56,28,17408
MA 182,184,66
buffer_load_dwordx4 v19, s[44:47], s60 offen offset:1024 lds
MA 186,188,66
s_add_u32 s60, s60, 229376
WL 4
MA 126,176,70
RT 58,29,16384
MA 130,180,70
RT 60,29,17408
MA 190,184,70
MA 194,188,70
WL 4
MA 134,176,74
RT 62,30,16384
MA 138,180,74
RT 64,30,17408
s_add_u32 m0, s52, 147456
MA 198,184,74
buffer_load_dwordx4 v18, s[44:47], s60 offen offset:0 lds
MA 202,188,74
s_waitcnt vmcnt(33) lgkmcnt(4)
MA 78,32,54
RT 66,31,16384
MA 82,36,54
RT 68,31,17408
MA 142,40,54
buffer_load_dwordx4 v19, s[44:47], s60 offen offset:1024 lds
MA 146,44,54
s_add_u32 s60, s60, 229376
WL 4
MA 86,32,58
RT 70,32,16384
MA 90,36,58
RT 72,32,17408
MA 150,40,58
buffer_load_dwordx4 a[128:131], v17, s[40:43], s86 offen offset:0 sc1
MA 154,44,58
WL 4
MA 94,32,62
RT 74,33,16384
MA 98,36,62
RT 76,33,17408
MA 158,40,62
buffer_load_dwordx4 a[132:135], v17, s[40:43], s86 offen offset:1024 sc1
MA 162,44,62
s_add_u32 s86, s86, 4096
WL 4
MA 102,32,66
RT 54,34,16384
MA 106,36,66
RT 56,34,17408
s_add_u32 m0, s52, 155648
MA 166,40,66
buffer_load_dwordx4 v18, s[44:47], s60 offen offset:0 lds
MA 170,44,66
WL 4
MA 110,32,70
RT 58,35,16384
MA 114,36,70
RT 60,35,17408
MA 174,40,70
buffer_load_dwordx4 v19, s[44:47], s60 offen offset:1024 lds
MA 178,44,70
s_add_u32 s60, s60, 229376
WL 4
MA 118,32,74
RT 62,28,24576
MA 122,36,74
RT 64,28,25600
MA 182,40,74
buffer_load_dwordx4 a[136:139], v17, s[40:43], s86 offen offset:0 sc1
MA 186,44,74
WL 4
MA 126,32,54
RT 66,29,24576
MA 130,36,54
RT 68,29,25600
)"
R"(MA 190,40,54
buffer_load_dwordx4 a[140:143], v17, s[40:43], s86 offen offset:1024 sc1
MA 194,44,54
s_add_u32 s86, s86, 4096
WL 4
MA 134,32,58
RT 70,30,24576
MA 138,36,58
RT 72,30,25600
s_add_u32 m0, s52, 0
MA 198,40,58
buffer_load_dwordx4 v18, s[44:47], s60 offen offset:0 lds
MA 202,44,58
s_waitcnt vmcnt(35) lgkmcnt(4)
MA 78,48,62
RT 74,31,24576
MA 82,52,62
RT 76,31,25600
MA 142,56,62
buffer_load_dwordx4 v19, s[44:47], s60 offen offset:1024 lds
MA 146,60,62
s_add_u32 s60, s60, 229376
WL 4
MA 86,48,66
RT 54,32,24576
MA 90,52,66
RT 56,32,25600
MA 150,56,66
buffer_load_dwordx4 a[144:147], v17, s[40:43], s86 offen offset:0 sc1
MA 154,60,66
WL 4
MA 94,48,70
RT 58,33,24576
MA 98,52,70
RT 60,33,25600
MA 158,56,70
buffer_load_dwordx4 a[148:151], v17, s[40:43], s86 offen offset:1024 sc1
MA 162,60,70
s_add_u32 s86, s86, 4096
WL 4
MA 102,48,74
RT 62,34,24576
MA 106,52,74
RT 64,34,25600
s_add_u32 m0, s52, 8192
MA 166,56,74
buffer_load_dwordx4 v18, s[44:47], s60 offen offset:0 lds
MA 170,60,74
WL 4
MA 110,48,54
RT 66,35,24576
MA 114,52,54
RT 68,35,25600
MA 174,56,54
buffer_load_dwordx4 v19, s[44:47], s60 offen offset:1024 lds
MA 178,60,54
s_add_u32 s60, s60, 229376
WL 4
MA 118,48,58
s_waitcnt vmcnt(38)
s_barrier
RT 70,28,32768
MA 122,52,58
RT 72,28,33792
MA 182,56,58
buffer_load_dwordx4 a[152:155], v17, s[40:43], s86 offen offset:0 sc1
MA 186,60,58
WL 4
MA 126,48,62
RT 74,29,32768
MA 130,52,62
RT 76,29,33792
MA 190,56,62
buffer_load_dwordx4 a[156:159], v17, s[40:43], s86 offen offset:1024 sc1
MA 194,60,62
s_add_u32 s86, s86, 4096
WL 4
MA 134,48,66
RT 54,30,32768
MA 138,52,66
RT 56,30,33792
s_add_u32 m0, s52, 16384
MA 198,56,66
buffer_load_dwordx4 v18, s[44:47], s60 offen offset:0 lds
MA 202,60,66
s_waitcnt vmcnt(37) lgkmcnt(4)
MA 78,64,70
RT 58,31,32768
MA 82,68,70
RT 60,31,33792
MA 142,72,70
buffer_load_dwordx4 v19, s[44:47], s60 offen offset:1024 lds
MA 146,76,70
s_add_u32 s60, s60, 229376
WL 4
MA 86,64,74
RT 62,32,32768
MA 90,68,74
RT 64,32,33792
MA 150,72,74
buffer_load_dwordx4 a[160:163], v17, s[40:43], s86 offen offset:0 sc1
MA 154,76,74
WL 4
MA 94,64,54
RT 66,33,32768
MA 98,68,54
RT 68,33,33792
MA 158,72,54
buffer_load_dwordx4 a[164:167], v17, s[40:43], s86 offen offset:1024 sc1
MA 162,76,54
s_add_u32 s86, s86, 4096
WL 4
MA 102,64,58
RT 70,34,32768
MA 106,68,58
RT 72,34,33792
s_add_u32 m0, s52, 24576
MA 166,72,58
buffer_load_dwordx4 v18, s[44:47], s60 offen offset:0 lds
MA 170,76,58
WL 4
MA 110,64,62
RT 74,35,32768
MA 114,68,62
RT 76,35,33792
MA 174,72,62
buffer_load_dwordx4 v19, s[44:47], s60 offen offset:1024 lds
MA 178,76,62
s_add_u32 s60, s60, 229376
WL 4
MA 118,64,66
RT 54,28,40960
MA 122,68,66
RT 56,28,41984
MA 182,72,66
buffer_load_dwordx4 a[168:171], v17, s[40:43], s86 offen offset:0 sc1
MA 186,76,66
WL 4
MA 126,64,70
RT 58,29,40960
MA 130,68,70
RT 60,29,41984
MA 190,72,70
buffer_load_dwordx4 a[172:175], v17, s[40:43], s86 offen offset:1024 sc1
MA 194,76,70
s_add_u32 s86, s86, 4096
WL 4
MA 134,64,74
RT 62,30,40960
MA 138,68,74
RT 64,30,41984
s_add_u32 m0, s52, 32768
MA 198,72,74
buffer_load_dwordx4 v18, s[44:47], s60 offen offset:0 lds
MA 202,76,74
s_waitcnt vmcnt(39) lgkmcnt(4)
MA 78,80,54
RT 66,31,40960
MA 82,84,54
RT 68,31,41984
MA 142,88,54
buffer_load_dwordx4 v19, s[44:47], s60 offen offset:1024 lds
MA 146,92,54
s_add_u32 s60, s60, 229376
WL 4
MA 86,80,58
RT 70,32,40960
MA 90,84,58
RT 72,32,41984
MA 150,88,58
buffer_load_dwordx4 a[176:179], v17, s[40:43], s86 offen offset:0 sc1
MA 154,92,58
WL 4
MA 94,80,62
RT 74,33,40960
MA 98,84,62
RT 76,33,41984
MA 158,88,62
buffer_load_dwordx4 a[180:183], v17, s[40:43], s86 offen offset:1024 sc1
MA 162,92,62
s_add_u32 s86, s86, 4096
WL 4
MA 102,80,66
RT 54,34,40960
MA 106,84,66
RT 56,34,41984
s_add_u32 m0, s52, 40960
MA 166,88,66
buffer_load_dwordx4 v18, s[44:47], s60 offen offset:0 lds
MA 170,92,66
WL 4
MA 110,80,70
RT 58,35,40960
MA 114,84,70
RT 60,35,41984
MA 174,88,70
buffer_load_dwordx4 v19, s[44:47], s60 offen offset:1024 lds
MA 178,92,70
s_add_u32 s60, s60, 229376
WL 4
MA 118,80,74
s_waitcnt vmcnt(42)
s_barrier
RT 62,28,49152
MA 122,84,74
RT 64,28,50176
MA 182,88,74
buffer_load_dwordx4 a[184:187], v17, s[40:43], s86 offen offset:0 sc1
MA 186,92,74
WL 4
MA 126,80,54
RT 66,29,49152
MA 130,84,54
RT 68,29,50176
MA 190,88,54
buffer_load_dwordx4 a[188:191], v17, s[40:43], s86 offen offset:1024 sc1
MA 194,92,54
s_add_u32 s86, s86, 4096
WL 4
MA 134,80,58
RT 70,30,49152
MA 138,84,58
RT 72,30,50176
s_add_u32 m0, s52, 49152
MA 198,88,58
buffer_load_dwordx4 v18, s[44:47], s60 offen offset:0 lds
MA 202,92,58
s_waitcnt vmcnt(41) lgkmcnt(4)
MA 78,96,62
RT 74,31,49152
MA 82,100,62
RT 76,31,50176
MA 142,104,62
buffer_load_dwordx4 v19, s[44:47], s60 offen offset:1024 lds
MA 146,108,62
s_add_u32 s60, s60, 229376
WL 4
MA 86,96,66
RT 54,32,49152
MA 90,100,66
RT 56,32,50176
MA 150,104,66
buffer_load_dwordx4 a[32:35], v17, s[40:43], s86 offen offset:0 sc1
MA 154,108,66
WL 4
MA 94,96,70
RT 58,33,49152
MA 98,100,70
RT 60,33,50176
MA 158,104,70
buffer_load_dwordx4 a[36:39], v17, s[40:43], s86 offen offset:1024 sc1
MA 162,108,70
s_add_u32 s86, s86, 4096
WL 4
MA 102,96,74
RT 62,34,49152
MA 106,100,74
RT 64,34,50176
s_add_u32 m0, s52, 57344
MA 166,104,74
buffer_load_dwordx4 v18, s[44:47], s60 offen offset:0 lds
MA 170,108,74
WL 4
MA 110,96,54
RT 66,35,49152
MA 114,100,54
RT 68,35,50176
MA 174,104,54
buffer_load_dwordx4 v19, s[44:47], s60 offen offset:1024 lds
MA 178,108,54
s_add_u32 s60, s60, 229376
WL 4
MA 118,96,58
RT 70,28,57344
MA 122,100,58
RT 72,28,58368
MA 182,104,58
buffer_load_dwordx4 a[40:43], v17, s[40:43], s86 offen offset:0 sc1
MA 186,108,58
WL 4
MA 126,96,62
RT 74,29,57344
MA 130,100,62
RT 76,29,58368
MA 190,104,62
buffer_load_dwordx4 a[44:47], v17, s[40:43], s86 offen offset:1024 sc1
MA 194,108,62
s_add_u32 s86, s86, 4096
WL 4
MA 134,96,66
RT 54,30,57344
MA 138,100,66
RT 56,30,58368
s_add_u32 m0, s52, 65536
MA 198,104,66
buffer_load_dwordx4 v18, s[44:47], s60 offen offset:0 lds
MA 202,108,66
s_waitcnt vmcnt(43) lgkmcnt(4)
MA 78,112,70
RT 58,31,57344
MA 82,116,70
RT 60,31,58368
MA 142,120,70
buffer_load_dwordx4 v19, s[44:47], s60 offen offset:1024 lds
MA 146,124,70
s_add_u32 s60, s60, 229376
WL 4
MA 86,112,74
RT 62,32,57344
MA 90,116,74
RT 64,32,58368
MA 150,120,74
buffer_load_dwordx4 a[48:51], v17, s[40:43], s86 offen offset:0 sc1
MA 154,124,74
WL 4
MA 94,112,54
RT 66,33,57344
MA 98,116,54
RT 68,33,58368
MA 158,120,54
buffer_load_dwordx4 a[52:55], v17, s[40:43], s86 offen offset:1024 sc1
MA 162,124,54
s_add_u32 s86, s86, 4096
WL 4
MA 102,112,58
RT 70,34,57344
MA 106,116,58
RT 72,34,58368
s_add_u32 m0, s52, 73728
MA 166,120,58
buffer_load_dwordx4 v18, s[44:47], s60 offen offset:0 lds
MA 170,124,58
WL 4
MA 110,112,62
RT 74,35,57344
MA 114,116,62
RT 76,35,58368
MA 174,120,62
buffer_load_dwordx4 v19, s[44:47], s60 offen offset:1024 lds
MA 178,124,62
s_add_u32 s60, s60, 229376
WL 4
MA 118,112,66
s_waitcnt vmcnt(40)
s_barrier
RT 54,36,0
MA 122,116,66
RT 56,36,1024
MA 182,120,66
buffer_load_dwordx4 a[56:59], v17, s[40:43], s86 offen offset:0 sc1
MA 186,124,66
WL 4
MA 126,112,70
RT 58,37,0
MA 130,116,70
RT 60,37,1024
MA 190,120,70
buffer_load_dwordx4 a[60:63], v17, s[40:43], s86 offen offset:1024 sc1
MA 194,124,70
s_add_u32 s86, s86, 4096
WL 4
MA 134,112,74
RT 62,38,0
MA 138,116,74
RT 64,38,1024
s_add_u32 m0, s52, 81920
MA 198,120,74
buffer_load_dwordx4 v18, s[44:47], s60 offen offset:0 lds
MA 202,124,74
WL 4
MA 142,128,54
RT 66,39,0
MA 146,132,54
RT 68,39,1024
WL 4
MA 150,128,58
RT 70,40,0
MA 154,132,58
RT 72,40,1024
buffer_load_dwordx4 v19, s[44:47], s60 offen offset:1024 lds
WL 4
MA 158,128,62
RT 74,41,0
s_add_u32 s60, s60, 229376
MA 162,132,62
RT 76,41,1024
WL 4
MA 166,128,66
RT 54,42,0
MA 170,132,66
RT 56,42,1024
buffer_load_dwordx4 a[64:67], v17, s[40:43], s86 offen offset:0 sc1
WL 4
MA 174,128,70
RT 58,43,0
MA 178,132,70
RT 60,43,1024
WL 4
MA 182,128,74
RT 62,36,8192
)"
R"(MA 186,132,74
RT 64,36,9216
buffer_load_dwordx4 a[68:71], v17, s[40:43], s86 offen offset:1024 sc1
WL 4
MA 190,128,54
RT 66,37,8192
s_add_u32 s86, s86, 4096
MA 194,132,54
RT 68,37,9216
WL 4
MA 198,128,58
RT 70,38,8192
s_add_u32 m0, s52, 90112
MA 202,132,58
RT 72,38,9216
buffer_load_dwordx4 v18, s[44:47], s60 offen offset:0 lds
s_waitcnt vmcnt(45) lgkmcnt(4)
MA 142,136,62
RT 74,39,8192
MA 146,140,62
RT 76,39,9216
WL 4
MA 150,136,66
RT 54,40,8192
MA 154,140,66
RT 56,40,9216
buffer_load_dwordx4 v19, s[44:47], s60 offen offset:1024 lds
WL 4
MA 158,136,70
RT 58,41,8192
s_add_u32 s60, s60, 229376
MA 162,140,70
RT 60,41,9216
WL 4
MA 166,136,74
RT 62,42,8192
MA 170,140,74
RT 64,42,9216
buffer_load_dwordx4 a[72:75], v17, s[40:43], s86 offen offset:0 sc1
WL 4
MA 174,136,54
RT 66,43,8192
MA 178,140,54
RT 68,43,9216
WL 4
MA 182,136,58
RT 70,36,16384
MA 186,140,58
RT 72,36,17408
buffer_load_dwordx4 a[76:79], v17, s[40:43], s86 offen offset:1024 sc1
WL 4
MA 190,136,62
RT 74,37,16384
s_add_u32 s86, s86, 4096
MA 194,140,62
RT 76,37,17408
WL 4
MA 198,136,66
RT 54,38,16384
s_add_u32 m0, s52, 98304
MA 202,140,66
RT 56,38,17408
buffer_load_dwordx4 v18, s[44:47], s60 offen offset:0 lds
s_waitcnt vmcnt(45) lgkmcnt(4)
MA 142,144,70
RT 58,39,16384
MA 146,148,70
RT 60,39,17408
WL 4
MA 150,144,74
RT 62,40,16384
MA 154,148,74
RT 64,40,17408
buffer_load_dwordx4 v19, s[44:47], s60 offen offset:1024 lds
WL 4
MA 158,144,54
RT 66,41,16384
s_add_u32 s60, s60, 229376
MA 162,148,54
RT 68,41,17408
WL 4
MA 166,144,58
RT 70,42,16384
MA 170,148,58
RT 72,42,17408
buffer_load_dwordx4 a[80:83], v17, s[40:43], s86 offen offset:0 sc1
WL 4
MA 174,144,62
RT 74,43,16384
MA 178,148,62
RT 76,43,17408
WL 4
MA 182,144,66
RT 54,36,24576
MA 186,148,66
RT 56,36,25600
buffer_load_dwordx4 a[84:87], v17, s[40:43], s86 offen offset:1024 sc1
WL 4
MA 190,144,70
RT 58,37,24576
s_add_u32 s86, s86, 4096
MA 194,148,70
RT 60,37,25600
WL 4
MA 198,144,74
RT 62,38,24576
s_add_u32 m0, s52, 106496
MA 202,148,74
RT 64,38,25600
buffer_load_dwordx4 v18, s[44:47], s60 offen offset:0 lds
s_waitcnt vmcnt(45) lgkmcnt(4)
MA 142,152,54
RT 66,39,24576
MA 146,156,54
RT 68,39,25600
WL 4
MA 150,152,58
RT 70,40,24576
MA 154,156,58
RT 72,40,25600
buffer_load_dwordx4 v19, s[44:47], s60 offen offset:1024 lds
WL 4
MA 158,152,62
RT 74,41,24576
s_add_u32 s60, s60, 229376
MA 162,156,62
RT 76,41,25600
WL 4
MA 166,152,66
RT 54,42,24576
MA 170,156,66
RT 56,42,25600
buffer_load_dwordx4 a[88:91], v17, s[40:43], s86 offen offset:0 sc1
WL 4
MA 174,152,70
RT 58,43,24576
MA 178,156,70
RT 60,43,25600
WL 4
MA 182,152,74
s_waitcnt vmcnt(41)
s_barrier
RT 62,20,0
MA 186,156,74
RT 64,20,1024
buffer_load_dwordx4 a[92:95], v17, s[40:43], s86 offen offset:1024 sc1
WL 4
MA 190,152,54
RT 66,21,0
s_add_u32 s86, s86, 4096
MA 194,156,54
RT 68,21,1024
WL 4
MA 198,152,58
RT 70,22,0
s_add_u32 m0, s52, 114688
MA 202,156,58
RT 72,22,1024
buffer_load_dwordx4 v18, s[44:47], s60 offen offset:0 lds
WL 4
MA 142,160,62
RT 74,23,0
MA 146,164,62
RT 76,23,1024
WL 4
MA 150,160,66
RT 54,24,0
MA 154,164,66
RT 56,24,1024
buffer_load_dwordx4 v19, s[44:47], s60 offen offset:1024 lds
WL 4
MA 158,160,70
RT 58,25,0
s_add_u32 s60, s60, 229376
MA 162,164,70
RT 60,25,1024
WL 4
MA 166,160,74
RT 62,26,0
MA 170,164,74
RT 64,26,1024
buffer_load_dwordx4 a[96:99], v17, s[40:43], s86 offen offset:0 sc1
WL 4
MA 174,160,54
RT 66,27,0
MA 178,164,54
RT 68,27,1024
WL 4
MA 182,160,58
RT 70,20,8192
MA 186,164,58
RT 72,20,9216
buffer_load_dwordx4 a[100:103], v17, s[40:43], s86 offen offset:1024 sc1
WL 4
MA 190,160,62
RT 74,21,8192
s_add_u32 s86, s86, 4096
MA 194,164,62
RT 76,21,9216
WL 4
MA 198,160,66
RT 54,22,8192
s_add_u32 m0, s52, 122880
MA 202,164,66
RT 56,22,9216
buffer_load_dwordx4 v18, s[44:47], s60 offen offset:0 lds
s_waitcnt vmcnt(45) lgkmcnt(4)
MA 142,168,70
RT 58,23,8192
MA 146,172,70
RT 60,23,9216
WL 4
MA 150,168,74
RT 62,24,8192
MA 154,172,74
RT 64,24,9216
buffer_load_dwordx4 v19, s[44:47], s60 offen offset:1024 lds
WL 4
MA 158,168,54
RT 66,25,8192
s_add_u32 s60, s60, 229376
MA 162,172,54
RT 68,25,9216
WL 4
MA 166,168,58
RT 70,26,8192
MA 170,172,58
RT 72,26,9216
buffer_load_dwordx4 a[104:107], v17, s[40:43], s86 offen offset:0 sc1
WL 4
MA 174,168,62
RT 74,27,8192
MA 178,172,62
RT 76,27,9216
WL 4
MA 182,168,66
RT 54,20,16384
MA 186,172,66
RT 56,20,17408
buffer_load_dwordx4 a[108:111], v17, s[40:43], s86 offen offset:1024 sc1
WL 4
MA 190,168,70
RT 58,21,16384
s_add_u32 s86, s86, 4096
MA 194,172,70
RT 60,21,17408
WL 4
MA 198,168,74
RT 62,22,16384
MA 202,172,74
RT 64,22,17408
s_waitcnt vmcnt(44) lgkmcnt(4)
MA 142,176,54
RT 66,23,16384
MA 146,180,54
RT 68,23,17408
buffer_load_dwordx4 a[112:115], v17, s[40:43], s86 offen offset:0 sc1
WL 4
MA 150,176,58
RT 70,24,16384
MA 154,180,58
RT 72,24,17408
WL 4
MA 158,176,62
RT 74,25,16384
MA 162,180,62
RT 76,25,17408
buffer_load_dwordx4 a[116:119], v17, s[40:43], s86 offen offset:1024 sc1
WL 4
MA 166,176,66
RT 54,26,16384
s_add_u32 s86, s86, 4096
MA 170,180,66
RT 56,26,17408
WL 4
MA 174,176,70
RT 58,27,16384
MA 178,180,70
RT 60,27,17408
WL 4
MA 182,176,74
RT 62,20,24576
MA 186,180,74
RT 64,20,25600
WL 4
MA 190,176,54
RT 66,21,24576
MA 194,180,54
RT 68,21,25600
WL 4
MA 198,176,58
RT 70,22,24576
MA 202,180,58
RT 72,22,25600
s_waitcnt vmcnt(42) lgkmcnt(4)
MA 142,184,62
RT 74,23,24576
MA 146,188,62
RT 76,23,25600
buffer_load_dwordx4 a[120:123], v17, s[40:43], s86 offen offset:0 sc1
WL 4
MA 150,184,66
RT 54,24,24576
MA 154,188,66
RT 56,24,25600
WL 4
MA 158,184,70
RT 58,25,24576
MA 162,188,70
RT 60,25,25600
buffer_load_dwordx4 a[124:127], v17, s[40:43], s86 offen offset:1024 sc1
WL 4
MA 166,184,74
RT 62,26,24576
s_add_u32 s86, s86, 4096
MA 170,188,74
RT 64,26,25600
WL 4
MA 174,184,54
RT 66,27,24576
MA 178,188,54
RT 68,27,25600
WL 4
MA 182,184,58
s_waitcnt vmcnt(38)
s_barrier
RT 70,20,32768
MA 186,188,58
RT 72,20,33792
WL 4
MA 190,184,62
RT 74,21,32768
MA 194,188,62
RT 76,21,33792
WL 4
MA 198,184,66
RT 54,22,32768
MA 202,188,66
RT 56,22,33792
WL 4
MA 142,32,70
RT 58,23,32768
MA 146,36,70
RT 60,23,33792
buffer_load_dwordx4 a[128:131], v16, s[40:43], s63 offen offset:0 sc1
WL 4
MA 150,32,74
RT 62,24,32768
MA 154,36,74
RT 64,24,33792
WL 4
MA 158,32,54
RT 66,25,32768
MA 162,36,54
RT 68,25,33792
buffer_load_dwordx4 a[132:135], v16, s[40:43], s63 offen offset:1024 sc1
WL 4
MA 166,32,58
RT 70,26,32768
s_add_u32 s63, s63, 4096
MA 170,36,58
RT 72,26,33792
WL 4
MA 174,32,62
RT 74,27,32768
MA 178,36,62
RT 76,27,33792
WL 4
MA 182,32,66
RT 54,20,40960
MA 186,36,66
RT 56,20,41984
WL 4
MA 190,32,70
RT 58,21,40960
MA 194,36,70
RT 60,21,41984
WL 4
MA 198,32,74
RT 62,22,40960
MA 202,36,74
RT 64,22,41984
s_waitcnt vmcnt(38) lgkmcnt(4)
MA 142,40,54
RT 66,23,40960
MA 146,44,54
RT 68,23,41984
buffer_load_dwordx4 a[136:139], v16, s[40:43], s63 offen offset:0 sc1
WL 4
MA 150,40,58
RT 70,24,40960
MA 154,44,58
RT 72,24,41984
WL 4
MA 158,40,62
RT 74,25,40960
MA 162,44,62
RT 76,25,41984
buffer_load_dwordx4 a[140:143], v16, s[40:43], s63 offen offset:1024 sc1
WL 4
)"
R"(MA 166,40,66
RT 54,26,40960
s_add_u32 s63, s63, 4096
MA 170,44,66
RT 56,26,41984
WL 4
MA 174,40,70
RT 58,27,40960
MA 178,44,70
RT 60,27,41984
WL 4
MA 182,40,74
RT 62,20,49152
MA 186,44,74
RT 64,20,50176
WL 4
MA 190,40,54
RT 66,21,49152
MA 194,44,54
RT 68,21,50176
WL 4
MA 198,40,58
RT 70,22,49152
MA 202,44,58
RT 72,22,50176
s_waitcnt vmcnt(36) lgkmcnt(4)
MA 142,48,62
RT 74,23,49152
MA 146,52,62
RT 76,23,50176
buffer_load_dwordx4 a[144:147], v16, s[40:43], s63 offen offset:0 sc1
WL 4
MA 150,48,66
RT 54,24,49152
MA 154,52,66
RT 56,24,50176
WL 4
MA 158,48,70
RT 58,25,49152
MA 162,52,70
RT 60,25,50176
buffer_load_dwordx4 a[148:151], v16, s[40:43], s63 offen offset:1024 sc1
WL 4
MA 166,48,74
RT 62,26,49152
s_add_u32 s63, s63, 4096
MA 170,52,74
RT 64,26,50176
WL 4
MA 174,48,54
RT 66,27,49152
MA 178,52,54
RT 68,27,50176
WL 4
MA 182,48,58
RT 70,20,57344
MA 186,52,58
RT 72,20,58368
WL 4
MA 190,48,62
RT 74,21,57344
MA 194,52,62
RT 76,21,58368
WL 4
MA 198,48,66
RT 54,22,57344
MA 202,52,66
RT 56,22,58368
L_join_%=:
s_waitcnt vmcnt(34) lgkmcnt(4)
MA 142,56,70
RT 58,23,57344
MA 146,60,70
RT 60,23,58368
buffer_load_dwordx4 a[152:155], v16, s[40:43], s63 offen offset:0 sc1
WL 4
MA 150,56,74
RT 62,24,57344
MA 154,60,74
RT 64,24,58368
WL 4
MA 158,56,54
RT 66,25,57344
MA 162,60,54
RT 68,25,58368
buffer_load_dwordx4 a[156:159], v16, s[40:43], s63 offen offset:1024 sc1
WL 4
MA 166,56,58
RT 70,26,57344
s_add_u32 s63, s63, 4096
MA 170,60,58
RT 72,26,58368
WL 4
MA 174,56,62
RT 74,27,57344
MA 178,60,62
RT 76,27,58368
buffer_load_dwordx4 a[160:163], v16, s[40:43], s63 offen offset:0 sc1
WL 4
MA 182,56,66
s_waitcnt vmcnt(31)
s_barrier
RT 54,28,0
MA 186,60,66
RT 56,28,1024
WL 4
MA 190,56,70
RT 58,29,0
MA 194,60,70
RT 60,29,1024
buffer_load_dwordx4 a[164:167], v16, s[40:43], s63 offen offset:1024 sc1
WL 4
MA 198,56,74
RT 62,30,0
s_add_u32 s63, s63, 4096
MA 202,60,74
RT 64,30,1024
WL 4
MA 142,64,54
RT 66,31,0
MA 146,68,54
RT 68,31,1024
buffer_load_dwordx4 a[168:171], v16, s[40:43], s63 offen offset:0 sc1
WL 4
MA 150,64,58
RT 70,32,0
MA 154,68,58
RT 72,32,1024
WL 4
MA 158,64,62
RT 74,33,0
MA 162,68,62
RT 76,33,1024
buffer_load_dwordx4 a[172:175], v16, s[40:43], s63 offen offset:1024 sc1
WL 4
MA 166,64,66
RT 54,34,0
s_add_u32 s63, s63, 4096
MA 170,68,66
RT 56,34,1024
WL 4
MA 174,64,70
RT 58,35,0
MA 178,68,70
RT 60,35,1024
buffer_load_dwordx4 a[176:179], v16, s[40:43], s63 offen offset:0 sc1
WL 4
MA 182,64,74
RT 62,28,8192
MA 186,68,74
RT 64,28,9216
WL 4
MA 190,64,54
RT 66,29,8192
MA 194,68,54
RT 68,29,9216
buffer_load_dwordx4 a[180:183], v16, s[40:43], s63 offen offset:1024 sc1
WL 4
MA 198,64,58
RT 70,30,8192
s_add_u32 s63, s63, 4096
MA 202,68,58
RT 72,30,9216
s_waitcnt vmcnt(34) lgkmcnt(4)
MA 142,72,62
RT 74,31,8192
MA 146,76,62
RT 76,31,9216
buffer_load_dwordx4 a[184:187], v16, s[40:43], s63 offen offset:0 sc1
WL 4
MA 150,72,66
RT 54,32,8192
MA 154,76,66
RT 56,32,9216
WL 4
MA 158,72,70
RT 58,33,8192
MA 162,76,70
RT 60,33,9216
buffer_load_dwordx4 a[188:191], v16, s[40:43], s63 offen offset:1024 sc1
WL 4
MA 166,72,74
RT 62,34,8192
s_add_u32 s63, s63, 4096
MA 170,76,74
RT 64,34,9216
WL 4
MA 174,72,54
RT 66,35,8192
MA 178,76,54
RT 68,35,9216
buffer_load_dwordx4 a[32:35], v16, s[40:43], s63 offen offset:0 sc1
WL 4
MA 182,72,58
RT 70,28,16384
MA 186,76,58
RT 72,28,17408
WL 4
MA 190,72,62
RT 74,29,16384
MA 194,76,62
RT 76,29,17408
buffer_load_dwordx4 a[36:39], v16, s[40:43], s63 offen offset:1024 sc1
WL 4
MA 198,72,66
RT 54,30,16384
s_add_u32 s63, s63, 4096
MA 202,76,66
RT 56,30,17408
s_waitcnt vmcnt(34) lgkmcnt(4)
MA 142,80,70
RT 58,31,16384
MA 146,84,70
RT 60,31,17408
buffer_load_dwordx4 a[40:43], v16, s[40:43], s63 offen offset:0 sc1
WL 4
MA 150,80,74
RT 62,32,16384
MA 154,84,74
RT 64,32,17408
WL 4
MA 158,80,54
RT 66,33,16384
MA 162,84,54
RT 68,33,17408
buffer_load_dwordx4 a[44:47], v16, s[40:43], s63 offen offset:1024 sc1
WL 4
MA 166,80,58
RT 70,34,16384
s_add_u32 s63, s63, 4096
MA 170,84,58
RT 72,34,17408
WL 4
MA 174,80,62
RT 74,35,16384
MA 178,84,62
RT 76,35,17408
buffer_load_dwordx4 a[48:51], v16, s[40:43], s63 offen offset:0 sc1
WL 4
MA 182,80,66
RT 54,28,24576
MA 186,84,66
RT 56,28,25600
WL 4
MA 190,80,70
RT 58,29,24576
MA 194,84,70
RT 60,29,25600
buffer_load_dwordx4 a[52:55], v16, s[40:43], s63 offen offset:1024 sc1
WL 4
MA 198,80,74
RT 62,30,24576
s_add_u32 s63, s63, 4096
MA 202,84,74
RT 64,30,25600
s_waitcnt vmcnt(34) lgkmcnt(4)
MA 142,88,54
RT 66,31,24576
MA 146,92,54
RT 68,31,25600
buffer_load_dwordx4 a[56:59], v16, s[40:43], s63 offen offset:0 sc1
WL 4
MA 150,88,58
RT 70,32,24576
MA 154,92,58
RT 72,32,25600
WL 4
MA 158,88,62
RT 74,33,24576
MA 162,92,62
RT 76,33,25600
buffer_load_dwordx4 a[60:63], v16, s[40:43], s63 offen offset:1024 sc1
WL 4
MA 166,88,66
RT 54,34,24576
s_add_u32 s63, s63, 4096
MA 170,92,66
RT 56,34,25600
WL 4
MA 174,88,70
RT 58,35,24576
MA 178,92,70
RT 60,35,25600
buffer_load_dwordx4 a[64:67], v16, s[40:43], s63 offen offset:0 sc1
WL 4
MA 182,88,74
s_waitcnt vmcnt(31)
s_barrier
RT 62,28,32768
MA 186,92,74
RT 64,28,33792
WL 4
MA 190,88,54
RT 66,29,32768
MA 194,92,54
RT 68,29,33792
buffer_load_dwordx4 a[68:71], v16, s[40:43], s63 offen offset:1024 sc1
WL 4
MA 198,88,58
RT 70,30,32768
s_add_u32 s63, s63, 4096
MA 202,92,58
RT 72,30,33792
WL 4
MA 142,96,62
RT 74,31,32768
MA 146,100,62
RT 76,31,33792
buffer_load_dwordx4 a[72:75], v16, s[40:43], s63 offen offset:0 sc1
WL 4
MA 150,96,66
RT 54,32,32768
MA 154,100,66
RT 56,32,33792
WL 4
MA 158,96,70
RT 58,33,32768
MA 162,100,70
RT 60,33,33792
buffer_load_dwordx4 a[76:79], v16, s[40:43], s63 offen offset:1024 sc1
WL 4
MA 166,96,74
RT 62,34,32768
s_add_u32 s63, s63, 4096
MA 170,100,74
RT 64,34,33792
WL 4
MA 174,96,54
RT 66,35,32768
MA 178,100,54
RT 68,35,33792
buffer_load_dwordx4 a[80:83], v16, s[40:43], s63 offen offset:0 sc1
WL 4
MA 182,96,58
RT 70,28,40960
MA 186,100,58
RT 72,28,41984
WL 4
MA 190,96,62
RT 74,29,40960
MA 194,100,62
RT 76,29,41984
buffer_load_dwordx4 a[84:87], v16, s[40:43], s63 offen offset:1024 sc1
WL 4
MA 198,96,66
RT 54,30,40960
s_add_u32 s63, s63, 4096
MA 202,100,66
RT 56,30,41984
s_waitcnt vmcnt(34) lgkmcnt(4)
MA 142,104,70
RT 58,31,40960
MA 146,108,70
RT 60,31,41984
buffer_load_dwordx4 a[88:91], v16, s[40:43], s63 offen offset:0 sc1
WL 4
MA 150,104,74
RT 62,32,40960
MA 154,108,74
RT 64,32,41984
WL 4
MA 158,104,54
RT 66,33,40960
MA 162,108,54
RT 68,33,41984
buffer_load_dwordx4 a[92:95], v16, s[40:43], s63 offen offset:1024 sc1
WL 4
MA 166,104,58
RT 70,34,40960
s_add_u32 s63, s63, 4096
MA 170,108,58
RT 72,34,41984
WL 4
MA 174,104,62
RT 74,35,40960
MA 178,108,62
RT 76,35,41984
buffer_load_dwordx4 a[96:99], v16, s[40:43], s63 offen offset:0 sc1
WL 4
MA 182,104,66
RT 54,28,49152
MA 186,108,66
RT 56,28,50176
WL 4
MA 190,104,70
RT 58,29,49152
MA 194,108,70
RT 60,29,50176
buffer_load_dwordx4 a[100:103], v16, s[40:43], s63 offen offset:1024 sc1
WL 4
MA 198,104,74
RT 62,30,49152
s_add_u32 s63, s63, 4096
MA 202,108,74
RT 64,30,50176
s_waitcnt vmcnt(36) lgkmcnt(4)
MA 142,112,54
RT 66,31,49152
MA 146,116,54
RT 68,31,50176
)"
R"(buffer_load_dwordx4 a[104:107], v16, s[40:43], s63 offen offset:0 sc1
WL 4
MA 150,112,58
RT 70,32,49152
MA 154,116,58
RT 72,32,50176
WL 4
MA 158,112,62
RT 74,33,49152
MA 162,116,62
RT 76,33,50176
buffer_load_dwordx4 a[108:111], v16, s[40:43], s63 offen offset:1024 sc1
WL 4
MA 166,112,66
RT 54,34,49152
s_add_u32 s63, s63, 4096
MA 170,116,66
RT 56,34,50176
WL 4
MA 174,112,70
RT 58,35,49152
MA 178,116,70
RT 60,35,50176
WL 4
MA 182,112,74
RT 62,28,57344
MA 186,116,74
RT 64,28,58368
WL 4
MA 190,112,54
RT 66,29,57344
MA 194,116,54
RT 68,29,58368
WL 4
MA 198,112,58
RT 70,30,57344
MA 202,116,58
RT 72,30,58368
s_waitcnt vmcnt(36) lgkmcnt(4)
MA 142,120,62
RT 74,31,57344
MA 146,124,62
RT 76,31,58368
buffer_load_dwordx4 a[112:115], v16, s[40:43], s63 offen offset:0 sc1
WL 4
MA 150,120,66
RT 54,32,57344
MA 154,124,66
RT 56,32,58368
WL 4
MA 158,120,70
RT 58,33,57344
MA 162,124,70
RT 60,33,58368
buffer_load_dwordx4 a[116:119], v16, s[40:43], s63 offen offset:1024 sc1
WL 4
MA 166,120,74
RT 62,34,57344
s_add_u32 s63, s63, 4096
MA 170,124,74
RT 64,34,58368
WL 4
MA 174,120,54
RT 66,35,57344
MA 178,124,54
RT 68,35,58368
WL 4
MA 182,120,58
RT 70,36,0
MA 186,124,58
RT 72,36,1024
WL 4
MA 190,120,62
RT 74,37,0
MA 194,124,62
RT 76,37,1024
WL 4
MA 198,120,66
RT 54,38,0
MA 202,124,66
RT 56,38,1024
s_waitcnt vmcnt(36) lgkmcnt(4)
MA 78,128,70
RT 58,39,0
MA 82,132,70
RT 60,39,1024
buffer_load_dwordx4 a[120:123], v16, s[40:43], s63 offen offset:0 sc1
WL 4
MA 86,128,74
RT 62,40,0
MA 90,132,74
RT 64,40,1024
WL 4
MA 94,128,54
RT 66,41,0
MA 98,132,54
RT 68,41,1024
buffer_load_dwordx4 a[124:127], v16, s[40:43], s63 offen offset:1024 sc1
WL 4
MA 102,128,58
RT 70,42,0
s_add_u32 s63, s63, 4096
MA 106,132,58
RT 72,42,1024
WL 4
MA 110,128,62
RT 74,43,0
MA 114,132,62
RT 76,43,1024
WL 4
MA 118,128,66
RT 54,36,8192
MA 122,132,66
RT 56,36,9216
WL 4
MA 126,128,70
RT 58,37,8192
MA 130,132,70
RT 60,37,9216
WL 4
MA 134,128,74
RT 62,38,8192
MA 138,132,74
RT 64,38,9216
s_waitcnt vmcnt(36) lgkmcnt(4)
MA 78,136,54
RT 66,39,8192
v_cvt_pk_bf16_f32 v142, v142, v143
MA 82,140,54
RT 68,39,9216
v_cvt_pk_bf16_f32 v143, v144, v145
WL 4
MA 86,136,58
RT 70,40,8192
v_cvt_pk_bf16_f32 v144, v146, v147
MA 90,140,58
RT 72,40,9216
v_cvt_pk_bf16_f32 v145, v148, v149
WL 4
MA 94,136,62
RT 74,41,8192
MA 98,140,62
RT 76,41,9216
buffer_store_dwordx4 v[142:145], v3, s[92:95], s56 offen offset:0 sc1
v_cvt_pk_bf16_f32 v150, v150, v151
WL 4
MA 102,136,66
RT 54,42,8192
v_cvt_pk_bf16_f32 v151, v152, v153
MA 106,140,66
RT 56,42,9216
v_cvt_pk_bf16_f32 v152, v154, v155
WL 4
MA 110,136,70
RT 58,43,8192
v_cvt_pk_bf16_f32 v153, v156, v157
MA 114,140,70
RT 60,43,9216
buffer_store_dwordx4 v[150:153], v3, s[92:95], s56 offen offset:1024 sc1
v_cvt_pk_bf16_f32 v158, v158, v159
WL 4
MA 118,136,74
RT 62,36,16384
v_cvt_pk_bf16_f32 v159, v160, v161
MA 122,140,74
RT 64,36,17408
v_cvt_pk_bf16_f32 v160, v162, v163
WL 4
MA 126,136,54
RT 66,37,16384
v_cvt_pk_bf16_f32 v161, v164, v165
MA 130,140,54
RT 68,37,17408
buffer_store_dwordx4 v[158:161], v3, s[92:95], s56 offen offset:2048 sc1
v_cvt_pk_bf16_f32 v166, v166, v167
WL 4
MA 134,136,58
RT 70,38,16384
v_cvt_pk_bf16_f32 v167, v168, v169
MA 138,140,58
RT 72,38,17408
v_cvt_pk_bf16_f32 v168, v170, v171
s_waitcnt vmcnt(37) lgkmcnt(4)
MA 78,144,62
RT 74,39,16384
v_cvt_pk_bf16_f32 v169, v172, v173
MA 82,148,62
RT 76,39,17408
buffer_store_dwordx4 v[166:169], v3, s[92:95], s56 offen offset:3072 sc1
v_cvt_pk_bf16_f32 v174, v174, v175
WL 4
MA 86,144,66
RT 54,40,16384
v_cvt_pk_bf16_f32 v175, v176, v177
MA 90,148,66
RT 56,40,17408
v_cvt_pk_bf16_f32 v176, v178, v179
WL 4
MA 94,144,70
RT 58,41,16384
v_cvt_pk_bf16_f32 v177, v180, v181
MA 98,148,70
RT 60,41,17408
s_add_u32 s56, s56, 4096
WL 4
MA 102,144,74
RT 62,42,16384
MA 106,148,74
RT 64,42,17408
buffer_store_dwordx4 v[174:177], v3, s[92:95], s56 offen offset:0 sc1
v_cvt_pk_bf16_f32 v182, v182, v183
WL 4
MA 110,144,54
RT 66,43,16384
v_cvt_pk_bf16_f32 v183, v184, v185
MA 114,148,54
RT 68,43,17408
v_cvt_pk_bf16_f32 v184, v186, v187
WL 4
MA 118,144,58
RT 70,36,24576
v_cvt_pk_bf16_f32 v185, v188, v189
MA 122,148,58
RT 72,36,25600
buffer_store_dwordx4 v[182:185], v3, s[92:95], s56 offen offset:1024 sc1
v_cvt_pk_bf16_f32 v190, v190, v191
WL 4
MA 126,144,62
RT 74,37,24576
v_cvt_pk_bf16_f32 v191, v192, v193
MA 130,148,62
RT 76,37,25600
v_cvt_pk_bf16_f32 v192, v194, v195
WL 4
MA 134,144,66
RT 54,38,24576
v_cvt_pk_bf16_f32 v193, v196, v197
MA 138,148,66
RT 56,38,25600
buffer_store_dwordx4 v[190:193], v3, s[92:95], s56 offen offset:2048 sc1
v_cvt_pk_bf16_f32 v198, v198, v199
s_waitcnt vmcnt(39) lgkmcnt(4)
MA 78,152,70
RT 58,39,24576
v_cvt_pk_bf16_f32 v199, v200, v201
MA 82,156,70
RT 60,39,25600
v_cvt_pk_bf16_f32 v200, v202, v203
WL 4
MA 86,152,74
RT 62,40,24576
v_cvt_pk_bf16_f32 v201, v204, v205
MA 90,156,74
RT 64,40,25600
buffer_store_dwordx4 v[198:201], v3, s[92:95], s56 offen offset:3072 sc1
WL 4
MA 94,152,54
RT 66,41,24576
MA 98,156,54
RT 68,41,25600
WL 4
MA 102,152,58
RT 70,42,24576
MA 106,156,58
RT 72,42,25600
WL 4
MA 110,152,62
RT 74,43,24576
MA 114,156,62
RT 76,43,25600
WL 4
MA 118,152,66
RT 54,20,0
MA 122,156,66
RT 56,20,1024
WL 4
MA 126,152,70
RT 58,21,0
MA 130,156,70
RT 60,21,1024
WL 4
MA 134,152,74
RT 62,22,0
MA 138,156,74
RT 64,22,1024
s_waitcnt vmcnt(38) lgkmcnt(4)
MA 78,160,54
RT 66,23,0
MA 82,164,54
RT 68,23,1024
WL 4
MA 86,160,58
RT 70,24,0
MA 90,164,58
RT 72,24,1024
WL 4
MA 94,160,62
RT 74,25,0
MA 98,164,62
RT 76,25,1024
WL 4
MA 102,160,66
RT 54,26,0
MA 106,164,66
RT 56,26,1024
WL 4
MA 110,160,70
RT 58,27,0
MA 114,164,70
RT 60,27,1024
WL 4
MA 118,160,74
RT 62,20,8192
MA 122,164,74
RT 64,20,9216
WL 4
MA 126,160,54
RT 66,21,8192
MA 130,164,54
RT 68,21,9216
WL 4
MA 134,160,58
RT 70,22,8192
MA 138,164,58
RT 72,22,9216
s_waitcnt vmcnt(36) lgkmcnt(4)
MA 78,168,62
RT 74,23,8192
MA 82,172,62
RT 76,23,9216
WL 4
MA 86,168,66
RT 54,24,8192
MA 90,172,66
RT 56,24,9216
WL 4
MA 94,168,70
RT 58,25,8192
MA 98,172,70
RT 60,25,9216
WL 4
MA 102,168,74
RT 62,26,8192
MA 106,172,74
RT 64,26,9216
WL 4
MA 110,168,54
RT 66,27,8192
MA 114,172,54
RT 68,27,9216
WL 4
MA 118,168,58
RT 70,20,16384
MA 122,172,58
RT 72,20,17408
WL 4
MA 126,168,62
RT 74,21,16384
MA 130,172,62
RT 76,21,17408
WL 4
MA 134,168,66
RT 54,22,16384
MA 138,172,66
RT 56,22,17408
s_waitcnt vmcnt(34) lgkmcnt(4)
MA 78,176,70
RT 58,23,16384
MA 82,180,70
RT 60,23,17408
WL 4
MA 86,176,74
RT 62,24,16384
MA 90,180,74
RT 64,24,17408
WL 4
MA 94,176,54
RT 66,25,16384
MA 98,180,54
RT 68,25,17408
WL 4
MA 102,176,58
RT 70,26,16384
MA 106,180,58
RT 72,26,17408
WL 4
MA 110,176,62
RT 74,27,16384
MA 114,180,62
RT 76,27,17408
WL 4
MA 118,176,66
RT 54,20,24576
MA 122,180,66
RT 56,20,25600
WL 4
MA 126,176,70
RT 58,21,24576
MA 130,180,70
RT 60,21,25600
)"
R"(WL 4
MA 134,176,74
RT 62,22,24576
MA 138,180,74
RT 64,22,25600
s_waitcnt vmcnt(32) lgkmcnt(4)
MA 78,184,54
RT 66,23,24576
MA 82,188,54
RT 68,23,25600
WL 4
MA 86,184,58
RT 70,24,24576
MA 90,188,58
RT 72,24,25600
WL 4
MA 94,184,62
RT 74,25,24576
MA 98,188,62
RT 76,25,25600
WL 4
MA 102,184,66
RT 54,26,24576
MA 106,188,66
RT 56,26,25600
WL 4
MA 110,184,70
RT 58,27,24576
MA 114,188,70
RT 60,27,25600
WL 4
MA 118,184,74
RT 62,20,32768
MA 122,188,74
RT 64,20,33792
WL 4
MA 126,184,54
RT 66,21,32768
MA 130,188,54
RT 68,21,33792
WL 4
MA 134,184,58
RT 70,22,32768
MA 138,188,58
RT 72,22,33792
s_waitcnt vmcnt(0)
s_lshl_b32 s53, %1, 2
s_add_u32 s53, s53, s7
s_lshl_b32 s53, s53, 6
s_add_u32 s53, s53, 0x1010000
v_mov_b32 v206, s98
v_mov_b32 v207, s99
s_mov_b64 exec, 1
buffer_store_dwordx2 v[206:207], off, s[92:95], s53 offset:0 sc1
s_mov_b64 exec, -1
s_mov_b32 s62, 0
v_not_b32 v230, s98
v_not_b32 v231, s99
WL 4
MA 78,32,62
RT 74,23,32768
MA 82,36,62
RT 76,23,33792
WL 4
MA 86,32,66
RT 54,24,32768
MA 90,36,66
RT 56,24,33792
WL 4
MA 94,32,70
RT 58,25,32768
MA 98,36,70
RT 60,25,33792
WL 4
MA 102,32,74
RT 62,26,32768
MA 106,36,74
RT 64,26,33792
WL 4
MA 110,32,54
RT 66,27,32768
MA 114,36,54
RT 68,27,33792
WL 4
MA 118,32,58
RT 70,20,40960
MA 122,36,58
RT 72,20,41984
WL 4
MA 126,32,62
RT 74,21,40960
MA 130,36,62
RT 76,21,41984
WL 4
MA 134,32,66
RT 54,22,40960
MA 138,36,66
RT 56,22,41984
buffer_load_dwordx2 v[230:231], v46, s[92:95], 0 offen sc1
L_ck65_%=:
WL 4
MA 78,40,70
RT 58,23,40960
MA 82,44,70
RT 60,23,41984
WL 4
MA 86,40,74
RT 62,24,40960
MA 90,44,74
RT 64,24,41984
WL 4
MA 94,40,54
RT 66,25,40960
MA 98,44,54
RT 68,25,41984
WL 4
MA 102,40,58
RT 70,26,40960
MA 106,44,58
RT 72,26,41984
WL 4
MA 110,40,62
RT 74,27,40960
MA 114,44,62
RT 76,27,41984
WL 4
MA 118,40,66
RT 54,20,49152
MA 122,44,66
RT 56,20,50176
WL 4
MA 126,40,70
RT 58,21,49152
MA 130,44,70
RT 60,21,50176
WL 4
MA 134,40,74
RT 62,22,49152
MA 138,44,74
RT 64,22,50176
buffer_load_dwordx2 v[230:231], v46, s[92:95], 0 offen sc1
L_ck66_%=:
WL 4
MA 78,48,54
RT 66,23,49152
MA 82,52,54
RT 68,23,50176
WL 4
MA 86,48,58
RT 70,24,49152
MA 90,52,58
RT 72,24,50176
WL 4
MA 94,48,62
RT 74,25,49152
MA 98,52,62
RT 76,25,50176
WL 4
MA 102,48,66
RT 54,26,49152
MA 106,52,66
RT 56,26,50176
WL 4
MA 110,48,70
RT 58,27,49152
MA 114,52,70
RT 60,27,50176
WL 4
MA 118,48,74
RT 62,20,57344
MA 122,52,74
RT 64,20,58368
WL 4
MA 126,48,54
RT 66,21,57344
MA 130,52,54
RT 68,21,58368
WL 4
MA 134,48,58
RT 70,22,57344
MA 138,52,58
RT 72,22,58368
v_cmp_ne_u64 vcc, s[98:99], v[230:231]
s_cmp_eq_u64 vcc, 0
s_cbranch_scc0 L_ckn67_%=
buffer_load_dwordx4 a[0:3], v3, s[92:95], s57 offen offset:0 sc1
buffer_load_dwordx4 a[4:7], v3, s[92:95], s57 offen offset:1024 sc1
buffer_load_dwordx4 a[8:11], v3, s[92:95], s57 offen offset:2048 sc1
buffer_load_dwordx4 a[12:15], v3, s[92:95], s57 offen offset:3072 sc1
s_add_u32 s57, s57, 4096
buffer_load_dwordx4 a[16:19], v3, s[92:95], s57 offen offset:0 sc1
buffer_load_dwordx4 a[20:23], v3, s[92:95], s57 offen offset:1024 sc1
buffer_load_dwordx4 a[24:27], v3, s[92:95], s57 offen offset:2048 sc1
buffer_load_dwordx4 a[28:31], v3, s[92:95], s57 offen offset:3072 sc1
s_mov_b32 s62, 1
s_branch L_ck67_%=
L_ckn67_%=:
buffer_load_dwordx2 v[230:231], v46, s[92:95], 0 offen sc1
L_ck67_%=:
WL 4
MA 78,56,62
RT 74,23,57344
MA 82,60,62
RT 76,23,58368
WL 4
MA 86,56,66
RT 54,24,57344
MA 90,60,66
RT 56,24,58368
WL 4
MA 94,56,70
RT 58,25,57344
MA 98,60,70
RT 60,25,58368
WL 4
MA 102,56,74
RT 62,26,57344
MA 106,60,74
RT 64,26,58368
WL 4
MA 110,56,54
RT 66,27,57344
MA 114,60,54
RT 68,27,58368
WL 4
MA 118,56,58
RT 70,28,0
MA 122,60,58
RT 72,28,1024
WL 4
MA 126,56,62
RT 74,29,0
MA 130,60,62
RT 76,29,1024
WL 4
MA 134,56,66
RT 54,30,0
MA 138,60,66
RT 56,30,1024
s_cmp_lg_u32 s62, 0
s_cbranch_scc1 L_ck68_%=
v_cmp_ne_u64 vcc, s[98:99], v[230:231]
s_cmp_eq_u64 vcc, 0
s_cbranch_scc0 L_ckn68_%=
buffer_load_dwordx4 a[0:3], v3, s[92:95], s57 offen offset:0 sc1
buffer_load_dwordx4 a[4:7], v3, s[92:95], s57 offen offset:1024 sc1
buffer_load_dwordx4 a[8:11], v3, s[92:95], s57 offen offset:2048 sc1
buffer_load_dwordx4 a[12:15], v3, s[92:95], s57 offen offset:3072 sc1
s_add_u32 s57, s57, 4096
buffer_load_dwordx4 a[16:19], v3, s[92:95], s57 offen offset:0 sc1
buffer_load_dwordx4 a[20:23], v3, s[92:95], s57 offen offset:1024 sc1
buffer_load_dwordx4 a[24:27], v3, s[92:95], s57 offen offset:2048 sc1
buffer_load_dwordx4 a[28:31], v3, s[92:95], s57 offen offset:3072 sc1
s_mov_b32 s62, 1
s_branch L_ck68_%=
L_ckn68_%=:
buffer_load_dwordx2 v[230:231], v46, s[92:95], 0 offen sc1
L_ck68_%=:
WL 4
MA 78,64,70
RT 58,31,0
MA 82,68,70
RT 60,31,1024
WL 4
MA 86,64,74
RT 62,32,0
MA 90,68,74
RT 64,32,1024
WL 4
MA 94,64,54
RT 66,33,0
MA 98,68,54
RT 68,33,1024
WL 4
MA 102,64,58
RT 70,34,0
MA 106,68,58
RT 72,34,1024
WL 4
MA 110,64,62
RT 74,35,0
MA 114,68,62
RT 76,35,1024
WL 4
MA 118,64,66
RT 54,28,8192
MA 122,68,66
RT 56,28,9216
WL 4
MA 126,64,70
RT 58,29,8192
MA 130,68,70
RT 60,29,9216
WL 4
MA 134,64,74
RT 62,30,8192
MA 138,68,74
RT 64,30,9216
s_cmp_lg_u32 s62, 0
s_cbranch_scc1 L_ck69_%=
v_cmp_ne_u64 vcc, s[98:99], v[230:231]
s_cmp_eq_u64 vcc, 0
s_cbranch_scc0 L_ckn69_%=
buffer_load_dwordx4 a[0:3], v3, s[92:95], s57 offen offset:0 sc1
buffer_load_dwordx4 a[4:7], v3, s[92:95], s57 offen offset:1024 sc1
buffer_load_dwordx4 a[8:11], v3, s[92:95], s57 offen offset:2048 sc1
buffer_load_dwordx4 a[12:15], v3, s[92:95], s57 offen offset:3072 sc1
s_add_u32 s57, s57, 4096
buffer_load_dwordx4 a[16:19], v3, s[92:95], s57 offen offset:0 sc1
buffer_load_dwordx4 a[20:23], v3, s[92:95], s57 offen offset:1024 sc1
buffer_load_dwordx4 a[24:27], v3, s[92:95], s57 offen offset:2048 sc1
buffer_load_dwordx4 a[28:31], v3, s[92:95], s57 offen offset:3072 sc1
s_mov_b32 s62, 1
s_branch L_ck69_%=
L_ckn69_%=:
buffer_load_dwordx2 v[230:231], v46, s[92:95], 0 offen sc1
L_ck69_%=:
WL 4
MA 78,72,54
RT 66,31,8192
MA 82,76,54
RT 68,31,9216
WL 4
MA 86,72,58
RT 70,32,8192
MA 90,76,58
RT 72,32,9216
WL 4
MA 94,72,62
RT 74,33,8192
MA 98,76,62
RT 76,33,9216
WL 4
MA 102,72,66
RT 54,34,8192
MA 106,76,66
RT 56,34,9216
WL 4
MA 110,72,70
RT 58,35,8192
MA 114,76,70
RT 60,35,9216
WL 4
MA 118,72,74
RT 62,28,16384
MA 122,76,74
RT 64,28,17408
WL 4
MA 126,72,54
RT 66,29,16384
MA 130,76,54
RT 68,29,17408
WL 4
MA 134,72,58
RT 70,30,16384
MA 138,76,58
RT 72,30,17408
s_cmp_lg_u32 s62, 0
s_cbranch_scc1 L_ck70_%=
v_cmp_ne_u64 vcc, s[98:99], v[230:231]
s_cmp_eq_u64 vcc, 0
s_cbranch_scc0 L_ckn70_%=
buffer_load_dwordx4 a[0:3], v3, s[92:95], s57 offen offset:0 sc1
buffer_load_dwordx4 a[4:7], v3, s[92:95], s57 offen offset:1024 sc1
buffer_load_dwordx4 a[8:11], v3, s[92:95], s57 offen offset:2048 sc1
buffer_load_dwordx4 a[12:15], v3, s[92:95], s57 offen offset:3072 sc1
s_add_u32 s57, s57, 4096
buffer_load_dwordx4 a[16:19], v3, s[92:95], s57 offen offset:0 sc1
buffer_load_dwordx4 a[20:23], v3, s[92:95], s57 offen offset:1024 sc1
buffer_load_dwordx4 a[24:27], v3, s[92:95], s57 offen offset:2048 sc1
buffer_load_dwordx4 a[28:31], v3, s[92:95], s57 offen offset:3072 sc1
s_mov_b32 s62, 1
s_branch L_ck70_%=
L_ckn70_%=:
buffer_load_dwordx2 v[230:231], v46, s[92:95], 0 offen sc1
L_ck70_%=:
WL 4
MA 78,80,62
RT 74,31,16384
MA 82,84,62
RT 76,31,17408
WL 4
MA 86,80,66
RT 54,32,16384
MA 90,84,66
RT 56,32,17408
WL 4
MA 94,80,70
RT 58,33,16384
MA 98,84,70
RT 60,33,17408
WL 4
MA 102,80,74
RT 62,34,16384
MA 106,84,74
RT 64,34,17408
WL 4
MA 110,80,54
RT 66,35,16384
MA 114,84,54
)"
R"(RT 68,35,17408
WL 4
MA 118,80,58
RT 70,28,24576
MA 122,84,58
RT 72,28,25600
WL 4
MA 126,80,62
RT 74,29,24576
MA 130,84,62
RT 76,29,25600
WL 4
MA 134,80,66
RT 54,30,24576
MA 138,84,66
RT 56,30,25600
s_cmp_lg_u32 s62, 0
s_cbranch_scc1 L_ck71_%=
v_cmp_ne_u64 vcc, s[98:99], v[230:231]
s_cmp_eq_u64 vcc, 0
s_cbranch_scc0 L_ckn71_%=
buffer_load_dwordx4 a[0:3], v3, s[92:95], s57 offen offset:0 sc1
buffer_load_dwordx4 a[4:7], v3, s[92:95], s57 offen offset:1024 sc1
buffer_load_dwordx4 a[8:11], v3, s[92:95], s57 offen offset:2048 sc1
buffer_load_dwordx4 a[12:15], v3, s[92:95], s57 offen offset:3072 sc1
s_add_u32 s57, s57, 4096
buffer_load_dwordx4 a[16:19], v3, s[92:95], s57 offen offset:0 sc1
buffer_load_dwordx4 a[20:23], v3, s[92:95], s57 offen offset:1024 sc1
buffer_load_dwordx4 a[24:27], v3, s[92:95], s57 offen offset:2048 sc1
buffer_load_dwordx4 a[28:31], v3, s[92:95], s57 offen offset:3072 sc1
s_mov_b32 s62, 1
s_branch L_ck71_%=
L_ckn71_%=:
buffer_load_dwordx2 v[230:231], v46, s[92:95], 0 offen sc1
L_ck71_%=:
WL 4
MA 78,88,70
RT 58,31,24576
MA 82,92,70
RT 60,31,25600
WL 4
MA 86,88,74
RT 62,32,24576
MA 90,92,74
RT 64,32,25600
WL 4
MA 94,88,54
RT 66,33,24576
MA 98,92,54
RT 68,33,25600
WL 4
MA 102,88,58
RT 70,34,24576
MA 106,92,58
RT 72,34,25600
WL 4
MA 110,88,62
RT 74,35,24576
MA 114,92,62
RT 76,35,25600
WL 4
MA 118,88,66
RT 54,28,32768
MA 122,92,66
RT 56,28,33792
WL 4
MA 126,88,70
RT 58,29,32768
MA 130,92,70
RT 60,29,33792
WL 4
MA 134,88,74
RT 62,30,32768
MA 138,92,74
RT 64,30,33792
s_cmp_lg_u32 s62, 0
s_cbranch_scc1 L_ck72_%=
v_cmp_ne_u64 vcc, s[98:99], v[230:231]
s_cmp_eq_u64 vcc, 0
s_cbranch_scc0 L_ckn72_%=
buffer_load_dwordx4 a[0:3], v3, s[92:95], s57 offen offset:0 sc1
buffer_load_dwordx4 a[4:7], v3, s[92:95], s57 offen offset:1024 sc1
buffer_load_dwordx4 a[8:11], v3, s[92:95], s57 offen offset:2048 sc1
buffer_load_dwordx4 a[12:15], v3, s[92:95], s57 offen offset:3072 sc1
s_add_u32 s57, s57, 4096
buffer_load_dwordx4 a[16:19], v3, s[92:95], s57 offen offset:0 sc1
buffer_load_dwordx4 a[20:23], v3, s[92:95], s57 offen offset:1024 sc1
buffer_load_dwordx4 a[24:27], v3, s[92:95], s57 offen offset:2048 sc1
buffer_load_dwordx4 a[28:31], v3, s[92:95], s57 offen offset:3072 sc1
s_mov_b32 s62, 1
s_branch L_ck72_%=
L_ckn72_%=:
buffer_load_dwordx2 v[230:231], v46, s[92:95], 0 offen sc1
L_ck72_%=:
WL 4
MA 78,96,54
RT 66,31,32768
MA 82,100,54
RT 68,31,33792
WL 4
MA 86,96,58
RT 70,32,32768
MA 90,100,58
RT 72,32,33792
WL 4
MA 94,96,62
RT 74,33,32768
MA 98,100,62
RT 76,33,33792
WL 4
MA 102,96,66
RT 54,34,32768
MA 106,100,66
RT 56,34,33792
WL 4
MA 110,96,70
RT 58,35,32768
MA 114,100,70
RT 60,35,33792
WL 4
MA 118,96,74
RT 62,28,40960
MA 122,100,74
RT 64,28,41984
WL 4
MA 126,96,54
RT 66,29,40960
MA 130,100,54
RT 68,29,41984
WL 4
MA 134,96,58
RT 70,30,40960
MA 138,100,58
RT 72,30,41984
s_cmp_lg_u32 s62, 0
s_cbranch_scc1 L_ck73_%=
v_cmp_ne_u64 vcc, s[98:99], v[230:231]
s_cmp_eq_u64 vcc, 0
s_cbranch_scc0 L_ckn73_%=
buffer_load_dwordx4 a[0:3], v3, s[92:95], s57 offen offset:0 sc1
buffer_load_dwordx4 a[4:7], v3, s[92:95], s57 offen offset:1024 sc1
buffer_load_dwordx4 a[8:11], v3, s[92:95], s57 offen offset:2048 sc1
buffer_load_dwordx4 a[12:15], v3, s[92:95], s57 offen offset:3072 sc1
s_add_u32 s57, s57, 4096
buffer_load_dwordx4 a[16:19], v3, s[92:95], s57 offen offset:0 sc1
buffer_load_dwordx4 a[20:23], v3, s[92:95], s57 offen offset:1024 sc1
buffer_load_dwordx4 a[24:27], v3, s[92:95], s57 offen offset:2048 sc1
buffer_load_dwordx4 a[28:31], v3, s[92:95], s57 offen offset:3072 sc1
s_mov_b32 s62, 1
s_branch L_ck73_%=
L_ckn73_%=:
buffer_load_dwordx2 v[230:231], v46, s[92:95], 0 offen sc1
L_ck73_%=:
WL 4
MA 78,104,62
RT 74,31,40960
MA 82,108,62
RT 76,31,41984
WL 4
MA 86,104,66
RT 54,32,40960
MA 90,108,66
RT 56,32,41984
WL 4
MA 94,104,70
RT 58,33,40960
MA 98,108,70
RT 60,33,41984
WL 4
MA 102,104,74
RT 62,34,40960
MA 106,108,74
RT 64,34,41984
WL 4
MA 110,104,54
RT 66,35,40960
MA 114,108,54
RT 68,35,41984
WL 4
MA 118,104,58
RT 70,28,49152
MA 122,108,58
RT 72,28,50176
WL 4
MA 126,104,62
RT 74,29,49152
MA 130,108,62
RT 76,29,50176
WL 4
MA 134,104,66
RT 54,30,49152
MA 138,108,66
RT 56,30,50176
s_cmp_lg_u32 s62, 0
s_cbranch_scc1 L_ck74_%=
v_cmp_ne_u64 vcc, s[98:99], v[230:231]
s_cmp_eq_u64 vcc, 0
s_cbranch_scc0 L_ckn74_%=
buffer_load_dwordx4 a[0:3], v3, s[92:95], s57 offen offset:0 sc1
buffer_load_dwordx4 a[4:7], v3, s[92:95], s57 offen offset:1024 sc1
buffer_load_dwordx4 a[8:11], v3, s[92:95], s57 offen offset:2048 sc1
buffer_load_dwordx4 a[12:15], v3, s[92:95], s57 offen offset:3072 sc1
s_add_u32 s57, s57, 4096
buffer_load_dwordx4 a[16:19], v3, s[92:95], s57 offen offset:0 sc1
buffer_load_dwordx4 a[20:23], v3, s[92:95], s57 offen offset:1024 sc1
buffer_load_dwordx4 a[24:27], v3, s[92:95], s57 offen offset:2048 sc1
buffer_load_dwordx4 a[28:31], v3, s[92:95], s57 offen offset:3072 sc1
s_mov_b32 s62, 1
L_ckn74_%=:
L_ck74_%=:
WL 4
MA 78,112,70
RT 58,31,49152
MA 82,116,70
RT 60,31,50176
WL 4
MA 86,112,74
RT 62,32,49152
MA 90,116,74
RT 64,32,50176
WL 4
MA 94,112,54
RT 66,33,49152
MA 98,116,54
RT 68,33,50176
WL 4
MA 102,112,58
RT 70,34,49152
MA 106,116,58
RT 72,34,50176
WL 4
MA 110,112,62
RT 74,35,49152
MA 114,116,62
RT 76,35,50176
WL 4
MA 118,112,66
RT 54,28,57344
MA 122,116,66
RT 56,28,58368
WL 4
MA 126,112,70
RT 58,29,57344
MA 130,116,70
RT 60,29,58368
WL 4
MA 134,112,74
RT 62,30,57344
MA 138,116,74
RT 64,30,58368
s_cmp_lg_u32 s62, 0
s_cbranch_scc1 L_ck75_%=
v_cmp_ne_u64 vcc, s[98:99], v[230:231]
s_cmp_eq_u64 vcc, 0
s_cbranch_scc0 L_ckn75_%=
buffer_load_dwordx4 a[0:3], v3, s[92:95], s57 offen offset:0 sc1
buffer_load_dwordx4 a[4:7], v3, s[92:95], s57 offen offset:1024 sc1
buffer_load_dwordx4 a[8:11], v3, s[92:95], s57 offen offset:2048 sc1
buffer_load_dwordx4 a[12:15], v3, s[92:95], s57 offen offset:3072 sc1
s_add_u32 s57, s57, 4096
buffer_load_dwordx4 a[16:19], v3, s[92:95], s57 offen offset:0 sc1
buffer_load_dwordx4 a[20:23], v3, s[92:95], s57 offen offset:1024 sc1
buffer_load_dwordx4 a[24:27], v3, s[92:95], s57 offen offset:2048 sc1
buffer_load_dwordx4 a[28:31], v3, s[92:95], s57 offen offset:3072 sc1
s_mov_b32 s62, 1
L_ckn75_%=:
L_ck75_%=:
WL 4
MA 78,120,54
RT 66,31,57344
MA 82,124,54
RT 68,31,58368
WL 4
MA 86,120,58
RT 70,32,57344
MA 90,124,58
RT 72,32,58368
WL 4
MA 94,120,62
RT 74,33,57344
MA 98,124,62
RT 76,33,58368
WL 4
MA 102,120,66
RT 54,34,57344
MA 106,124,66
RT 56,34,58368
WL 4
MA 110,120,70
RT 58,35,57344
MA 114,124,70
RT 60,35,58368
WL 4
MA 118,120,74
MA 122,124,74
WL 2
MA 126,120,54
MA 130,124,54
WL 0
MA 134,120,58
MA 138,124,58
s_cmp_lg_u32 s62, 0
s_cbranch_scc0 L_slowx_%=
L_backx_%=:
s_nop 7
s_nop 1
s_waitcnt vmcnt(7)
MV 78,222,0
MV 82,226,0
s_waitcnt vmcnt(6)
MV 86,222,4
MV 90,226,4
v_cvt_pk_bf16_f32 v206, v78, v79
v_cvt_pk_bf16_f32 v207, v80, v81
v_cvt_pk_bf16_f32 v208, v82, v83
v_cvt_pk_bf16_f32 v209, v84, v85
buffer_store_dwordx4 v[206:209], v3, s[48:51], s64 offen sc1
s_waitcnt vmcnt(6)
MV 94,222,8
MV 98,226,8
v_cvt_pk_bf16_f32 v210, v86, v87
v_cvt_pk_bf16_f32 v211, v88, v89
v_cvt_pk_bf16_f32 v212, v90, v91
v_cvt_pk_bf16_f32 v213, v92, v93
buffer_store_dwordx4 v[210:213], v3, s[48:51], s65 offen sc1
s_waitcnt vmcnt(6)
MV 102,222,12
MV 106,226,12
v_cvt_pk_bf16_f32 v214, v94, v95
v_cvt_pk_bf16_f32 v215, v96, v97
v_cvt_pk_bf16_f32 v216, v98, v99
v_cvt_pk_bf16_f32 v217, v100, v101
buffer_store_dwordx4 v[214:217], v3, s[48:51], s66 offen sc1
s_waitcnt vmcnt(6)
MV 110,222,16
MV 114,226,16
v_cvt_pk_bf16_f32 v218, v102, v103
v_cvt_pk_bf16_f32 v219, v104, v105
v_cvt_pk_bf16_f32 v220, v106, v107
v_cvt_pk_bf16_f32 v221, v108, v109
buffer_store_dwordx4 v[218:221], v3, s[48:51], s67 offen sc1
s_waitcnt vmcnt(6)
MV 118,222,20
MV 122,226,20
v_cvt_pk_bf16_f32 v206, v110, v111
v_cvt_pk_bf16_f32 v207, v112, v113
v_cvt_pk_bf16_f32 v208, v114, v115
v_cvt_pk_bf16_f32 v209, v116, v117
buffer_store_dwordx4 v[206:209], v3, s[48:51], s68 offen sc1
s_waitcnt vmcnt(6)
MV 126,222,24
MV 130,226,24
v_cvt_pk_bf16_f32 v210, v118, v119
v_cvt_pk_bf16_f32 v211, v120, v121
v_cvt_pk_bf16_f32 v212, v122, v123
v_cvt_pk_bf16_f32 v213, v124, v125
buffer_store_dwordx4 v[210:213], v3, s[48:51], s69 offen sc1
s_waitcnt vmcnt(6)
MV 134,222,28
MV 138,226,28
v_cvt_pk_bf16_f32 v214, v126, v127
v_cvt_pk_bf16_f32 v215, v128, v129
v_cvt_pk_bf16_f32 v216, v130, v131
v_cvt_pk_bf16_f32 v217, v132, v133
buffer_store_dwordx4 v[214:217], v3, s[48:51], s70 offen sc1
s_nop 7
v_cvt_pk_bf16_f32 v218, v134, v135
v_cvt_pk_bf16_f32 v219, v136, v137
v_cvt_pk_bf16_f32 v220, v138, v139
v_cvt_pk_bf16_f32 v221, v140, v141
buffer_store_dwordx4 v[218:221], v3, s[48:51], s71 offen sc1
buffer_load_dwordx2 v[52:53], v48, s[92:95], 0 offen sc1
s_lshl_b32 s53, s26, 4
s_add_u32 s53, s53, s25
s_lshl_b32 s53, s53, 1
s_add_u32 s53, s53, s24
s_lshl_b32 s53, s53, 3
s_add_u32 s53, s53, 0x1002000
s_waitcnt vmcnt(1)
s_barrier
s_cmp_eq_u32 s7, 0
s_cselect_b32 s53, s53, 0x80000000
v_mov_b32 v44, s98
v_mov_b32 v45, s99
s_mov_b64 exec, 1
buffer_store_dwordx2 v[44:45], off, s[92:95], s53 sc1
s_mov_b64 exec, -1
s_waitcnt vmcnt(1)
v_cmp_ne_u64 vcc, s[98:99], v[52:53]
s_cmp_eq_u64 vcc, 0
s_cbranch_scc0 L_slowgX_%=
L_backgX_%=:
buffer_load_dwordx4 a[192:195], v2, s[36:39], s35 offen offset:0 sc1
buffer_load_dwordx4 a[196:199], v2, s[36:39], s35 offen offset:1024 sc1
s_add_u32 s35, s35, 4096
buffer_load_dwordx4 a[200:203], v2, s[36:39], s35 offen offset:0 sc1
buffer_load_dwordx4 a[204:207], v2, s[36:39], s35 offen offset:1024 sc1
s_add_u32 s35, s35, 4096
buffer_load_dwordx4 a[208:211], v2, s[36:39], s35 offen offset:0 sc1
buffer_load_dwordx4 a[212:215], v2, s[36:39], s35 offen offset:1024 sc1
)"
R"(s_add_u32 s35, s35, 4096
buffer_load_dwordx4 a[216:219], v2, s[36:39], s35 offen offset:0 sc1
buffer_load_dwordx4 a[220:223], v2, s[36:39], s35 offen offset:1024 sc1
s_add_u32 s35, s35, 4096
buffer_load_dwordx4 a[224:227], v2, s[36:39], s35 offen offset:0 sc1
buffer_load_dwordx4 a[228:231], v2, s[36:39], s35 offen offset:1024 sc1
s_add_u32 s35, s35, 4096
buffer_load_dwordx4 a[232:235], v2, s[36:39], s35 offen offset:0 sc1
buffer_load_dwordx4 a[236:239], v2, s[36:39], s35 offen offset:1024 sc1
s_add_u32 s35, s35, 4096
buffer_load_dwordx4 a[240:243], v2, s[36:39], s35 offen offset:0 sc1
buffer_load_dwordx4 a[244:247], v2, s[36:39], s35 offen offset:1024 sc1
s_add_u32 s35, s35, 4096
buffer_load_dwordx4 a[248:251], v2, s[36:39], s35 offen offset:0 sc1
buffer_load_dwordx4 a[252:255], v2, s[36:39], s35 offen offset:1024 sc1
s_add_u32 s35, s35, 4096
.Lcstart:
s_bfe_u32 s25, %1, 0x20001
s_lshl_b32 s25, s25, 2
s_lshr_b32 s26, %1, 6
s_add_u32 s25, s25, s26
s_mul_i32 s26, s25, 14
s_add_u32 s27, s26, 13
s_lshr_b32 s26, s26, 3
s_lshr_b32 s27, s27, 3
s_sub_u32 s27, s27, s26
s_lshl_b32 s27, s27, 3
s_add_u32 s27, s27, 7
s_lshl_b32 s26, s26, 8
s_add_u32 s26, s26, 0x1002000
v_min_u32 v42, s27, v1
v_lshrrev_b32 v43, 3, v42
v_and_b32 v42, 7, v42
v_lshl_add_u32 v42, v42, 3, s26
v_lshl_add_u32 v30, v43, 8, v42
s_and_b32 s24, %1, 1
s_lshl_b32 s24, s24, 3
s_bfe_u32 s26, %1, 0x30003
s_add_u32 s24, s24, s26
s_cmp_lg_u32 s7, 0
s_cbranch_scc1 L_np_%=
buffer_load_dwordx2 v[28:29], v30, s[92:95], 0 offen sc1
L_np_%=:
s_mul_i32 s29, s25, 458752
s_bitcmp1_b32 s25, 0
s_cselect_b32 s26, 6, 0
s_cselect_b32 s27, 2, 8
s_cselect_b32 s28, 0, 12
s_add_u32 s44, s20, 0x1000000
s_addc_u32 s45, s21, 0
s_mov_b32 s46, 0x700000
s_mov_b32 s47, 0x20000
s_mov_b32 s55, 0
s_mul_i32 s56, s7, 4160
s_lshr_b32 s30, s7, 1
s_mul_i32 s30, s30, 14
s_and_b32 s31, s7, 1
s_lshl_b32 s31, s31, 2
s_add_u32 s30, s30, s31
s_add_u32 s30, s30, s26
s_lshl_b32 s30, s30, 10
s_add_u32 s30, s30, s29
v_add_u32 v31, s30, v3
v_add_u32 v32, 1024, v31
v_add_u32 v33, 2048, v31
v_add_u32 v34, 3072, v31
s_mul_i32 s57, s7, 4160
s_mul_i32 s30, s7, 14
s_add_u32 s30, s30, s27
s_lshl_b32 s30, s30, 10
s_add_u32 s30, s30, s29
v_add_u32 v35, s30, v3
v_add_u32 v36, 1024, v35
v_add_u32 v37, 2048, v35
v_add_u32 v38, 3072, v35
s_mul_i32 s58, s7, 2080
s_mul_i32 s30, s7, 14
s_add_u32 s30, s30, s28
s_lshl_b32 s30, s30, 10
s_add_u32 s30, s30, s29
v_add_u32 v39, s30, v3
v_add_u32 v40, 1024, v39
s_mov_b32 s48, s22
s_mov_b32 s49, s23
s_mov_b32 s50, 0xe00000
s_mov_b32 s51, 0x20000
s_lshl_b32 s30, s24, 2
s_add_u32 s30, s30, s7
s_mul_i32 s30, s30, 229376
s_mul_i32 s31, s25, 448
s_add_u32 s60, s30, s31
v_and_b32 v42, 15, v1
v_lshlrev_b32 v42, 3, v42
v_lshrrev_b32 v43, 4, v42
v_mul_u32_u24 v43, 1040, v43
v_and_b32 v42, 15, v42
v_lshl_add_u32 v42, v42, 4, v43
v_lshrrev_b32 v43, 4, v1
v_lshl_add_u32 v16, v43, 8, v42
v_add_u32 v17, 49920, v16
v_add_u32 v18, 99840, v16
v_and_b32 v42, 15, v1
v_lshlrev_b32 v42, 2, v42
v_lshrrev_b32 v43, 4, v42
v_mul_u32_u24 v43, 1040, v43
v_and_b32 v42, 15, v42
v_lshl_add_u32 v42, v42, 4, v43
v_lshrrev_b32 v43, 4, v1
v_lshl_add_u32 v19, v43, 8, v42
v_add_u32 v20, 49920, v19
v_add_u32 v21, 99840, v19
v_and_b32 v42, 15, v1
v_lshlrev_b32 v42, 1, v42
v_lshrrev_b32 v43, 4, v42
v_mul_u32_u24 v43, 1040, v43
v_and_b32 v42, 15, v42
v_lshl_add_u32 v42, v42, 4, v43
v_lshrrev_b32 v43, 4, v1
v_lshl_add_u32 v22, v43, 8, v42
v_add_u32 v23, 49920, v22
v_add_u32 v24, 99840, v22
v_lshrrev_b32 v42, 4, v1
v_mul_u32_u24 v42, 28672, v42
v_and_b32 v43, 15, v1
v_mul_u32_u24 v43, 16, v43
s_lshl_b32 s53, s26, 5
v_add3_u32 v25, v42, v43, s53
v_lshrrev_b32 v42, 4, v1
v_mul_u32_u24 v42, 28672, v42
v_and_b32 v43, 15, v1
v_mul_u32_u24 v43, 8, v43
s_lshl_b32 s53, s27, 5
v_add3_u32 v26, v42, v43, s53
v_lshrrev_b32 v42, 4, v1
v_mul_u32_u24 v42, 28672, v42
v_and_b32 v43, 15, v1
v_mul_u32_u24 v43, 4, v43
s_lshl_b32 s53, s28, 5
v_add3_u32 v27, v42, v43, s53
s_mov_b32 s31, 0x80000000
s_cmp_lg_u32 s7, 0
s_cbranch_scc1 L_spwx_%=
s_waitcnt vmcnt(0)
v_cmp_ne_u64 vcc, s[98:99], v[28:29]
s_cmp_eq_u64 vcc, 0
s_cbranch_scc1 L_spwx_%=
s_mov_b32 s54, 0x40000
L_spw_%=:
buffer_load_dwordx2 v[28:29], v30, s[92:95], 0 offen offset:0 sc1
s_waitcnt vmcnt(0)
v_cmp_ne_u64 vcc, s[98:99], v[28:29]
s_sub_u32 s54, s54, 1
s_cbranch_scc1 L_spwx_%=
s_cmp_eq_u64 vcc, 0
s_cbranch_scc1 L_spwx_%=
s_sleep 1
s_branch L_spw_%=
L_spwx_%=:
s_barrier
s_cmp_eq_u32 %1, 224
s_cselect_b32 s53, 0x1020000, s31
s_cmp_eq_u32 s7, 0
s_cselect_b32 s53, s53, 0x80000000
v_mov_b32 v28, s98
v_mov_b32 v29, s99
s_mov_b64 exec, 1
buffer_store_dwordx2 v[28:29], off, s[92:95], s53 sc1
s_mov_b64 exec, -1
s_add_u32 m0, s56, 0
s_nop 0
buffer_load_dwordx4 v31, s[44:47], s55 offen sc1 lds
s_add_u32 m0, s56, 1040
s_nop 0
buffer_load_dwordx4 v32, s[44:47], s55 offen sc1 lds
s_add_u32 m0, s56, 2080
s_nop 0
buffer_load_dwordx4 v33, s[44:47], s55 offen sc1 lds
s_add_u32 m0, s56, 3120
s_nop 0
buffer_load_dwordx4 v34, s[44:47], s55 offen sc1 lds
s_add_u32 s55, s55, 28672
s_add_u32 m0, s56, 16640
s_nop 0
buffer_load_dwordx4 v31, s[44:47], s55 offen sc1 lds
s_add_u32 m0, s56, 17680
s_nop 0
buffer_load_dwordx4 v32, s[44:47], s55 offen sc1 lds
s_add_u32 m0, s56, 18720
s_nop 0
buffer_load_dwordx4 v33, s[44:47], s55 offen sc1 lds
s_add_u32 m0, s56, 19760
s_nop 0
buffer_load_dwordx4 v34, s[44:47], s55 offen sc1 lds
s_add_u32 s55, s55, 28672
s_add_u32 m0, s56, 33280
s_nop 0
buffer_load_dwordx4 v31, s[44:47], s55 offen sc1 lds
s_add_u32 m0, s56, 34320
s_nop 0
buffer_load_dwordx4 v32, s[44:47], s55 offen sc1 lds
s_add_u32 m0, s56, 35360
s_nop 0
buffer_load_dwordx4 v33, s[44:47], s55 offen sc1 lds
s_add_u32 m0, s56, 36400
s_nop 0
buffer_load_dwordx4 v34, s[44:47], s55 offen sc1 lds
s_add_u32 s55, s55, 28672
s_add_u32 m0, s56, 49920
s_nop 0
buffer_load_dwordx4 v31, s[44:47], s55 offen sc1 lds
s_add_u32 m0, s56, 50960
s_nop 0
buffer_load_dwordx4 v32, s[44:47], s55 offen sc1 lds
s_add_u32 m0, s56, 52000
s_nop 0
buffer_load_dwordx4 v33, s[44:47], s55 offen sc1 lds
s_add_u32 m0, s56, 53040
s_nop 0
buffer_load_dwordx4 v34, s[44:47], s55 offen sc1 lds
s_add_u32 s55, s55, 28672
s_waitcnt vmcnt(12)
s_barrier
RB 42,16,0
RB 46,16,16
RB 50,16,32
RB 54,16,48
RB 58,16,64
RB 62,16,80
WL 5
v_mfma_f32_16x16x32_bf16 v[90:93], a[192:195], v[42:45], 0
RB 66,16,96
v_mfma_f32_16x16x32_bf16 v[94:97], a[196:199], v[42:45], 0
WL 5
v_mfma_f32_16x16x32_bf16 v[98:101], a[192:195], v[46:49], 0
RB 70,16,112
v_mfma_f32_16x16x32_bf16 v[102:105], a[196:199], v[46:49], 0
WL 5
v_mfma_f32_16x16x32_bf16 v[106:109], a[192:195], v[50:53], 0
RB 42,16,8320
v_mfma_f32_16x16x32_bf16 v[110:113], a[196:199], v[50:53], 0
WL 5
v_mfma_f32_16x16x32_bf16 v[114:117], a[192:195], v[54:57], 0
RB 46,16,8336
v_mfma_f32_16x16x32_bf16 v[118:121], a[196:199], v[54:57], 0
WL 5
v_mfma_f32_16x16x32_bf16 v[122:125], a[192:195], v[58:61], 0
RB 50,16,8352
v_mfma_f32_16x16x32_bf16 v[126:129], a[196:199], v[58:61], 0
WL 5
v_mfma_f32_16x16x32_bf16 v[130:133], a[192:195], v[62:65], 0
RB 54,16,8368
v_mfma_f32_16x16x32_bf16 v[134:137], a[196:199], v[62:65], 0
WL 5
v_mfma_f32_16x16x32_bf16 v[138:141], a[192:195], v[66:69], 0
RB 58,16,8384
v_mfma_f32_16x16x32_bf16 v[142:145], a[196:199], v[66:69], 0
WL 5
v_mfma_f32_16x16x32_bf16 v[146:149], a[192:195], v[70:73], 0
RB 62,16,8400
v_mfma_f32_16x16x32_bf16 v[150:153], a[196:199], v[70:73], 0
WL 5
MA 90,200,42
RB 66,16,8416
MA 94,204,42
WL 5
MA 98,200,46
RB 70,16,8432
MA 102,204,46
WL 5
MA 106,200,50
s_waitcnt vmcnt(8)
s_barrier
RB 42,16,16640
s_add_u32 m0, s56, 66560
MA 110,204,50
buffer_load_dwordx4 v31, s[44:47], s55 offen sc1 lds
WL 5
MA 114,200,54
RB 46,16,16656
MA 118,204,54
WL 5
MA 122,200,58
RB 50,16,16672
s_add_u32 m0, s56, 67600
MA 126,204,58
buffer_load_dwordx4 v32, s[44:47], s55 offen sc1 lds
WL 5
MA 130,200,62
RB 54,16,16688
MA 134,204,62
WL 5
MA 138,200,66
RB 58,16,16704
s_add_u32 m0, s56, 68640
MA 142,204,66
buffer_load_dwordx4 v33, s[44:47], s55 offen sc1 lds
WL 5
MA 146,200,70
RB 62,16,16720
MA 150,204,70
WL 5
MA 90,208,42
RB 66,16,16736
s_add_u32 m0, s56, 69680
MA 94,212,42
buffer_load_dwordx4 v34, s[44:47], s55 offen sc1 lds
WL 5
MA 98,208,46
RB 70,16,16752
s_add_u32 s55, s55, 28672
MA 102,212,46
WL 5
MA 106,208,50
RB 42,16,24960
MA 110,212,50
buffer_load_dwordx4 a[128:131], v2, s[36:39], s35 offen offset:0 sc1
WL 5
MA 114,208,54
RB 46,16,24976
MA 118,212,54
WL 5
MA 122,208,58
RB 50,16,24992
MA 126,212,58
buffer_load_dwordx4 a[132:135], v2, s[36:39], s35 offen offset:1024 sc1
WL 5
MA 130,208,62
RB 54,16,25008
s_add_u32 s35, s35, 4096
MA 134,212,62
WL 5
MA 138,208,66
RB 58,16,25024
MA 142,212,66
WL 5
MA 146,208,70
RB 62,16,25040
MA 150,212,70
WL 5
MA 90,216,42
RB 66,16,25056
MA 94,220,42
buffer_load_dwordx4 a[136:139], v2, s[36:39], s35 offen offset:0 sc1
WL 5
MA 98,216,46
RB 70,16,25072
MA 102,220,46
WL 5
MA 106,216,50
s_waitcnt vmcnt(11)
s_barrier
RB 42,16,33280
MA 110,220,50
buffer_load_dwordx4 a[140:143], v2, s[36:39], s35 offen offset:1024 sc1
WL 5
MA 114,216,54
RB 46,16,33296
s_add_u32 s35, s35, 4096
MA 118,220,54
WL 5
MA 122,216,58
RB 50,16,33312
s_add_u32 m0, s56, 83200
MA 126,220,58
buffer_load_dwordx4 v31, s[44:47], s55 offen sc1 lds
WL 5
MA 130,216,62
RB 54,16,33328
MA 134,220,62
WL 5
MA 138,216,66
RB 58,16,33344
s_add_u32 m0, s56, 84240
MA 142,220,66
buffer_load_dwordx4 v32, s[44:47], s55 offen sc1 lds
WL 5
MA 146,216,70
RB 62,16,33360
MA 150,220,70
WL 5
MA 90,224,42
RB 66,16,33376
s_add_u32 m0, s56, 85280
MA 94,228,42
buffer_load_dwordx4 v33, s[44:47], s55 offen sc1 lds
WL 5
MA 98,224,46
RB 70,16,33392
MA 102,228,46
WL 5
MA 106,224,50
RB 42,16,41600
s_add_u32 m0, s56, 86320
MA 110,228,50
buffer_load_dwordx4 v34, s[44:47], s55 offen sc1 lds
WL 5
MA 114,224,54
RB 46,16,41616
s_add_u32 s55, s55, 28672
MA 118,228,54
)"
R"(WL 5
MA 122,224,58
RB 50,16,41632
MA 126,228,58
buffer_load_dwordx4 a[144:147], v2, s[36:39], s35 offen offset:0 sc1
WL 5
MA 130,224,62
RB 54,16,41648
MA 134,228,62
WL 5
MA 138,224,66
RB 58,16,41664
MA 142,228,66
buffer_load_dwordx4 a[148:151], v2, s[36:39], s35 offen offset:1024 sc1
WL 5
MA 146,224,70
RB 62,16,41680
s_add_u32 s35, s35, 4096
MA 150,228,70
WL 5
MA 90,232,42
RB 66,16,41696
MA 94,236,42
buffer_load_dwordx4 a[152:155], v2, s[36:39], s35 offen offset:0 sc1
WL 5
MA 98,232,46
RB 70,16,41712
MA 102,236,46
WL 5
MA 106,232,50
s_waitcnt vmcnt(15)
s_barrier
RB 42,17,0
MA 110,236,50
buffer_load_dwordx4 a[156:159], v2, s[36:39], s35 offen offset:1024 sc1
WL 5
MA 114,232,54
RB 46,17,16
s_add_u32 s35, s35, 4096
MA 118,236,54
WL 5
MA 122,232,58
RB 50,17,32
s_add_u32 m0, s56, 99840
MA 126,236,58
buffer_load_dwordx4 v31, s[44:47], s55 offen sc1 lds
WL 5
MA 130,232,62
RB 54,17,48
MA 134,236,62
WL 5
MA 138,232,66
RB 58,17,64
s_add_u32 m0, s56, 100880
MA 142,236,66
buffer_load_dwordx4 v32, s[44:47], s55 offen sc1 lds
WL 5
MA 146,232,70
RB 62,17,80
MA 150,236,70
WL 5
MA 90,240,42
RB 66,17,96
s_add_u32 m0, s56, 101920
MA 94,244,42
buffer_load_dwordx4 v33, s[44:47], s55 offen sc1 lds
WL 5
MA 98,240,46
RB 70,17,112
MA 102,244,46
WL 5
MA 106,240,50
RB 42,17,8320
s_add_u32 m0, s56, 102960
MA 110,244,50
buffer_load_dwordx4 v34, s[44:47], s55 offen sc1 lds
WL 5
MA 114,240,54
RB 46,17,8336
s_add_u32 s55, s55, 28672
MA 118,244,54
WL 5
MA 122,240,58
RB 50,17,8352
MA 126,244,58
buffer_load_dwordx4 a[160:163], v2, s[36:39], s35 offen offset:0 sc1
WL 5
MA 130,240,62
RB 54,17,8368
MA 134,244,62
WL 5
MA 138,240,66
RB 58,17,8384
MA 142,244,66
buffer_load_dwordx4 a[164:167], v2, s[36:39], s35 offen offset:1024 sc1
WL 5
MA 146,240,70
RB 62,17,8400
s_add_u32 s35, s35, 4096
MA 150,244,70
WL 5
MA 90,248,42
RB 66,17,8416
MA 94,252,42
buffer_load_dwordx4 a[168:171], v2, s[36:39], s35 offen offset:0 sc1
WL 5
MA 98,248,46
RB 70,17,8432
MA 102,252,46
WL 5
MA 106,248,50
s_waitcnt vmcnt(19)
s_barrier
RB 42,17,16640
MA 110,252,50
buffer_load_dwordx4 a[172:175], v2, s[36:39], s35 offen offset:1024 sc1
WL 5
MA 114,248,54
RB 46,17,16656
s_add_u32 s35, s35, 4096
MA 118,252,54
WL 5
MA 122,248,58
RB 50,17,16672
s_add_u32 m0, s56, 116480
MA 126,252,58
buffer_load_dwordx4 v31, s[44:47], s55 offen sc1 lds
WL 5
MA 130,248,62
RB 54,17,16688
MA 134,252,62
WL 5
MA 138,248,66
RB 58,17,16704
s_add_u32 m0, s56, 117520
MA 142,252,66
buffer_load_dwordx4 v32, s[44:47], s55 offen sc1 lds
WL 5
MA 146,248,70
RB 62,17,16720
MA 150,252,70
s_waitcnt vmcnt(20) lgkmcnt(5)
MA 90,128,42
RB 66,17,16736
s_add_u32 m0, s56, 118560
MA 94,132,42
buffer_load_dwordx4 v33, s[44:47], s55 offen sc1 lds
WL 5
MA 98,128,46
RB 70,17,16752
MA 102,132,46
WL 5
MA 106,128,50
RB 42,17,24960
s_add_u32 m0, s56, 119600
MA 110,132,50
buffer_load_dwordx4 v34, s[44:47], s55 offen sc1 lds
WL 5
MA 114,128,54
RB 46,17,24976
s_add_u32 s55, s55, 28672
MA 118,132,54
WL 5
MA 122,128,58
RB 50,17,24992
MA 126,132,58
buffer_load_dwordx4 a[176:179], v2, s[36:39], s35 offen offset:0 sc1
WL 5
MA 130,128,62
RB 54,17,25008
MA 134,132,62
WL 5
MA 138,128,66
RB 58,17,25024
MA 142,132,66
buffer_load_dwordx4 a[180:183], v2, s[36:39], s35 offen offset:1024 sc1
WL 5
MA 146,128,70
RB 62,17,25040
s_add_u32 s35, s35, 4096
MA 150,132,70
s_waitcnt vmcnt(22) lgkmcnt(5)
MA 90,136,42
RB 66,17,25056
MA 94,140,42
buffer_load_dwordx4 a[184:187], v2, s[36:39], s35 offen offset:0 sc1
WL 5
MA 98,136,46
RB 70,17,25072
MA 102,140,46
WL 5
MA 106,136,50
s_waitcnt vmcnt(19)
s_barrier
RB 42,17,33280
MA 110,140,50
buffer_load_dwordx4 a[188:191], v2, s[36:39], s35 offen offset:1024 sc1
WL 5
MA 114,136,54
RB 46,17,33296
s_add_u32 s35, s35, 4096
MA 118,140,54
WL 5
MA 122,136,58
RB 50,17,33312
s_add_u32 m0, s56, 133120
MA 126,140,58
buffer_load_dwordx4 v31, s[44:47], s55 offen sc1 lds
WL 5
MA 130,136,62
RB 54,17,33328
MA 134,140,62
WL 5
MA 138,136,66
RB 58,17,33344
s_add_u32 m0, s56, 134160
MA 142,140,66
buffer_load_dwordx4 v32, s[44:47], s55 offen sc1 lds
WL 5
MA 146,136,70
RB 62,17,33360
MA 150,140,70
s_waitcnt vmcnt(20) lgkmcnt(5)
MA 90,144,42
RB 66,17,33376
s_add_u32 m0, s56, 135200
MA 94,148,42
buffer_load_dwordx4 v33, s[44:47], s55 offen sc1 lds
WL 5
MA 98,144,46
RB 70,17,33392
MA 102,148,46
WL 5
MA 106,144,50
RB 42,17,41600
s_add_u32 m0, s56, 136240
MA 110,148,50
buffer_load_dwordx4 v34, s[44:47], s55 offen sc1 lds
WL 5
MA 114,144,54
RB 46,17,41616
s_add_u32 s55, s55, 28672
MA 118,148,54
WL 5
MA 122,144,58
RB 50,17,41632
MA 126,148,58
buffer_load_dwordx4 a[32:35], v2, s[36:39], s35 offen offset:0 sc1
WL 5
MA 130,144,62
RB 54,17,41648
MA 134,148,62
WL 5
MA 138,144,66
RB 58,17,41664
MA 142,148,66
buffer_load_dwordx4 a[36:39], v2, s[36:39], s35 offen offset:1024 sc1
WL 5
MA 146,144,70
RB 62,17,41680
s_add_u32 s35, s35, 4096
MA 150,148,70
s_waitcnt vmcnt(22) lgkmcnt(5)
MA 90,152,42
RB 66,17,41696
MA 94,156,42
buffer_load_dwordx4 a[40:43], v2, s[36:39], s35 offen offset:0 sc1
WL 5
MA 98,152,46
RB 70,17,41712
MA 102,156,46
WL 5
MA 106,152,50
s_waitcnt vmcnt(19)
s_barrier
RB 42,18,0
MA 110,156,50
buffer_load_dwordx4 a[44:47], v2, s[36:39], s35 offen offset:1024 sc1
WL 5
MA 114,152,54
RB 46,18,16
s_add_u32 s35, s35, 4096
MA 118,156,54
WL 5
MA 122,152,58
RB 50,18,32
s_add_u32 m0, s56, 0
MA 126,156,58
buffer_load_dwordx4 v31, s[44:47], s55 offen sc1 lds
WL 5
MA 130,152,62
RB 54,18,48
MA 134,156,62
WL 5
MA 138,152,66
RB 58,18,64
s_add_u32 m0, s56, 1040
MA 142,156,66
buffer_load_dwordx4 v32, s[44:47], s55 offen sc1 lds
WL 5
MA 146,152,70
RB 62,18,80
MA 150,156,70
s_waitcnt vmcnt(20) lgkmcnt(5)
MA 90,160,42
RB 66,18,96
s_add_u32 m0, s56, 2080
MA 94,164,42
buffer_load_dwordx4 v33, s[44:47], s55 offen sc1 lds
WL 5
MA 98,160,46
RB 70,18,112
MA 102,164,46
WL 5
MA 106,160,50
RB 42,18,8320
s_add_u32 m0, s56, 3120
MA 110,164,50
buffer_load_dwordx4 v34, s[44:47], s55 offen sc1 lds
WL 5
MA 114,160,54
RB 46,18,8336
s_add_u32 s55, s55, 28672
MA 118,164,54
WL 5
MA 122,160,58
RB 50,18,8352
MA 126,164,58
buffer_load_dwordx4 a[48:51], v2, s[36:39], s35 offen offset:0 sc1
WL 5
MA 130,160,62
RB 54,18,8368
MA 134,164,62
WL 5
MA 138,160,66
RB 58,18,8384
MA 142,164,66
buffer_load_dwordx4 a[52:55], v2, s[36:39], s35 offen offset:1024 sc1
WL 5
MA 146,160,70
RB 62,18,8400
s_add_u32 s35, s35, 4096
MA 150,164,70
s_waitcnt vmcnt(22) lgkmcnt(5)
MA 90,168,42
RB 66,18,8416
MA 94,172,42
buffer_load_dwordx4 a[56:59], v2, s[36:39], s35 offen offset:0 sc1
WL 5
MA 98,168,46
RB 70,18,8432
MA 102,172,46
WL 5
MA 106,168,50
s_waitcnt vmcnt(19)
s_barrier
RB 42,18,16640
MA 110,172,50
buffer_load_dwordx4 a[60:63], v2, s[36:39], s35 offen offset:1024 sc1
WL 5
MA 114,168,54
RB 46,18,16656
s_add_u32 s35, s35, 4096
MA 118,172,54
WL 5
MA 122,168,58
RB 50,18,16672
s_add_u32 m0, s56, 16640
MA 126,172,58
buffer_load_dwordx4 v31, s[44:47], s55 offen sc1 lds
WL 5
MA 130,168,62
RB 54,18,16688
MA 134,172,62
WL 5
MA 138,168,66
RB 58,18,16704
s_add_u32 m0, s56, 17680
MA 142,172,66
buffer_load_dwordx4 v32, s[44:47], s55 offen sc1 lds
WL 5
MA 146,168,70
RB 62,18,16720
MA 150,172,70
s_waitcnt vmcnt(20) lgkmcnt(5)
MA 90,176,42
RB 66,18,16736
s_add_u32 m0, s56, 18720
MA 94,180,42
buffer_load_dwordx4 v33, s[44:47], s55 offen sc1 lds
WL 5
MA 98,176,46
RB 70,18,16752
MA 102,180,46
WL 5
MA 106,176,50
RB 42,18,24960
s_add_u32 m0, s56, 19760
MA 110,180,50
buffer_load_dwordx4 v34, s[44:47], s55 offen sc1 lds
)"
R"(WL 5
MA 114,176,54
RB 46,18,24976
s_add_u32 s55, s55, 28672
MA 118,180,54
WL 5
MA 122,176,58
RB 50,18,24992
MA 126,180,58
buffer_load_dwordx4 a[64:67], v2, s[36:39], s35 offen offset:0 sc1
WL 5
MA 130,176,62
RB 54,18,25008
MA 134,180,62
WL 5
MA 138,176,66
RB 58,18,25024
MA 142,180,66
buffer_load_dwordx4 a[68:71], v2, s[36:39], s35 offen offset:1024 sc1
WL 5
MA 146,176,70
RB 62,18,25040
s_add_u32 s35, s35, 4096
MA 150,180,70
s_waitcnt vmcnt(22) lgkmcnt(5)
MA 90,184,42
RB 66,18,25056
MA 94,188,42
buffer_load_dwordx4 a[72:75], v2, s[36:39], s35 offen offset:0 sc1
WL 5
MA 98,184,46
RB 70,18,25072
MA 102,188,46
WL 5
MA 106,184,50
s_waitcnt vmcnt(19)
s_barrier
RB 42,18,33280
MA 110,188,50
buffer_load_dwordx4 a[76:79], v2, s[36:39], s35 offen offset:1024 sc1
WL 5
MA 114,184,54
RB 46,18,33296
s_add_u32 s35, s35, 4096
MA 118,188,54
WL 5
MA 122,184,58
RB 50,18,33312
s_add_u32 m0, s56, 33280
MA 126,188,58
buffer_load_dwordx4 v31, s[44:47], s55 offen sc1 lds
WL 5
MA 130,184,62
RB 54,18,33328
MA 134,188,62
WL 5
MA 138,184,66
RB 58,18,33344
s_add_u32 m0, s56, 34320
MA 142,188,66
buffer_load_dwordx4 v32, s[44:47], s55 offen sc1 lds
WL 5
MA 146,184,70
RB 62,18,33360
MA 150,188,70
s_waitcnt vmcnt(20) lgkmcnt(5)
MA 90,32,42
RB 66,18,33376
s_add_u32 m0, s56, 35360
MA 94,36,42
buffer_load_dwordx4 v33, s[44:47], s55 offen sc1 lds
WL 5
MA 98,32,46
RB 70,18,33392
MA 102,36,46
WL 5
MA 106,32,50
RB 42,18,41600
s_add_u32 m0, s56, 36400
MA 110,36,50
buffer_load_dwordx4 v34, s[44:47], s55 offen sc1 lds
WL 5
MA 114,32,54
RB 46,18,41616
s_add_u32 s55, s55, 28672
MA 118,36,54
WL 5
MA 122,32,58
RB 50,18,41632
MA 126,36,58
buffer_load_dwordx4 a[80:83], v2, s[36:39], s35 offen offset:0 sc1
WL 5
MA 130,32,62
RB 54,18,41648
MA 134,36,62
WL 5
MA 138,32,66
RB 58,18,41664
MA 142,36,66
buffer_load_dwordx4 a[84:87], v2, s[36:39], s35 offen offset:1024 sc1
WL 5
MA 146,32,70
RB 62,18,41680
s_add_u32 s35, s35, 4096
MA 150,36,70
s_waitcnt vmcnt(22) lgkmcnt(5)
MA 90,40,42
RB 66,18,41696
MA 94,44,42
buffer_load_dwordx4 a[88:91], v2, s[36:39], s35 offen offset:0 sc1
WL 5
MA 98,40,46
RB 70,18,41712
MA 102,44,46
WL 5
MA 106,40,50
s_waitcnt vmcnt(19)
s_barrier
RB 42,16,0
MA 110,44,50
buffer_load_dwordx4 a[92:95], v2, s[36:39], s35 offen offset:1024 sc1
WL 5
MA 114,40,54
RB 46,16,16
s_add_u32 s35, s35, 4096
MA 118,44,54
WL 5
MA 122,40,58
RB 50,16,32
s_add_u32 m0, s56, 49920
MA 126,44,58
buffer_load_dwordx4 v31, s[44:47], s55 offen sc1 lds
WL 5
MA 130,40,62
RB 54,16,48
MA 134,44,62
WL 5
MA 138,40,66
RB 58,16,64
s_add_u32 m0, s56, 50960
MA 142,44,66
buffer_load_dwordx4 v32, s[44:47], s55 offen sc1 lds
WL 5
MA 146,40,70
RB 62,16,80
MA 150,44,70
s_waitcnt vmcnt(20) lgkmcnt(5)
MA 90,48,42
RB 66,16,96
s_add_u32 m0, s56, 52000
MA 94,52,42
buffer_load_dwordx4 v33, s[44:47], s55 offen sc1 lds
WL 5
MA 98,48,46
RB 70,16,112
MA 102,52,46
WL 5
MA 106,48,50
RB 42,16,8320
s_add_u32 m0, s56, 53040
MA 110,52,50
buffer_load_dwordx4 v34, s[44:47], s55 offen sc1 lds
WL 5
MA 114,48,54
RB 46,16,8336
s_add_u32 s55, s55, 28672
MA 118,52,54
WL 5
MA 122,48,58
RB 50,16,8352
MA 126,52,58
buffer_load_dwordx4 a[96:99], v2, s[36:39], s35 offen offset:0 sc1
WL 5
MA 130,48,62
RB 54,16,8368
MA 134,52,62
WL 5
MA 138,48,66
RB 58,16,8384
MA 142,52,66
buffer_load_dwordx4 a[100:103], v2, s[36:39], s35 offen offset:1024 sc1
WL 5
MA 146,48,70
RB 62,16,8400
s_add_u32 s35, s35, 4096
MA 150,52,70
s_waitcnt vmcnt(22) lgkmcnt(5)
MA 90,56,42
RB 66,16,8416
MA 94,60,42
buffer_load_dwordx4 a[104:107], v2, s[36:39], s35 offen offset:0 sc1
WL 5
MA 98,56,46
RB 70,16,8432
MA 102,60,46
WL 5
MA 106,56,50
s_waitcnt vmcnt(19)
s_barrier
RB 42,16,16640
MA 110,60,50
buffer_load_dwordx4 a[108:111], v2, s[36:39], s35 offen offset:1024 sc1
WL 5
MA 114,56,54
RB 46,16,16656
s_add_u32 s35, s35, 4096
MA 118,60,54
WL 5
MA 122,56,58
RB 50,16,16672
s_add_u32 m0, s56, 66560
MA 126,60,58
buffer_load_dwordx4 v31, s[44:47], s55 offen sc1 lds
WL 5
MA 130,56,62
RB 54,16,16688
MA 134,60,62
WL 5
MA 138,56,66
RB 58,16,16704
s_add_u32 m0, s56, 67600
MA 142,60,66
buffer_load_dwordx4 v32, s[44:47], s55 offen sc1 lds
WL 5
MA 146,56,70
RB 62,16,16720
MA 150,60,70
s_waitcnt vmcnt(20) lgkmcnt(5)
MA 90,64,42
RB 66,16,16736
s_add_u32 m0, s56, 68640
MA 94,68,42
buffer_load_dwordx4 v33, s[44:47], s55 offen sc1 lds
WL 5
MA 98,64,46
RB 70,16,16752
MA 102,68,46
WL 5
MA 106,64,50
RB 42,16,24960
s_add_u32 m0, s56, 69680
MA 110,68,50
buffer_load_dwordx4 v34, s[44:47], s55 offen sc1 lds
WL 5
MA 114,64,54
RB 46,16,24976
s_add_u32 s55, s55, 28672
MA 118,68,54
WL 5
MA 122,64,58
RB 50,16,24992
MA 126,68,58
buffer_load_dwordx4 a[112:115], v2, s[36:39], s35 offen offset:0 sc1
WL 5
MA 130,64,62
RB 54,16,25008
MA 134,68,62
WL 5
MA 138,64,66
RB 58,16,25024
MA 142,68,66
buffer_load_dwordx4 a[116:119], v2, s[36:39], s35 offen offset:1024 sc1
WL 5
MA 146,64,70
RB 62,16,25040
s_add_u32 s35, s35, 4096
MA 150,68,70
s_waitcnt vmcnt(22) lgkmcnt(5)
MA 90,72,42
RB 66,16,25056
MA 94,76,42
buffer_load_dwordx4 a[120:123], v2, s[36:39], s35 offen offset:0 sc1
WL 5
MA 98,72,46
RB 70,16,25072
MA 102,76,46
WL 5
MA 106,72,50
s_waitcnt vmcnt(19)
s_barrier
RB 42,16,33280
MA 110,76,50
buffer_load_dwordx4 a[124:127], v2, s[36:39], s35 offen offset:1024 sc1
WL 5
MA 114,72,54
RB 46,16,33296
s_add_u32 s35, s35, 4096
MA 118,76,54
WL 5
MA 122,72,58
RB 50,16,33312
s_add_u32 m0, s56, 83200
MA 126,76,58
buffer_load_dwordx4 v31, s[44:47], s55 offen sc1 lds
WL 5
MA 130,72,62
RB 54,16,33328
MA 134,76,62
WL 5
MA 138,72,66
RB 58,16,33344
s_add_u32 m0, s56, 84240
MA 142,76,66
buffer_load_dwordx4 v32, s[44:47], s55 offen sc1 lds
WL 5
MA 146,72,70
RB 62,16,33360
MA 150,76,70
s_waitcnt vmcnt(20) lgkmcnt(5)
MA 90,80,42
RB 66,16,33376
s_add_u32 m0, s56, 85280
MA 94,84,42
buffer_load_dwordx4 v33, s[44:47], s55 offen sc1 lds
WL 5
MA 98,80,46
RB 70,16,33392
MA 102,84,46
WL 5
MA 106,80,50
RB 42,16,41600
s_add_u32 m0, s56, 86320
MA 110,84,50
buffer_load_dwordx4 v34, s[44:47], s55 offen sc1 lds
WL 5
MA 114,80,54
RB 46,16,41616
s_add_u32 s55, s55, 28672
MA 118,84,54
WL 5
MA 122,80,58
RB 50,16,41632
MA 126,84,58
buffer_load_dwordx4 a[0:3], v2, s[36:39], s35 offen offset:0 sc1
WL 5
MA 130,80,62
RB 54,16,41648
MA 134,84,62
WL 5
MA 138,80,66
RB 58,16,41664
MA 142,84,66
buffer_load_dwordx4 a[4:7], v2, s[36:39], s35 offen offset:1024 sc1
WL 5
MA 146,80,70
RB 62,16,41680
s_add_u32 s35, s35, 4096
MA 150,84,70
s_waitcnt vmcnt(22) lgkmcnt(5)
MA 90,88,42
RB 66,16,41696
MA 94,92,42
buffer_load_dwordx4 a[8:11], v2, s[36:39], s35 offen offset:0 sc1
WL 5
MA 98,88,46
RB 70,16,41712
MA 102,92,46
WL 5
MA 106,88,50
s_waitcnt vmcnt(19)
s_barrier
RB 42,17,0
MA 110,92,50
buffer_load_dwordx4 a[12:15], v2, s[36:39], s35 offen offset:1024 sc1
WL 5
MA 114,88,54
RB 46,17,16
s_add_u32 s35, s35, 4096
MA 118,92,54
WL 5
MA 122,88,58
RB 50,17,32
s_add_u32 m0, s56, 99840
MA 126,92,58
buffer_load_dwordx4 v31, s[44:47], s55 offen sc1 lds
WL 5
MA 130,88,62
RB 54,17,48
MA 134,92,62
WL 5
MA 138,88,66
RB 58,17,64
s_add_u32 m0, s56, 100880
MA 142,92,66
buffer_load_dwordx4 v32, s[44:47], s55 offen sc1 lds
WL 5
MA 146,88,70
RB 62,17,80
MA 150,92,70
s_waitcnt vmcnt(20) lgkmcnt(5)
MA 90,96,42
RB 66,17,96
s_add_u32 m0, s56, 101920
MA 94,100,42
buffer_load_dwordx4 v33, s[44:47], s55 offen sc1 lds
WL 5
MA 98,96,46
RB 70,17,112
MA 102,100,46
WL 5
)"
R"(MA 106,96,50
RB 42,17,8320
s_add_u32 m0, s56, 102960
MA 110,100,50
buffer_load_dwordx4 v34, s[44:47], s55 offen sc1 lds
WL 5
MA 114,96,54
RB 46,17,8336
s_mov_b32 s55, 0
MA 118,100,54
WL 5
MA 122,96,58
RB 50,17,8352
MA 126,100,58
buffer_load_dwordx4 a[16:19], v2, s[36:39], s35 offen offset:0 sc1
WL 5
MA 130,96,62
RB 54,17,8368
MA 134,100,62
WL 5
MA 138,96,66
RB 58,17,8384
MA 142,100,66
buffer_load_dwordx4 a[20:23], v2, s[36:39], s35 offen offset:1024 sc1
WL 5
MA 146,96,70
RB 62,17,8400
s_add_u32 s35, s35, 4096
MA 150,100,70
s_waitcnt vmcnt(22) lgkmcnt(5)
MA 90,104,42
RB 66,17,8416
MA 94,108,42
buffer_load_dwordx4 a[24:27], v2, s[36:39], s35 offen offset:0 sc1
WL 5
MA 98,104,46
RB 70,17,8432
MA 102,108,46
WL 5
MA 106,104,50
s_waitcnt vmcnt(19)
s_barrier
RB 42,17,16640
MA 110,108,50
buffer_load_dwordx4 a[28:31], v2, s[36:39], s35 offen offset:1024 sc1
WL 5
MA 114,104,54
RB 46,17,16656
s_add_u32 s35, s35, 4096
MA 118,108,54
WL 5
MA 122,104,58
RB 50,17,16672
s_add_u32 m0, s57, 116480
MA 126,108,58
buffer_load_dwordx4 v35, s[44:47], s55 offen sc1 lds
WL 5
MA 130,104,62
RB 54,17,16688
MA 134,108,62
WL 5
MA 138,104,66
RB 58,17,16704
s_add_u32 m0, s57, 117520
MA 142,108,66
buffer_load_dwordx4 v36, s[44:47], s55 offen sc1 lds
WL 5
MA 146,104,70
RB 62,17,16720
MA 150,108,70
s_waitcnt vmcnt(20) lgkmcnt(5)
MA 90,112,42
RB 66,17,16736
s_add_u32 m0, s57, 118560
MA 94,116,42
buffer_load_dwordx4 v37, s[44:47], s55 offen sc1 lds
WL 5
MA 98,112,46
RB 70,17,16752
MA 102,116,46
WL 5
MA 106,112,50
RB 42,17,24960
s_add_u32 m0, s57, 119600
MA 110,116,50
buffer_load_dwordx4 v38, s[44:47], s55 offen sc1 lds
WL 5
MA 114,112,54
RB 46,17,24976
s_add_u32 s55, s55, 57344
MA 118,116,54
WL 5
MA 122,112,58
RB 50,17,24992
s_add_u32 m0, s57, 133120
MA 126,116,58
buffer_load_dwordx4 v35, s[44:47], s55 offen sc1 lds
WL 5
MA 130,112,62
RB 54,17,25008
MA 134,116,62
WL 5
MA 138,112,66
RB 58,17,25024
s_add_u32 m0, s57, 134160
MA 142,116,66
buffer_load_dwordx4 v36, s[44:47], s55 offen sc1 lds
WL 5
MA 146,112,70
RB 62,17,25040
MA 150,116,70
s_waitcnt vmcnt(22) lgkmcnt(5)
MA 90,120,42
RB 66,17,25056
s_add_u32 m0, s57, 135200
MA 94,124,42
buffer_load_dwordx4 v37, s[44:47], s55 offen sc1 lds
WL 5
MA 98,120,46
RB 70,17,25072
MA 102,124,46
WL 5
MA 106,120,50
s_waitcnt vmcnt(19)
s_barrier
RB 42,17,33280
s_add_u32 m0, s57, 136240
MA 110,124,50
buffer_load_dwordx4 v38, s[44:47], s55 offen sc1 lds
WL 5
MA 114,120,54
RB 46,17,33296
s_add_u32 s55, s55, 57344
MA 118,124,54
WL 5
MA 122,120,58
RB 50,17,33312
s_add_u32 m0, s57, 0
MA 126,124,58
buffer_load_dwordx4 v35, s[44:47], s55 offen sc1 lds
WL 5
MA 130,120,62
RB 54,17,33328
MA 134,124,62
WL 5
MA 138,120,66
RB 58,17,33344
s_add_u32 m0, s57, 1040
MA 142,124,66
buffer_load_dwordx4 v36, s[44:47], s55 offen sc1 lds
WL 5
MA 146,120,70
RB 62,17,33360
MA 150,124,70
s_waitcnt vmcnt(20) lgkmcnt(5)
MA 90,0,42
RB 66,17,33376
s_add_u32 m0, s57, 2080
MA 94,4,42
buffer_load_dwordx4 v37, s[44:47], s55 offen sc1 lds
WL 5
MA 98,0,46
RB 70,17,33392
MA 102,4,46
WL 5
MA 106,0,50
RB 42,17,41600
s_add_u32 m0, s57, 3120
MA 110,4,50
buffer_load_dwordx4 v38, s[44:47], s55 offen sc1 lds
WL 5
MA 114,0,54
RB 46,17,41616
s_add_u32 s55, s55, 57344
MA 118,4,54
WL 5
MA 122,0,58
RB 50,17,41632
s_add_u32 m0, s57, 16640
MA 126,4,58
buffer_load_dwordx4 v35, s[44:47], s55 offen sc1 lds
WL 5
MA 130,0,62
RB 54,17,41648
MA 134,4,62
WL 5
MA 138,0,66
RB 58,17,41664
s_add_u32 m0, s57, 17680
MA 142,4,66
buffer_load_dwordx4 v36, s[44:47], s55 offen sc1 lds
WL 5
MA 146,0,70
RB 62,17,41680
MA 150,4,70
s_waitcnt vmcnt(22) lgkmcnt(5)
MA 90,8,42
RB 66,17,41696
s_add_u32 m0, s57, 18720
MA 94,12,42
buffer_load_dwordx4 v37, s[44:47], s55 offen sc1 lds
WL 5
MA 98,8,46
RB 70,17,41712
MA 102,12,46
WL 5
MA 106,8,50
s_waitcnt vmcnt(19)
s_barrier
RB 42,18,0
s_add_u32 m0, s57, 19760
MA 110,12,50
buffer_load_dwordx4 v38, s[44:47], s55 offen sc1 lds
WL 5
MA 114,8,54
RB 46,18,16
s_add_u32 s55, s55, 57344
MA 118,12,54
WL 5
MA 122,8,58
RB 50,18,32
s_add_u32 m0, s57, 33280
MA 126,12,58
buffer_load_dwordx4 v35, s[44:47], s55 offen sc1 lds
WL 5
MA 130,8,62
RB 54,18,48
MA 134,12,62
WL 5
MA 138,8,66
RB 58,18,64
s_add_u32 m0, s57, 34320
MA 142,12,66
buffer_load_dwordx4 v36, s[44:47], s55 offen sc1 lds
WL 5
MA 146,8,70
RB 62,18,80
MA 150,12,70
s_waitcnt vmcnt(20) lgkmcnt(5)
MA 90,16,42
RB 66,18,96
s_add_u32 m0, s57, 35360
MA 94,20,42
buffer_load_dwordx4 v37, s[44:47], s55 offen sc1 lds
WL 5
MA 98,16,46
RB 70,18,112
MA 102,20,46
WL 5
MA 106,16,50
RB 42,18,8320
s_add_u32 m0, s57, 36400
MA 110,20,50
buffer_load_dwordx4 v38, s[44:47], s55 offen sc1 lds
WL 5
MA 114,16,54
RB 46,18,8336
s_add_u32 s55, s55, 57344
MA 118,20,54
WL 5
MA 122,16,58
RB 50,18,8352
s_add_u32 m0, s57, 49920
MA 126,20,58
buffer_load_dwordx4 v35, s[44:47], s55 offen sc1 lds
WL 5
MA 130,16,62
RB 54,18,8368
MA 134,20,62
WL 5
MA 138,16,66
RB 58,18,8384
s_add_u32 m0, s57, 50960
MA 142,20,66
buffer_load_dwordx4 v36, s[44:47], s55 offen sc1 lds
WL 5
MA 146,16,70
RB 62,18,8400
MA 150,20,70
s_waitcnt vmcnt(22) lgkmcnt(5)
MA 90,24,42
RB 66,18,8416
s_add_u32 m0, s57, 52000
MA 94,28,42
buffer_load_dwordx4 v37, s[44:47], s55 offen sc1 lds
WL 5
MA 98,24,46
RB 70,18,8432
MA 102,28,46
WL 5
MA 106,24,50
s_waitcnt vmcnt(19)
s_barrier
RB 42,21,16640
s_add_u32 m0, s57, 53040
MA 110,28,50
buffer_load_dwordx4 v38, s[44:47], s55 offen sc1 lds
WL 5
MA 114,24,54
RB 46,21,16656
s_add_u32 s55, s55, 57344
MA 118,28,54
WL 5
MA 122,24,58
RB 50,21,16672
s_add_u32 m0, s57, 66560
MA 126,28,58
buffer_load_dwordx4 v35, s[44:47], s55 offen sc1 lds
WL 5
MA 130,24,62
RB 54,21,16688
MA 134,28,62
WL 5
MA 138,24,66
RB 58,21,20800
s_add_u32 m0, s57, 67600
MA 142,28,66
buffer_load_dwordx4 v36, s[44:47], s55 offen sc1 lds
WL 5
MA 146,24,70
RB 62,21,20816
MA 150,28,70
WL 5
v_mfma_f32_16x16x32_bf16 v[154:157], a[192:195], v[42:45], 0
RB 66,21,20832
s_add_u32 m0, s57, 68640
v_mfma_f32_16x16x32_bf16 v[158:161], a[196:199], v[42:45], 0
buffer_load_dwordx4 v37, s[44:47], s55 offen sc1 lds
WL 5
v_mfma_f32_16x16x32_bf16 v[162:165], a[192:195], v[46:49], 0
RB 70,21,20848
v_mfma_f32_16x16x32_bf16 v[166:169], a[196:199], v[46:49], 0
WL 5
v_mfma_f32_16x16x32_bf16 v[170:173], a[192:195], v[50:53], 0
RB 42,21,24960
s_add_u32 m0, s57, 69680
v_mfma_f32_16x16x32_bf16 v[174:177], a[196:199], v[50:53], 0
buffer_load_dwordx4 v38, s[44:47], s55 offen sc1 lds
WL 5
v_mfma_f32_16x16x32_bf16 v[178:181], a[192:195], v[54:57], 0
RB 46,21,24976
s_add_u32 s55, s55, 57344
v_mfma_f32_16x16x32_bf16 v[182:185], a[196:199], v[54:57], 0
WL 5
MA 154,200,58
RB 50,21,24992
s_add_u32 m0, s57, 83200
MA 158,204,58
buffer_load_dwordx4 v35, s[44:47], s55 offen sc1 lds
WL 5
MA 162,200,62
RB 54,21,25008
MA 166,204,62
WL 5
MA 170,200,66
RB 58,21,29120
s_add_u32 m0, s57, 84240
MA 174,204,66
buffer_load_dwordx4 v36, s[44:47], s55 offen sc1 lds
WL 5
MA 178,200,70
RB 62,21,29136
MA 182,204,70
WL 5
MA 154,208,42
RB 66,21,29152
s_add_u32 m0, s57, 85280
MA 158,212,42
buffer_load_dwordx4 v37, s[44:47], s55 offen sc1 lds
WL 5
MA 162,208,46
RB 70,21,29168
MA 166,212,46
WL 5
MA 170,208,50
s_waitcnt vmcnt(23)
s_barrier
RB 42,21,33280
s_add_u32 m0, s57, 86320
MA 174,212,50
buffer_load_dwordx4 v38, s[44:47], s55 offen sc1 lds
WL 5
MA 178,208,54
RB 46,21,33296
s_mov_b32 s55, 0
MA 182,212,54
WL 5
MA 154,216,58
RB 50,21,33312
s_add_u32 m0, s58, 99840
MA 158,220,58
buffer_load_dwordx4 v39, s[44:47], s55 offen sc1 lds
WL 5
MA 162,216,62
RB 54,21,33328
MA 166,220,62
WL 5
MA 170,216,66
RB 58,21,37440
s_add_u32 m0, s58, 100880
)"
R"(MA 174,220,66
buffer_load_dwordx4 v40, s[44:47], s55 offen sc1 lds
WL 5
MA 178,216,70
RB 62,21,37456
s_add_u32 s55, s55, 57344
MA 182,220,70
WL 5
MA 154,224,42
RB 66,21,37472
MA 158,228,42
WL 5
MA 162,224,46
RB 70,21,37488
MA 166,228,46
WL 5
MA 170,224,50
RB 42,21,41600
MA 174,228,50
WL 5
MA 178,224,54
RB 46,21,41616
MA 182,228,54
WL 5
MA 154,232,58
RB 50,21,41632
MA 158,236,58
WL 5
MA 162,232,62
RB 54,21,41648
MA 166,236,62
WL 5
MA 170,232,66
RB 58,21,45760
MA 174,236,66
WL 5
MA 178,232,70
RB 62,21,45776
MA 182,236,70
WL 5
MA 154,240,42
RB 66,21,45792
MA 158,244,42
WL 5
MA 162,240,46
RB 70,21,45808
MA 166,244,46
WL 5
MA 170,240,50
s_waitcnt vmcnt(22)
s_barrier
RB 42,19,0
s_add_u32 m0, s58, 116480
MA 174,244,50
buffer_load_dwordx4 v39, s[44:47], s55 offen sc1 lds
WL 5
MA 178,240,54
RB 46,19,16
MA 182,244,54
WL 5
MA 154,248,58
RB 50,19,32
s_add_u32 m0, s58, 117520
MA 158,252,58
buffer_load_dwordx4 v40, s[44:47], s55 offen sc1 lds
WL 5
MA 162,248,62
RB 54,19,48
s_add_u32 s55, s55, 57344
MA 166,252,62
WL 5
MA 170,248,66
RB 58,19,4160
MA 174,252,66
WL 5
MA 178,248,70
RB 62,19,4176
MA 182,252,70
WL 5
MA 154,128,42
RB 66,19,4192
MA 158,132,42
WL 5
MA 162,128,46
RB 70,19,4208
MA 166,132,46
WL 5
MA 170,128,50
RB 42,19,8320
MA 174,132,50
WL 5
MA 178,128,54
RB 46,19,8336
MA 182,132,54
WL 5
MA 154,136,58
RB 50,19,8352
MA 158,140,58
WL 5
MA 162,136,62
RB 54,19,8368
MA 166,140,62
WL 5
MA 170,136,66
RB 58,19,12480
MA 174,140,66
WL 5
MA 178,136,70
RB 62,19,12496
MA 182,140,70
WL 5
MA 154,144,42
RB 66,19,12512
MA 158,148,42
WL 5
MA 162,144,46
RB 70,19,12528
MA 166,148,46
WL 5
MA 170,144,50
s_waitcnt vmcnt(20)
s_barrier
RB 42,19,16640
s_add_u32 m0, s58, 133120
MA 174,148,50
buffer_load_dwordx4 v39, s[44:47], s55 offen sc1 lds
WL 5
MA 178,144,54
RB 46,19,16656
MA 182,148,54
WL 5
MA 154,152,58
RB 50,19,16672
s_add_u32 m0, s58, 134160
MA 158,156,58
buffer_load_dwordx4 v40, s[44:47], s55 offen sc1 lds
WL 5
MA 162,152,62
RB 54,19,16688
s_add_u32 s55, s55, 57344
MA 166,156,62
WL 5
MA 170,152,66
RB 58,19,20800
MA 174,156,66
WL 5
MA 178,152,70
RB 62,19,20816
MA 182,156,70
WL 5
MA 154,160,42
RB 66,19,20832
MA 158,164,42
WL 5
MA 162,160,46
RB 70,19,20848
MA 166,164,46
WL 5
MA 170,160,50
RB 42,19,24960
MA 174,164,50
WL 5
MA 178,160,54
RB 46,19,24976
MA 182,164,54
WL 5
MA 154,168,58
RB 50,19,24992
MA 158,172,58
WL 5
MA 162,168,62
RB 54,19,25008
MA 166,172,62
WL 5
MA 170,168,66
RB 58,19,29120
MA 174,172,66
WL 5
MA 178,168,70
RB 62,19,29136
MA 182,172,70
WL 5
MA 154,176,42
RB 66,19,29152
v_cvt_pk_bf16_f32 v74, v90, v98
MA 158,180,42
v_cvt_pk_bf16_f32 v75, v106, v114
v_cvt_pk_bf16_f32 v76, v122, v130
WL 5
MA 162,176,46
RB 70,19,29168
v_cvt_pk_bf16_f32 v77, v138, v146
MA 166,180,46
s_add_u32 s53, s60, 0
buffer_store_dwordx4 v[74:77], v25, s[48:51], s53 offen sc1 nt
WL 5
MA 170,176,50
s_waitcnt vmcnt(19)
s_barrier
RB 42,19,33280
v_cvt_pk_bf16_f32 v78, v91, v99
MA 174,180,50
v_cvt_pk_bf16_f32 v79, v107, v115
v_cvt_pk_bf16_f32 v80, v123, v131
WL 5
MA 178,176,54
RB 46,19,33296
s_add_u32 m0, s58, 0
MA 182,180,54
buffer_load_dwordx4 v39, s[44:47], s55 offen sc1 lds
WL 5
MA 154,184,58
RB 50,19,33312
v_cvt_pk_bf16_f32 v81, v139, v147
MA 158,188,58
s_add_u32 s53, s60, 7168
WL 5
MA 162,184,62
RB 54,19,33328
s_add_u32 m0, s58, 1040
MA 166,188,62
buffer_load_dwordx4 v40, s[44:47], s55 offen sc1 lds
WL 5
MA 170,184,66
RB 58,19,37440
s_add_u32 s55, s55, 57344
MA 174,188,66
WL 5
MA 178,184,70
RB 62,19,37456
MA 182,188,70
buffer_store_dwordx4 v[78:81], v25, s[48:51], s53 offen sc1 nt
WL 5
MA 154,32,42
RB 66,19,37472
v_cvt_pk_bf16_f32 v82, v92, v100
MA 158,36,42
v_cvt_pk_bf16_f32 v83, v108, v116
v_cvt_pk_bf16_f32 v84, v124, v132
WL 5
MA 162,32,46
RB 70,19,37488
v_cvt_pk_bf16_f32 v85, v140, v148
MA 166,36,46
s_add_u32 s53, s60, 14336
buffer_store_dwordx4 v[82:85], v25, s[48:51], s53 offen sc1 nt
WL 5
MA 170,32,50
RB 42,19,41600
v_cvt_pk_bf16_f32 v86, v93, v101
MA 174,36,50
v_cvt_pk_bf16_f32 v87, v109, v117
v_cvt_pk_bf16_f32 v88, v125, v133
WL 5
MA 178,32,54
RB 46,19,41616
v_cvt_pk_bf16_f32 v89, v141, v149
MA 182,36,54
s_add_u32 s53, s60, 21504
buffer_store_dwordx4 v[86:89], v25, s[48:51], s53 offen sc1 nt
WL 5
MA 154,40,58
RB 50,19,41632
v_cvt_pk_bf16_f32 v74, v94, v102
MA 158,44,58
v_cvt_pk_bf16_f32 v75, v110, v118
v_cvt_pk_bf16_f32 v76, v126, v134
WL 5
MA 162,40,62
RB 54,19,41648
v_cvt_pk_bf16_f32 v77, v142, v150
MA 166,44,62
s_add_u32 s53, s60, 114688
buffer_store_dwordx4 v[74:77], v25, s[48:51], s53 offen sc1 nt
WL 5
MA 170,40,66
RB 58,19,45760
v_cvt_pk_bf16_f32 v78, v95, v103
MA 174,44,66
v_cvt_pk_bf16_f32 v79, v111, v119
v_cvt_pk_bf16_f32 v80, v127, v135
WL 5
MA 178,40,70
RB 62,19,45776
v_cvt_pk_bf16_f32 v81, v143, v151
MA 182,44,70
s_add_u32 s53, s60, 121856
buffer_store_dwordx4 v[78:81], v25, s[48:51], s53 offen sc1 nt
WL 5
MA 154,48,42
RB 66,19,45792
v_cvt_pk_bf16_f32 v82, v96, v104
MA 158,52,42
v_cvt_pk_bf16_f32 v83, v112, v120
v_cvt_pk_bf16_f32 v84, v128, v136
WL 5
MA 162,48,46
RB 70,19,45808
v_cvt_pk_bf16_f32 v85, v144, v152
MA 166,52,46
s_add_u32 s53, s60, 129024
buffer_store_dwordx4 v[82:85], v25, s[48:51], s53 offen sc1 nt
WL 5
MA 170,48,50
s_waitcnt vmcnt(23)
s_barrier
RB 42,20,0
v_cvt_pk_bf16_f32 v86, v97, v105
MA 174,52,50
v_cvt_pk_bf16_f32 v87, v113, v121
v_cvt_pk_bf16_f32 v88, v129, v137
WL 5
MA 178,48,54
RB 46,20,16
s_add_u32 m0, s58, 16640
MA 182,52,54
buffer_load_dwordx4 v39, s[44:47], s55 offen sc1 lds
WL 5
MA 154,56,58
RB 50,20,32
v_cvt_pk_bf16_f32 v89, v145, v153
MA 158,60,58
s_add_u32 s53, s60, 136192
WL 5
MA 162,56,62
RB 54,20,48
s_add_u32 m0, s58, 17680
MA 166,60,62
buffer_load_dwordx4 v40, s[44:47], s55 offen sc1 lds
WL 5
MA 170,56,66
RB 58,20,4160
s_add_u32 s55, s55, 57344
MA 174,60,66
WL 5
MA 178,56,70
RB 62,20,4176
MA 182,60,70
buffer_store_dwordx4 v[86:89], v25, s[48:51], s53 offen sc1 nt
WL 5
MA 154,64,42
RB 66,20,4192
MA 158,68,42
WL 5
MA 162,64,46
RB 70,20,4208
MA 166,68,46
WL 5
MA 170,64,50
RB 42,20,8320
MA 174,68,50
WL 5
MA 178,64,54
RB 46,20,8336
MA 182,68,54
WL 5
MA 154,72,58
RB 50,20,8352
MA 158,76,58
WL 5
MA 162,72,62
RB 54,20,8368
MA 166,76,62
WL 5
MA 170,72,66
RB 58,20,12480
MA 174,76,66
WL 5
MA 178,72,70
RB 62,20,12496
MA 182,76,70
WL 5
MA 154,80,42
RB 66,20,12512
MA 158,84,42
WL 5
MA 162,80,46
RB 70,20,12528
MA 166,84,46
WL 5
MA 170,80,50
s_waitcnt vmcnt(22)
s_barrier
RB 42,20,16640
s_add_u32 m0, s58, 33280
MA 174,84,50
buffer_load_dwordx4 v39, s[44:47], s55 offen sc1 lds
WL 5
MA 178,80,54
RB 46,20,16656
MA 182,84,54
WL 5
MA 154,88,58
RB 50,20,16672
s_add_u32 m0, s58, 34320
MA 158,92,58
buffer_load_dwordx4 v40, s[44:47], s55 offen sc1 lds
WL 5
MA 162,88,62
RB 54,20,16688
)"
R"(s_add_u32 s55, s55, 57344
MA 166,92,62
WL 5
MA 170,88,66
RB 58,20,20800
MA 174,92,66
WL 5
MA 178,88,70
RB 62,20,20816
MA 182,92,70
WL 5
MA 154,96,42
RB 66,20,20832
MA 158,100,42
WL 5
MA 162,96,46
RB 70,20,20848
MA 166,100,46
WL 5
MA 170,96,50
RB 42,20,24960
MA 174,100,50
WL 5
MA 178,96,54
RB 46,20,24976
MA 182,100,54
WL 5
MA 154,104,58
RB 50,20,24992
MA 158,108,58
WL 5
MA 162,104,62
RB 54,20,25008
MA 166,108,62
WL 5
MA 170,104,66
RB 58,20,29120
MA 174,108,66
WL 5
MA 178,104,70
RB 62,20,29136
MA 182,108,70
WL 5
MA 154,112,42
RB 66,20,29152
MA 158,116,42
WL 5
MA 162,112,46
RB 70,20,29168
MA 166,116,46
WL 5
MA 170,112,50
s_waitcnt vmcnt(20)
s_barrier
RB 42,20,33280
s_add_u32 m0, s58, 49920
MA 174,116,50
buffer_load_dwordx4 v39, s[44:47], s55 offen sc1 lds
WL 5
MA 178,112,54
RB 46,20,33296
MA 182,116,54
WL 5
MA 154,120,58
RB 50,20,33312
s_add_u32 m0, s58, 50960
MA 158,124,58
buffer_load_dwordx4 v40, s[44:47], s55 offen sc1 lds
WL 5
MA 162,120,62
RB 54,20,33328
s_add_u32 s55, s55, 57344
MA 166,124,62
WL 5
MA 170,120,66
RB 58,20,37440
MA 174,124,66
WL 5
MA 178,120,70
RB 62,20,37456
MA 182,124,70
WL 5
MA 154,0,42
RB 66,20,37472
MA 158,4,42
WL 5
MA 162,0,46
RB 70,20,37488
MA 166,4,46
WL 5
MA 170,0,50
RB 42,20,41600
MA 174,4,50
WL 5
MA 178,0,54
RB 46,20,41616
MA 182,4,54
WL 5
MA 154,8,58
RB 50,20,41632
MA 158,12,58
WL 5
MA 162,8,62
RB 54,20,41648
MA 166,12,62
WL 5
MA 170,8,66
RB 58,20,45760
MA 174,12,66
WL 5
MA 178,8,70
RB 62,20,45776
MA 182,12,70
WL 5
MA 154,16,42
RB 66,20,45792
MA 158,20,42
WL 5
MA 162,16,46
RB 70,20,45808
MA 166,20,46
WL 5
MA 170,16,50
s_waitcnt vmcnt(20)
s_barrier
RB 42,24,0
s_add_u32 m0, s58, 66560
MA 174,20,50
buffer_load_dwordx4 v39, s[44:47], s55 offen sc1 lds
WL 5
MA 178,16,54
RB 46,24,16
MA 182,20,54
WL 5
MA 154,24,58
RB 50,24,2080
s_add_u32 m0, s58, 67600
MA 158,28,58
buffer_load_dwordx4 v40, s[44:47], s55 offen sc1 lds
WL 5
MA 162,24,62
RB 54,24,2096
s_mov_b32 s55, 0
MA 166,28,62
WL 5
MA 170,24,66
RB 58,24,4160
MA 174,28,66
WL 5
MA 178,24,70
RB 62,24,4176
MA 182,28,70
WL 5
v_mfma_f32_16x16x32_bf16 v[186:189], a[192:195], v[42:45], 0
RB 66,24,6240
v_mfma_f32_16x16x32_bf16 v[190:193], a[196:199], v[42:45], 0
WL 5
v_mfma_f32_16x16x32_bf16 v[194:197], a[192:195], v[46:49], 0
RB 70,24,6256
v_mfma_f32_16x16x32_bf16 v[198:201], a[196:199], v[46:49], 0
WL 5
MA 186,200,50
s_waitcnt vmcnt(20)
s_barrier
RB 42,24,16640
MA 190,204,50
WL 5
MA 194,200,54
RB 46,24,16656
MA 198,204,54
WL 5
MA 186,208,58
RB 50,24,18720
MA 190,212,58
WL 5
MA 194,208,62
RB 54,24,18736
MA 198,212,62
WL 5
MA 186,216,66
RB 58,24,20800
MA 190,220,66
WL 5
MA 194,216,70
RB 62,24,20816
MA 198,220,70
WL 5
MA 186,224,42
RB 66,24,22880
v_cvt_pk_bf16_f32 v74, v154, v162
MA 190,228,42
v_cvt_pk_bf16_f32 v75, v170, v178
s_add_u32 s53, s60, 0
WL 5
MA 194,224,46
RB 70,24,22896
MA 198,228,46
buffer_store_dwordx2 v[74:75], v26, s[48:51], s53 offen sc1 nt
WL 5
MA 186,232,50
s_waitcnt vmcnt(19)
s_barrier
RB 42,24,33280
v_cvt_pk_bf16_f32 v78, v155, v163
MA 190,236,50
v_cvt_pk_bf16_f32 v79, v171, v179
s_add_u32 s53, s60, 7168
WL 5
MA 194,232,54
RB 46,24,33296
MA 198,236,54
buffer_store_dwordx2 v[78:79], v26, s[48:51], s53 offen sc1 nt
WL 5
MA 186,240,58
RB 50,24,35360
v_cvt_pk_bf16_f32 v82, v156, v164
MA 190,244,58
v_cvt_pk_bf16_f32 v83, v172, v180
s_add_u32 s53, s60, 14336
WL 5
MA 194,240,62
RB 54,24,35376
MA 198,244,62
buffer_store_dwordx2 v[82:83], v26, s[48:51], s53 offen sc1 nt
WL 5
MA 186,248,66
RB 58,24,37440
v_cvt_pk_bf16_f32 v86, v157, v165
MA 190,252,66
v_cvt_pk_bf16_f32 v87, v173, v181
s_add_u32 s53, s60, 21504
WL 5
MA 194,248,70
RB 62,24,37456
MA 198,252,70
buffer_store_dwordx2 v[86:87], v26, s[48:51], s53 offen sc1 nt
WL 5
MA 186,128,42
RB 66,24,39520
v_cvt_pk_bf16_f32 v74, v158, v166
MA 190,132,42
v_cvt_pk_bf16_f32 v75, v174, v182
s_add_u32 s53, s60, 114688
WL 5
MA 194,128,46
RB 70,24,39536
MA 198,132,46
buffer_store_dwordx2 v[74:75], v26, s[48:51], s53 offen sc1 nt
WL 5
MA 186,136,50
s_waitcnt vmcnt(20)
s_barrier
RB 42,22,0
v_cvt_pk_bf16_f32 v78, v159, v167
MA 190,140,50
v_cvt_pk_bf16_f32 v79, v175, v183
s_add_u32 s53, s60, 121856
WL 5
MA 194,136,54
RB 46,22,16
MA 198,140,54
buffer_store_dwordx2 v[78:79], v26, s[48:51], s53 offen sc1 nt
WL 5
MA 186,144,58
RB 50,22,2080
v_cvt_pk_bf16_f32 v82, v160, v168
MA 190,148,58
v_cvt_pk_bf16_f32 v83, v176, v184
s_add_u32 s53, s60, 129024
WL 5
MA 194,144,62
RB 54,22,2096
MA 198,148,62
buffer_store_dwordx2 v[82:83], v26, s[48:51], s53 offen sc1 nt
WL 5
MA 186,152,66
RB 58,22,4160
v_cvt_pk_bf16_f32 v86, v161, v169
MA 190,156,66
v_cvt_pk_bf16_f32 v87, v177, v185
s_add_u32 s53, s60, 136192
WL 5
MA 194,152,70
RB 62,22,4176
MA 198,156,70
buffer_store_dwordx2 v[86:87], v26, s[48:51], s53 offen sc1 nt
WL 5
MA 186,160,42
RB 66,22,6240
MA 190,164,42
WL 5
MA 194,160,46
RB 70,22,6256
MA 198,164,46
WL 5
MA 186,168,50
s_waitcnt vmcnt(15)
s_barrier
RB 42,22,16640
MA 190,172,50
WL 5
MA 194,168,54
RB 46,22,16656
MA 198,172,54
WL 5
MA 186,176,58
RB 50,22,18720
MA 190,180,58
WL 5
MA 194,176,62
RB 54,22,18736
MA 198,180,62
WL 5
MA 186,184,66
RB 58,22,20800
MA 190,188,66
WL 5
MA 194,184,70
RB 62,22,20816
MA 198,188,70
WL 5
MA 186,32,42
RB 66,22,22880
MA 190,36,42
WL 5
MA 194,32,46
RB 70,22,22896
MA 198,36,46
WL 5
MA 186,40,50
s_waitcnt vmcnt(12)
s_barrier
RB 42,22,33280
MA 190,44,50
WL 5
MA 194,40,54
RB 46,22,33296
MA 198,44,54
WL 5
MA 186,48,58
RB 50,22,35360
MA 190,52,58
WL 5
MA 194,48,62
RB 54,22,35376
MA 198,52,62
WL 5
MA 186,56,66
RB 58,22,37440
MA 190,60,66
WL 5
MA 194,56,70
RB 62,22,37456
MA 198,60,70
WL 5
MA 186,64,42
RB 66,22,39520
MA 190,68,42
WL 5
MA 194,64,46
RB 70,22,39536
MA 198,68,46
WL 5
MA 186,72,50
s_waitcnt vmcnt(10)
s_barrier
RB 42,23,0
MA 190,76,50
WL 5
MA 194,72,54
RB 46,23,16
MA 198,76,54
WL 5
MA 186,80,58
RB 50,23,2080
MA 190,84,58
WL 5
MA 194,80,62
RB 54,23,2096
MA 198,84,62
WL 5
MA 186,88,66
RB 58,23,4160
MA 190,92,66
WL 5
MA 194,88,70
RB 62,23,4176
MA 198,92,70
WL 5
MA 186,96,42
RB 66,23,6240
MA 190,100,42
WL 5
MA 194,96,46
RB 70,23,6256
MA 198,100,46
WL 5
MA 186,104,50
s_waitcnt vmcnt(8)
s_barrier
)"
R"(RB 42,23,16640
MA 190,108,50
WL 5
MA 194,104,54
RB 46,23,16656
MA 198,108,54
WL 5
MA 186,112,58
RB 50,23,18720
MA 190,116,58
WL 5
MA 194,112,62
RB 54,23,18736
MA 198,116,62
WL 5
MA 186,120,66
RB 58,23,20800
MA 190,124,66
WL 5
MA 194,120,70
RB 62,23,20816
MA 198,124,70
WL 5
MA 186,0,42
RB 66,23,22880
MA 190,4,42
WL 5
MA 194,0,46
RB 70,23,22896
MA 198,4,46
WL 5
MA 186,8,50
MA 190,12,50
WL 4
MA 194,8,54
MA 198,12,54
WL 3
MA 186,16,58
MA 190,20,58
WL 2
MA 194,16,62
MA 198,20,62
WL 1
MA 186,24,66
MA 190,28,66
WL 0
MA 194,24,70
MA 198,28,70
s_nop 7
s_nop 1
v_cvt_pk_bf16_f32 v74, v186, v194
s_add_u32 s53, s60, 0
buffer_store_dword v74, v27, s[48:51], s53 offen sc1 nt
v_cvt_pk_bf16_f32 v78, v187, v195
s_add_u32 s53, s60, 7168
buffer_store_dword v78, v27, s[48:51], s53 offen sc1 nt
v_cvt_pk_bf16_f32 v82, v188, v196
s_add_u32 s53, s60, 14336
buffer_store_dword v82, v27, s[48:51], s53 offen sc1 nt
v_cvt_pk_bf16_f32 v86, v189, v197
s_add_u32 s53, s60, 21504
buffer_store_dword v86, v27, s[48:51], s53 offen sc1 nt
v_cvt_pk_bf16_f32 v74, v190, v198
s_add_u32 s53, s60, 114688
buffer_store_dword v74, v27, s[48:51], s53 offen sc1 nt
v_cvt_pk_bf16_f32 v78, v191, v199
s_add_u32 s53, s60, 121856
buffer_store_dword v78, v27, s[48:51], s53 offen sc1 nt
v_cvt_pk_bf16_f32 v82, v192, v200
s_add_u32 s53, s60, 129024
buffer_store_dword v82, v27, s[48:51], s53 offen sc1 nt
v_cvt_pk_bf16_f32 v86, v193, v201
s_add_u32 s53, s60, 136192
buffer_store_dword v86, v27, s[48:51], s53 offen sc1 nt
s_endpgm
.Lkend:
.purgem MA
.purgem MV
.purgem MC
.purgem RT
.purgem RB
.purgem WL
)"
    :
    : "s"((unsigned long long)__builtin_amdgcn_kernarg_segment_ptr()), "s"(blockIdx.x), "v"(threadIdx.x)
    :
      "v1", "v2", "v3", "v4", "v5", "v6", "v7", "v8", "v9", "v10", "v11", "v12", "v13", "v14", "v15", "v16", "v17", "v18", "v19", "v20", "v21", "v22", "v23", "v24",
      "v25", "v26", "v27", "v28", "v29", "v30", "v31", "v32", "v33", "v34", "v35", "v36", "v37", "v38", "v39", "v40", "v41", "v42", "v43", "v44", "v45", "v46", "v47", "v48",
      "v49", "v50", "v51", "v52", "v53", "v54", "v55", "v56", "v57", "v58", "v59", "v60", "v61", "v62", "v63", "v64", "v65", "v66", "v67", "v68", "v69", "v70", "v71", "v72",
      "v73", "v74", "v75", "v76", "v77", "v78", "v79", "v80", "v81", "v82", "v83", "v84", "v85", "v86", "v87", "v88", "v89", "v90", "v91", "v92", "v93", "v94", "v95", "v96",
      "v97", "v98", "v99", "v100", "v101", "v102", "v103", "v104", "v105", "v106", "v107", "v108", "v109", "v110", "v111", "v112", "v113", "v114", "v115", "v116", "v117", "v118", "v119", "v120",
      "v121", "v122", "v123", "v124", "v125", "v126", "v127", "v128", "v129", "v130", "v131", "v132", "v133", "v134", "v135", "v136", "v137", "v138", "v139", "v140", "v141", "v142", "v143", "v144",
      "v145", "v146", "v147", "v148", "v149", "v150", "v151", "v152", "v153", "v154", "v155", "v156", "v157", "v158", "v159", "v160", "v161", "v162", "v163", "v164", "v165", "v166", "v167", "v168",
      "v169", "v170", "v171", "v172", "v173", "v174", "v175", "v176", "v177", "v178", "v179", "v180", "v181", "v182", "v183", "v184", "v185", "v186", "v187", "v188", "v189", "v190", "v191", "v192",
      "v193", "v194", "v195", "v196", "v197", "v198", "v199", "v200", "v201", "v202", "v203", "v204", "v205", "v206", "v207", "v208", "v209", "v210", "v211", "v212", "v213", "v214", "v215", "v216",
      "v217", "v218", "v219", "v220", "v221", "v222", "v223", "v224", "v225", "v226", "v227", "v228", "v229", "v230", "v231", "v232", "v233", "v234", "v235", "v236", "v237", "v238", "v239", "v240",
      "v241", "a0", "a1", "a2", "a3", "a4", "a5", "a6", "a7", "a8", "a9", "a10", "a11", "a12", "a13", "a14", "a15", "a16", "a17", "a18", "a19", "a20", "a21", "a22",
      "a23", "a24", "a25", "a26", "a27", "a28", "a29", "a30", "a31", "a32", "a33", "a34", "a35", "a36", "a37", "a38", "a39", "a40", "a41", "a42", "a43", "a44", "a45", "a46",
      "a47", "a48", "a49", "a50", "a51", "a52", "a53", "a54", "a55", "a56", "a57", "a58", "a59", "a60", "a61", "a62", "a63", "a64", "a65", "a66", "a67", "a68", "a69", "a70",
      "a71", "a72", "a73", "a74", "a75", "a76", "a77", "a78", "a79", "a80", "a81", "a82", "a83", "a84", "a85", "a86", "a87", "a88", "a89", "a90", "a91", "a92", "a93", "a94",
      "a95", "a96", "a97", "a98", "a99", "a100", "a101", "a102", "a103", "a104", "a105", "a106", "a107", "a108", "a109", "a110", "a111", "a112", "a113", "a114", "a115", "a116", "a117", "a118",
      "a119", "a120", "a121", "a122", "a123", "a124", "a125", "a126", "a127", "a128", "a129", "a130", "a131", "a132", "a133", "a134", "a135", "a136", "a137", "a138", "a139", "a140", "a141", "a142",
      "a143", "a144", "a145", "a146", "a147", "a148", "a149", "a150", "a151", "a152", "a153", "a154", "a155", "a156", "a157", "a158", "a159", "a160", "a161", "a162", "a163", "a164", "a165", "a166",
      "a167", "a168", "a169", "a170", "a171", "a172", "a173", "a174", "a175", "a176", "a177", "a178", "a179", "a180", "a181", "a182", "a183", "a184", "a185", "a186", "a187", "a188", "a189", "a190",
      "a191", "a192", "a193", "a194", "a195", "a196", "a197", "a198", "a199", "a200", "a201", "a202", "a203", "a204", "a205", "a206", "a207", "a208", "a209", "a210", "a211", "a212", "a213", "a214",
      "a215", "a216", "a217", "a218", "a219", "a220", "a221", "a222", "a223", "a224", "a225", "a226", "a227", "a228", "a229", "a230", "a231", "a232", "a233", "a234", "a235", "a236", "a237", "a238",
      "a239", "a240", "a241", "a242", "a243", "a244", "a245", "a246", "a247", "a248", "a249", "a250", "a251", "a252", "a253", "a254", "a255", "s4", "s5", "s6", "s7", "s8", "s9", "s10",
      "s11", "s12", "s13", "s14", "s15", "s16", "s17", "s18", "s19", "s20", "s21", "s22", "s23", "s24", "s25", "s26", "s27", "s28", "s29", "s30", "s31", "s34", "s35", "s36",
      "s37", "s38", "s39", "s40", "s41", "s42", "s43", "s44", "s45", "s46", "s47", "s48", "s49", "s50", "s51", "s52", "s53", "s54", "s55", "s56", "s57", "s58", "s59", "s60",
      "s61", "s62", "s63", "s64", "s65", "s66", "s67", "s68", "s69", "s70", "s71", "s72", "s73", "s74", "s75", "s76", "s77", "s78", "s79", "s80", "s81", "s82", "s83", "s84",
      "s85", "s86", "s87", "s88", "s89", "s90", "s91", "s92", "s93", "s94", "s95", "s96", "s97", "s98", "s99", "memory", "vcc", "scc"
  );
}

extern "C" void kernel_launch(void* const* d_in, const int* in_sizes, int n_in, void* d_out, int out_size, void* d_ws,
                              size_t ws_size, hipStream_t stream) {
  (void)hipFuncSetAttribute((const void*)k_all, hipFuncAttributeMaxDynamicSharedMemorySize, 163840);
  k_all<<<256, 256, 163840, stream>>>((const char*)d_in[0], (const char*)d_in[4], (const int*)d_in[1], (const unsigned short*)d_in[2],
                                      (const unsigned short*)d_in[3], (const char*)d_in[5], (char*)d_ws, (char*)d_out);
}
```
